```python
import torch

def _softmax(x, axis=-1):
    return torch.softmax(x, dim=axis)
def _gelu(x):
    return torch.nn.functional.gelu(x, approximate="tanh")
def _silu(x):
    return torch.nn.functional.silu(x)
def _sigmoid(x):
    return torch.sigmoid(x)
def _layernorm(x, axis=-1, eps=1e-5):
    m = x.mean(dim=axis, keepdim=True)
    v = x.var(dim=axis, keepdim=True, unbiased=False)
    return (x - m) / torch.sqrt(v + eps)
def _scatter_add(tgt, idx, src, axis=0):
    return tgt.clone().index_add_(axis, idx, src)
def _segment_sum(src, seg, n):
    out = torch.zeros((n,) + src.shape[1:], dtype=src.dtype)
    return out.index_add_(0, seg, src)
def _pad(x, pw):
    flat = tuple(v for pair in reversed(pw) for v in pair)
    return torch.nn.functional.pad(x, flat)

kernel_name = 'parallel_sum__softmax_full_mha__geglu_ffn__prompt'

def setup_inputs():

    _w = (lambda a: (lambda t: t.to(torch.bfloat16) if t.is_floating_point() else t)(torch.as_tensor(a)))
    def _A_setup():
        B, S, D = 1, 2048, 640
        return {
            "x": _w((lambda *s: torch.randn(*s, dtype=torch.bfloat16) * 0.05)(B, S, D)),
            "wqkv": _w((lambda *s: torch.randn(*s, dtype=torch.bfloat16) * 0.2)(D, 3 * D)),
            "wo": _w((lambda *s: torch.randn(*s, dtype=torch.bfloat16) * 0.1)(D, D)),
        }
    _A_in = _A_setup()

    def _B_setup():
        B, S, D, F = 1, 2048, 640, 1728
        return {
            "x": _w((lambda *s: torch.randn(*s, dtype=torch.bfloat16) * 0.05)(B, S, D)),
            "w_gate": _w((lambda *s: torch.randn(*s, dtype=torch.bfloat16) * 0.1)(D, F)),
            "w_up": _w((lambda *s: torch.randn(*s, dtype=torch.bfloat16) * 0.1)(D, F)),
            "w_down": _w((lambda *s: torch.randn(*s, dtype=torch.bfloat16) * 0.1)(F, D)),
        }
    _B_in = _B_setup()

    return {
        "A_x": _A_in["x"],
        "A_wqkv": _A_in["wqkv"],
        "A_wo": _A_in["wo"],
        "B_x": _B_in["x"],
        "B_w_gate": _B_in["w_gate"],
        "B_w_up": _B_in["w_up"],
        "B_w_down": _B_in["w_down"],
    }

def reference(A_x, A_wqkv, A_wo, B_x, B_w_gate, B_w_up, B_w_down):
    def _A_ref(x, wqkv, wo):
        B, S, D = x.shape
        H = 32; Dh = D // H
        qkv = (lambda x, *d: x.permute(*d))((x @ wqkv).reshape(B, S, 3, H, Dh), 2, 0, 3, 1, 4)
        q, k, v = qkv[0], qkv[1], qkv[2]
        scale = Dh ** -0.5
        scores = torch.einsum("bhqd,bhkd->bhqk", q, k) * scale
        p = _softmax(scores, axis=-1)
        o = torch.einsum("bhqk,bhkd->bhqd", p, v)
        return (lambda x, *d: x.permute(*d))(o, 0, 2, 1, 3).reshape(B, S, D) @ wo

    def _B_ref(x, w_gate, w_up, w_down):
        return (_gelu(x @ w_gate) * (x @ w_up)) @ w_down

    x = A_x
    xn = _layernorm(x, axis=-1)
    return x + _A_ref(xn, A_wqkv, A_wo) + _B_ref(xn, B_w_gate, B_w_up, B_w_down)
```

```cpp
#pragma clang diagnostic ignored "-Wint-to-pointer-cast"
#include <hip/hip_runtime.h>
#include <hip/hip_bf16.h>
#include <stdint.h>

typedef __attribute__((ext_vector_type(8))) __bf16 bf16x8;
typedef __attribute__((ext_vector_type(16))) __bf16 bf16x16;
typedef __attribute__((ext_vector_type(2))) __bf16 bf16x2;
typedef __attribute__((ext_vector_type(4))) short s16x4;
typedef __attribute__((ext_vector_type(16))) float f32x16;
typedef __attribute__((ext_vector_type(8))) float f32x8;
typedef __attribute__((ext_vector_type(4))) float f32x4;
typedef __attribute__((ext_vector_type(2))) float f32x2;
typedef __attribute__((ext_vector_type(4))) unsigned u32x4;
typedef __attribute__((ext_vector_type(2))) unsigned u32x2;
#define LAS __attribute__((address_space(3)))
#define GAS __attribute__((address_space(1)))
#define DEVI __device__ __forceinline__

constexpr int S_ = 2048, D_ = 640, F_ = 1728;
constexpr int KS2 = 152;
constexpr size_t WS_Q = 0;
constexpr size_t WS_KV = WS_Q + 32 * 16 * 5120;
constexpr size_t WS_A2 = WS_KV + 32 * 16 * 10240;
constexpr size_t WS_W2 = WS_A2 + 64 * KS2 * 1024;
constexpr size_t WS_P1 = WS_W2 + 20 * KS2 * 1024;
constexpr size_t WS_CNT = WS_P1 + 2048 * 640 * 4;

DEVI unsigned pk2(float a, float b) {
  f32x2 v = {a, b};
  return __builtin_bit_cast(unsigned, __builtin_convertvector(v, bf16x2));
}
DEVI void glds16(const void* sbase, unsigned voff, unsigned ldsaddr) {
  asm volatile("s_mov_b32 m0, %2\n\ts_nop 0\n\tglobal_load_lds_dwordx4 %0, %1" ::"v"(voff), "s"(sbase), "s"(ldsaddr) : "memory");
}
DEVI bf16x8 trfrag(unsigned a0, unsigned a1) {
  s16x4 lo = __builtin_amdgcn_ds_read_tr16_b64_v4i16((LAS s16x4*)a0);
  s16x4 hi = __builtin_amdgcn_ds_read_tr16_b64_v4i16((LAS s16x4*)a1);
  return __builtin_bit_cast(bf16x8, __builtin_shufflevector(lo, hi, 0, 1, 2, 3, 4, 5, 6, 7));
}
DEVI float sfma(float a, float b, float c) { float r; asm("v_fma_f32 %0, %1, %2, %3" : "=v"(r) : "v"(a), "v"(b), "v"(c)); return r; }
#define MFMA(a, b, c) __builtin_amdgcn_mfma_f32_32x32x16_bf16(a, b, c, 0, 0, 0)
#define WAITBAR(n) asm volatile("s_waitcnt vmcnt(" #n ") lgkmcnt(0)\n\ts_barrier" ::: "memory")

constexpr int K1_RING = 0, K1_XCH = 81920, K1_STAT = 147456, K1_LDS = 149504;

struct QuadSrc { const char* p0; const char* p1; unsigned rowb; unsigned voff; };

template <bool FIRST, bool SWAP>
DEVI void k1_quad(u32x4 (&xa)[20], float rs, float nm, f32x16 (&acc)[4], bf16x8 (&F0)[4], unsigned aK, unsigned aG,
                  const QuadSrc& cur, const QuadSrc& nxt, unsigned ldsw) {
#pragma unroll
  for (int s = 0; s < 10; s++) {
    WAITBAR(4);
    {
      const int sn = s + 4;
      const QuadSrc& q = sn < 10 ? cur : nxt;
      const int st = sn < 10 ? sn : sn - 10;
      const unsigned dst = ldsw + (sn % 5) * 16384;
      glds16(q.p0 + (size_t)st * 32 * q.rowb, q.voff, dst);
      glds16(q.p1 + (size_t)st * 32 * q.rowb, q.voff, dst + 4096);
    }
    bf16x8 F1[4];
    {
      const unsigned o = (s % 5) * 16384 + 2048;
      F1[0] = trfrag(aK + o, aK + o + 512);
      F1[1] = trfrag(aK + o + 4096, aK + o + 4096 + 512);
      F1[2] = trfrag(aG + o, aG + o + 512);
      F1[3] = trfrag(aG + o + 4096, aG + o + 4096 + 512);
    }
#pragma unroll
    for (int u = 0; u < 2; u++) {
      const int t = 2 * s + u;
      if (FIRST) {
#pragma unroll
        for (int i = 0; i < 4; i++) {
          unsigned v = xa[t][i];
          float lo = __builtin_bit_cast(float, v << 16), hi = __builtin_bit_cast(float, v & 0xffff0000u);
          xa[t][i] = pk2(sfma(lo, rs, nm), sfma(hi, rs, nm));
        }
      }
      bf16x8 a = __builtin_bit_cast(bf16x8, xa[t]);
#pragma unroll
      for (int j = 0; j < 4; j++) {
        bf16x8 b = u == 0 ? F0[j] : F1[j];
        if (t == 0) { f32x16 z = {}; acc[j] = SWAP ? MFMA(b, a, z) : MFMA(a, b, z); }
        else acc[j] = SWAP ? MFMA(b, a, acc[j]) : MFMA(a, b, acc[j]);
      }
      if (u == 0) {
        const unsigned o = ((s + 1) % 5) * 16384;
        F0[0] = trfrag(aK + o, aK + o + 512);
        F0[1] = trfrag(aK + o + 4096, aK + o + 4096 + 512);
        F0[2] = trfrag(aG + o, aG + o + 512);
        F0[3] = trfrag(aG + o + 4096, aG + o + 4096 + 512);
      }
    }
  }
}

DEVI void k1_xchg(f32x16 (&acc)[4], int w, int lane) {
  const unsigned wa = K1_XCH + w * 8192 + lane * 16, ra = K1_XCH + (w ^ 4) * 8192 + lane * 16;
#pragma unroll
  for (int j = 0; j < 2; j++)
#pragma unroll
    for (int g = 0; g < 4; g++) {
      f32x4 v = {acc[2 + j][4 * g], acc[2 + j][4 * g + 1], acc[2 + j][4 * g + 2], acc[2 + j][4 * g + 3]};
      *(LAS f32x4*)(wa + j * 4096 + g * 1024) = v;
    }
  asm volatile("s_waitcnt lgkmcnt(0)\n\ts_barrier" ::: "memory");
#pragma unroll
  for (int j = 0; j < 2; j++)
#pragma unroll
    for (int g = 0; g < 4; g++) {
      f32x4 v = *(LAS f32x4*)(ra + j * 4096 + g * 1024);
#pragma unroll
      for (int i = 0; i < 4; i++) acc[j][4 * g + i] += v[i];
    }
}

DEVI void k1_epi_ffn(f32x16 (&acc)[4], char* ws, int f, int rb, int m, int kh, int lane) {
  const float c0 = -2.0f * 1.4426950408889634f * 0.7978845608028654f, c1 = c0 * 0.044715f;
  char* dst = ws + WS_A2 + ((size_t)(rb * 4 + m) * KS2 + 4 * f + 2 * kh) * 1024 + lane * 16;
#pragma unroll
  for (int s = 0; s < 2; s++) {
    float hv[8];
#pragma unroll
    for (int i = 0; i < 8; i++) {
      float g = acc[0][8 * s + i], u = acc[1][8 * s + i];
      float z = g * __builtin_fmaf(g * g, c1, c0);
      float r = __builtin_amdgcn_rcpf(1.0f + __builtin_amdgcn_exp2f(z));
      hv[i] = (g * r) * u;
    }
    u32x4 o = {pk2(hv[0], hv[1]), pk2(hv[2], hv[3]), pk2(hv[4], hv[5]), pk2(hv[6], hv[7])};
    *(u32x4*)(dst + s * 1024) = o;
  }
}
DEVI void k1_epi_qk(f32x16 (&acc)[4], char* ws, int q, int rb, int m, int kh, int lane) {
  const bool isK = q >= 5;
  const float sc = isK ? 1.0f : 0.22360679774997896f * 1.4426950408889634f;
  char* base = ws + (isK ? WS_KV : WS_Q);
  const unsigned stride = isK ? 10240 : 5120;
  const int h = lane >> 5, tok = lane & 31;
  const unsigned offm = m * 1024 + tok * 16, offr = 4096 + (m >> 1) * 512 + tok * 16 + (m & 1) * 8;
#pragma unroll
  for (int j = 0; j < 2; j++)
#pragma unroll
    for (int g = 0; g < 4; g++) {
      const unsigned c4 = (q - (isK ? 5 : 0)) * 32 + 16 * j + 8 * kh + 2 * g + h;
      const unsigned head = (c4 * 205) >> 10, dg = c4 - head * 5;
      const unsigned off = (head * 16 + rb) * stride + (dg < 4 ? offm + (dg >> 1) * 512 + (dg & 1) * 8 : offr);
      u32x2 o = {pk2(acc[j][4 * g] * sc, acc[j][4 * g + 1] * sc), pk2(acc[j][4 * g + 2] * sc, acc[j][4 * g + 3] * sc)};
      *(u32x2*)(base + off) = o;
    }
}
DEVI void k1_epi_v(f32x16 (&acc)[4], char* ws, int q, int rb, int m, int kh, int lane) {
  const int h = lane >> 5;
#pragma unroll
  for (int j = 0; j < 2; j++) {
    const unsigned col = (q - 10) * 128 + 64 * j + 32 * kh + (lane & 31);
    const unsigned head = (col * 3277) >> 16, dim = col - head * 20;
    char* dst = ws + WS_KV + (head * 16 + rb) * 10240 + 5120 + 2 * m * 640 + dim * 16 + h * 8;
#pragma unroll
    for (int g = 0; g < 4; g++) {
      u32x2 o = {pk2(acc[j][4 * g], acc[j][4 * g + 1]), pk2(acc[j][4 * g + 2], acc[j][4 * g + 3])};
      *(u32x2*)(dst + (g >> 1) * 640 + (g & 1) * 320) = o;
    }
  }
}

DEVI void k1_relayout(const char* wd, const char* wo, char* ws, int id, int w, int lane) {
  for (int i = 0; i < 5; i++) {
    const int ks = id + 32 * i;
    if (ks >= KS2 || (ks >= 108 && ks < 112)) continue;
    for (int p = w; p < 20; p += 8) {
      const unsigned o = p * 1024 + lane * 16, sg = o / 1280, col = o - sg * 1280;
      unsigned row; const char* src;
      if (ks < 108) { src = wd; row = ks * 16 + sg; }
      else {
        src = wo; const int g = (ks - 112) / 5, e = (ks - 112) - g * 5;
        row = e < 4 ? 20 * (4 * g + e) + sg : 20 * (4 * g + 2 * ((sg >> 2) & 1) + (sg >> 3)) + 16 + (sg & 3);
      }
      glds16(src, row * 1280 + col, i * 20480 + p * 1024);
    }
  }
  WAITBAR(0);
  const unsigned la = (4 * (lane >> 5) + ((lane >> 2) & 3)) * 1280 + ((lane >> 4) & 1) * 32 + (lane & 3) * 8;
  for (int i = 0; i < 5; i++) {
    const int ks = id + 32 * i;
    if (ks >= KS2) continue;
    for (int nt = w; nt < 20; nt += 8) {
      const unsigned a = la + i * 20480 + nt * 64;
      u32x4 v = __builtin_bit_cast(u32x4, trfrag(a, a + 8 * 1280));
      if (ks >= 108 && ks < 112) v = u32x4{0, 0, 0, 0};
      *(u32x4*)(ws + WS_W2 + ((size_t)nt * KS2 + ks) * 1024 + lane * 16) = v;
    }
  }
}

__global__ __launch_bounds__(512, 2) void k1(const char* __restrict__ x, const char* __restrict__ wqkv, const char* __restrict__ wo,
                                             const char* __restrict__ wg, const char* __restrict__ wu, const char* __restrict__ wd,
                                             char* __restrict__ ws) {
  extern __shared__ __attribute__((aligned(256))) char smem[];
  const int tid = threadIdx.x, lane = tid & 63, w = __builtin_amdgcn_readfirstlane(tid >> 6);
  const int xcd = blockIdx.x & 7, jb = blockIdx.x >> 3;
  if (jb >= 28) {
    if (blockIdx.x == 255 && tid < 64) ((unsigned*)(ws + WS_CNT))[tid * 16] = 0;
    k1_relayout(wd, wo, ws, xcd * 4 + jb - 28, w, lane);
    return;
  }
  const int rb = 4 * (xcd >> 1) + (jb & 3), cb = 7 * (xcd & 1) + (jb >> 2);
  const int m = w & 3, kh = w >> 2, h = lane >> 5;
  u32x4 xa[20];
  {
    const char* xp = x + (size_t)(rb * 128 + m * 32 + (lane & 31)) * 1280 + kh * 640 + h * 16;
#pragma unroll
    for (int t = 0; t < 20; t++) xa[t] = *(const u32x4*)(xp + t * 32);
  }
  const unsigned krow = 320 * kh + 8 * m + (lane >> 3), ch = ((lane & 7) ^ (((lane >> 4) & 1) << 2)) * 16;
  const unsigned voffF = krow * (F_ * 2) + ch, voffQ = krow * 3840 + ch;
  const int f0 = cb, f1 = 14 + cb;
  const int q2 = cb + 1;
  QuadSrc s0 = {wg + f0 * 128, wu + f0 * 128, F_ * 2, voffF};
  QuadSrc s1 = cb < 13 ? QuadSrc{wg + f1 * 128, wu + f1 * 128, F_ * 2, voffF} : QuadSrc{wqkv, wqkv + 128, 3840, voffQ};
  QuadSrc s2 = {wqkv + q2 * 256, wqkv + q2 * 256 + 128, 3840, voffQ};
  const unsigned ldsw = kh * 8192 + m * 1024;
#pragma unroll
  for (int st = 0; st < 4; st++) {
    glds16(s0.p0 + (size_t)st * 32 * s0.rowb, s0.voff, ldsw + st * 16384);
    glds16(s0.p1 + (size_t)st * 32 * s0.rowb, s0.voff, ldsw + st * 16384 + 4096);
  }
  float rs, nm;
  {
    float a0 = 0, a1 = 0, b0 = 0, b1 = 0;
    const bf16x2 one = __builtin_bit_cast(bf16x2, 0x3f803f80u);
#pragma unroll
    for (int t = 0; t < 20; t++)
#pragma unroll
      for (int i = 0; i < 4; i += 2) {
        const unsigned up = xa[t][i], uq = xa[t][i + 1];
        bf16x2 p = __builtin_bit_cast(bf16x2, up), q = __builtin_bit_cast(bf16x2, uq);
        a0 = __builtin_amdgcn_fdot2_f32_bf16(p, one, a0, false);
        a1 = __builtin_amdgcn_fdot2_f32_bf16(q, one, a1, false);
        b0 = __builtin_amdgcn_fdot2_f32_bf16(p, p, b0, false);
        b1 = __builtin_amdgcn_fdot2_f32_bf16(q, q, b1, false);
      }
    a0 += a1; b0 += b1;
    a0 += __shfl_xor(a0, 32); b0 += __shfl_xor(b0, 32);
    *(LAS f32x2*)(K1_STAT + w * 256 + (lane & 31) * 8) = f32x2{a0, b0};
    WAITBAR(6);
    f32x2 o = *(LAS f32x2*)(K1_STAT + (w ^ 4) * 256 + (lane & 31) * 8);
    const float mu = (a0 + o[0]) * (1.0f / 640), var = fmaxf((b0 + o[1]) * (1.0f / 640) - mu * mu, 0.0f);
    rs = __builtin_amdgcn_rsqf(var + 1e-5f);
    nm = -mu * rs;
  }
  const unsigned q_ = (lane >> 2) & 3;
  const unsigned abase = kh * 8192 + (8 * h + q_) * 128 + ((lane >> 4) & 1) * 32 + (lane & 3) * 8;
  const unsigned aK = abase + ((kh ^ (q_ >> 1)) << 6), aG = aK ^ 64;
  bf16x8 F0[4];
  F0[0] = trfrag(aK, aK + 512); F0[1] = trfrag(aK + 4096, aK + 4096 + 512);
  F0[2] = trfrag(aG, aG + 512); F0[3] = trfrag(aG + 4096, aG + 4096 + 512);
  f32x16 acc[4];
  k1_quad<true, true>(xa, rs, nm, acc, F0, aK, aG, s0, s1, ldsw);
  k1_xchg(acc, w, lane);
  k1_epi_ffn(acc, ws, f0, rb, m, kh, lane);
  k1_quad<false, true>(xa, rs, nm, acc, F0, aK, aG, s1, s2, ldsw);
  k1_xchg(acc, w, lane);
  if (cb < 13) k1_epi_ffn(acc, ws, f1, rb, m, kh, lane);
  else k1_epi_qk(acc, ws, 0, rb, m, kh, lane);
  if (q2 < 10) {
    k1_quad<false, true>(xa, rs, nm, acc, F0, aK, aG, s2, s2, ldsw);
    k1_xchg(acc, w, lane);
    k1_epi_qk(acc, ws, q2, rb, m, kh, lane);
  } else {
    k1_quad<false, false>(xa, rs, nm, acc, F0, aK, aG, s2, s2, ldsw);
    k1_xchg(acc, w, lane);
    k1_epi_v(acc, ws, q2, rb, m, kh, lane);
  }
  asm volatile("s_waitcnt vmcnt(0)" ::: "memory");
}
constexpr int K2_ONES = 122880, K2_LDS = 129024;
typedef __attribute__((ext_vector_type(4))) __bf16 bf16x4;
#define LO8(S) __builtin_bit_cast(bf16x16, __builtin_shufflevector(S, S, 0, 1, 2, 3, 4, 5, 6, 7))
#define HI8(S) __builtin_bit_cast(bf16x16, __builtin_shufflevector(S, S, 8, 9, 10, 11, 12, 13, 14, 15))
#define SMF(a, b, c) __builtin_amdgcn_smfmac_f32_32x32x32_bf16(a, b, c, sidx, 0, 0)
#define MFMA8(a, b, c) __builtin_amdgcn_mfma_f32_32x32x8bf16_1k(a, b, c, 0, 0, 0)
#define SB() __builtin_amdgcn_sched_barrier(0)
#define EXP4(S, b) { for (int i_ = b; i_ < b + 4; i_++) S[i_] = __builtin_amdgcn_exp2f(S[i_]); SB(); }
DEVI s16x4 lo4(u32x4 v) { return __builtin_bit_cast(s16x4, u32x2{v[0], v[1]}); }
DEVI s16x4 hi4(u32x4 v) { return __builtin_bit_cast(s16x4, u32x2{v[2], v[3]}); }
DEVI bf16x8 ldsf(unsigned a) { return *(LAS bf16x8*)a; }
DEVI bf16x8 gldf(const char* p) { return *(const bf16x8*)p; }

struct G2 { bf16x8 aa, ab, b0a, b1a, b4a, b0b, b1b; };

template <bool FINAL>
DEVI void g2_reduce(f32x16 (&ga)[3], int w, int lane, int tid, int mt, int ng, const char* x, char* ws, char* out) {
  const unsigned wa = w * 13824 + (lane & 31) * 144 + (lane >> 5) * 16;
#pragma unroll
  for (int j = 0; j < 3; j++)
#pragma unroll
    for (int g = 0; g < 4; g++)
      *(LAS f32x4*)(wa + j * 4608 + g * 32) = f32x4{ga[j][4 * g], ga[j][4 * g + 1], ga[j][4 * g + 2], ga[j][4 * g + 3]};
  asm volatile("s_waitcnt lgkmcnt(0)\n\ts_barrier" ::: "memory");
  float* p1 = (float*)(ws + WS_P1);
  {
    const int tok = tid >> 4, n8 = tid & 15;
    const unsigned ra = (n8 >> 3) * 4 * 13824 + ((n8 >> 2) & 1) * 4608 + tok * 144 + (n8 & 3) * 32;
    f32x4 u = *(LAS f32x4*)ra, v = *(LAS f32x4*)(ra + 16);
#pragma unroll
    for (int k = 1; k < 4; k++) { u += *(LAS f32x4*)(ra + k * 13824); v += *(LAS f32x4*)(ra + k * 13824 + 16); }
    const size_t e = (size_t)(mt * 32 + tok) * 640 + ng * 160 + n8 * 8;
    if (FINAL) {
      u += *(const f32x4*)(p1 + e); v += *(const f32x4*)(p1 + e + 4);
      u32x4 xv = *(const u32x4*)(x + e * 2);
      u32x4 o;
#pragma unroll
      for (int i = 0; i < 2; i++) {
        o[i] = pk2(u[2 * i] + __builtin_bit_cast(float, xv[i] << 16), u[2 * i + 1] + __builtin_bit_cast(float, xv[i] & 0xffff0000u));
        o[2 + i] = pk2(v[2 * i] + __builtin_bit_cast(float, xv[2 + i] << 16), v[2 * i + 1] + __builtin_bit_cast(float, xv[2 + i] & 0xffff0000u));
      }
      *(u32x4*)(out + e * 2) = o;
    } else { *(f32x4*)(p1 + e) = u; *(f32x4*)(p1 + e + 4) = v; }
  }
  if (tid < 256) {
    const int tok = tid >> 3, n4 = tid & 7;
    const unsigned ra = 2 * 4608 + tok * 144 + n4 * 16;
    f32x4 u = *(LAS f32x4*)ra;
#pragma unroll
    for (int k = 1; k < 8; k++) u += *(LAS f32x4*)(ra + k * 13824);
    const size_t e = (size_t)(mt * 32 + tok) * 640 + ng * 160 + 128 + n4 * 4;
    if (FINAL) {
      u += *(const f32x4*)(p1 + e);
      u32x2 xv = *(const u32x2*)(x + e * 2);
      u32x2 o;
#pragma unroll
      for (int i = 0; i < 2; i++)
        o[i] = pk2(u[2 * i] + __builtin_bit_cast(float, xv[i] << 16), u[2 * i + 1] + __builtin_bit_cast(float, xv[i] & 0xffff0000u));
      *(u32x2*)(out + e * 2) = o;
    } else *(f32x4*)(p1 + e) = u;
  }
}

template <int MODE>
DEVI void k2_chunk(f32x16& sa, f32x16& sb, f32x16& o, const f32x16& negm, bf16x8 qm, s16x4 qa, s16x4 qb, bf16x8& kn, u32x4& kr,
                   bf16x8& v0, bf16x8& v1, unsigned ka, unsigned kan, unsigned va, int sidx, f32x16 (&ga)[3], G2& g,
                   const char* pa, const char* pb, const char* pb0, const char* pb4, int dab) {
#define STEP(CUR, PRV, T, KR, QR, GEMM)                                   \
  {                                                                       \
    bf16x8 nv0 = ldsf(va + (2 * T) * 640), nv1 = ldsf(va + (2 * T + 1) * 640); \
    bf16x8 nk = T < 2 ? ldsf(ka + (T + 2) * 1024) : ldsf(kan + (T - 2) * 1024); \
    o = SMF(v0, LO8(PRV), o); SB(); EXP4(CUR, 0)                          \
    o = SMF(v1, HI8(PRV), o); SB(); EXP4(CUR, 4)                          \
    PRV = MFMA(kn, qm, negm); SB(); EXP4(CUR, 8)                          \
    PRV = MFMA8(KR, QR, PRV); SB();                                       \
    CUR[12] = __builtin_amdgcn_exp2f(CUR[12]); CUR[13] = __builtin_amdgcn_exp2f(CUR[13]); SB(); \
    GEMM; SB();                                                           \
    CUR[14] = __builtin_amdgcn_exp2f(CUR[14]); CUR[15] = __builtin_amdgcn_exp2f(CUR[15]); SB(); \
    v0 = nv0; v1 = nv1; kn = nk;                                          \
  }
  STEP(sa, sb, 0, hi4(kr), qa, { if (MODE == 2) ga[0] = MFMA(g.b0a, g.aa, ga[0]); if (MODE) g.b0a = gldf(pb0); })
  STEP(sb, sa, 1, lo4(kr), qb, { if (MODE == 2) { ga[1] = MFMA(g.b1a, g.aa, ga[1]); ga[2] = MFMA(g.b4a, g.aa, ga[2]); }
                                 if (MODE) { g.b1a = gldf(pb0 + KS2 * 1024); g.b4a = gldf(pb4); g.aa = gldf(pa); } })
  u32x4 krn = *(LAS u32x4*)(kan + 4096);
  STEP(sa, sb, 2, hi4(kr), qb, { if (MODE == 2) ga[0] = MFMA(g.b0b, g.ab, ga[0]); if (MODE) g.b0b = gldf(pb0 + dab); })
  kr = krn;
  STEP(sb, sa, 3, lo4(kr), qa, { if (MODE == 2) ga[1] = MFMA(g.b1b, g.ab, ga[1]);
                                 if (MODE) { g.b1b = gldf(pb0 + KS2 * 1024 + dab); g.ab = gldf(pb); } })
#undef STEP
}

DEVI float max16(const f32x16& s, float m) {
#pragma unroll
  for (int i = 0; i < 16; i += 2) m = fmaxf(fmaxf(s[i], s[i + 1]), m);
  return m;
}

__global__ __launch_bounds__(512, 2) void k2(char* __restrict__ ws) {
  extern __shared__ __attribute__((aligned(256))) char smem[];
  const int tid = threadIdx.x, lane = tid & 63, w = __builtin_amdgcn_readfirstlane(tid >> 6);
  const int xcd = blockIdx.x & 7, jb = blockIdx.x >> 3;
  const int head = 4 * xcd + (jb & 3), qblk = jb >> 2, mt = 8 * xcd + (jb >> 2), ng = jb & 3;
  const int h = lane >> 5, row = lane & 31;
  const char* kv = ws + WS_KV + (size_t)head * 163840;
  const unsigned l16 = lane * 16;
  glds16(kv + w * 1024, l16, w * 1024);
  glds16(kv + w * 1024 + 8192, l16, w * 1024 + 8192);
  const int qt = qblk * 8 + w;
  const char* qp = ws + WS_Q + (size_t)(head * 16 + (qt >> 2)) * 5120;
  const bf16x8 qm = gldf(qp + (qt & 3) * 1024 + l16);
  const u32x2 qr = *(const u32x2*)(qp + 4096 + ((qt & 3) >> 1) * 512 + row * 16 + (qt & 1) * 8);
  if (tid < 384) *(LAS u32x4*)(K2_ONES + tid * 16) = u32x4{0x3f803f80u, 0x3f803f80u, 0x3f803f80u, 0x3f803f80u};
  const s16x4 qa = __builtin_bit_cast(s16x4, h == 0 ? qr : u32x2{0, 0}), qb = __builtin_bit_cast(s16x4, h == 1 ? qr : u32x2{0, 0});
  asm volatile("" ::"v"(qm), "v"(qa), "v"(qb));
#pragma unroll
  for (int i = 2; i < 15; i++) glds16(kv + w * 1024 + i * 8192, l16, w * 1024 + i * 8192);
  WAITBAR(13);
  int sidx = 0xdddd;
  asm volatile("" : "+v"(sidx));
  f32x16 sa, sb, o = {}, negm;
  bf16x8 kn, v0 = {}, v1 = {};
  u32x4 kr = *(LAS u32x4*)(l16 + 4096);
  {
    const f32x16 z = {};
    bf16x8 k0 = ldsf(l16);
    kn = ldsf(l16 + 1024);
    sa = MFMA8(lo4(kr), qa, MFMA(k0, qm, z));
    sb = MFMA8(hi4(kr), qa, MFMA(kn, qm, z));
    float mx = max16(sb, max16(sa, -3.0e38f));
    sa = MFMA8(lo4(kr), qb, MFMA(ldsf(l16 + 2048), qm, z));
    sb = MFMA8(hi4(kr), qb, MFMA(ldsf(l16 + 3072), qm, z));
    mx = max16(sb, max16(sa, mx));
    mx = fmaxf(mx, __shfl_xor(mx, 32));
    const float nm = -(mx + 64.0f);
#pragma unroll
    for (int i = 0; i < 16; i++) negm[i] = nm;
    asm volatile("" : "+v"(negm));
    sa = MFMA8(lo4(kr), qa, MFMA(k0, qm, negm));
    sb = z;
  }
  const unsigned vstep = row < 20 ? 10240 : 0;
  const unsigned va0 = (row < 20 ? 5120 : K2_ONES) + h * 320 + row * 16;
  f32x16 ga[3] = {};
  G2 g;
  const int kq = w & 3, wh = w >> 2;
  const char* a2 = ws + WS_A2 + (size_t)mt * KS2 * 1024 + l16;
  const char* pb0 = ws + WS_W2 + ((size_t)(ng * 5 + 2 * wh) * KS2 + kq * 28 + wh) * 1024 + l16;
  const char* pb4 = ws + WS_W2 + ((size_t)(ng * 5 + 4) * KS2 + kq * 28 + wh) * 1024 + l16;
  const int dab = wh ? -1024 : 1024;
  k2_chunk<0>(sa, sb, o, negm, qm, qa, qb, kn, kr, v0, v1, l16, l16 + 10240, va0, sidx, ga, g, a2, a2, pb0, pb4, dab);
  WAITBAR(9);
  k2_chunk<1>(sa, sb, o, negm, qm, qa, qb, kn, kr, v0, v1, l16 + 10240, l16 + 20480, va0 + vstep, sidx, ga, g,
              a2 + (kq * 28 + wh) * 1024, a2 + (kq * 28 + 1 - wh) * 1024, pb0, pb4, dab);
#pragma unroll 1
  for (int c = 2; c < 16; c++) {
    if (c == 4 || c == 11) WAITBAR(7);
    if (c >= 4 && c < 9) glds16(kv + w * 1024 + (c + 11) * 8192, l16, w * 1024 + (c - 4) * 8192);
    const int sl = c < 12 ? c : c - 12, sn = c < 11 ? c + 1 : c - 11;
    const int i2 = c < 15 ? 2 * (c - 1) : 26;
    const int ka_ = min(kq * 28 + i2 + wh, 107), kb_ = min(kq * 28 + i2 + 1 - wh, 107);
    k2_chunk<2>(sa, sb, o, negm, qm, qa, qb, kn, kr, v0, v1, l16 + sl * 10240, l16 + sn * 10240, va0 + sl * vstep, sidx, ga, g,
                a2 + ka_ * 1024, a2 + kb_ * 1024, pb0 + i2 * 1024, pb4 + i2 * 1024, dab);
  }
  o = SMF(v0, LO8(sb), o);
  o = SMF(v1, HI8(sb), o);
  float l = h ? o[8] : o[12];
  {
    float chk = 0.0f;
#pragma unroll
    for (int i = 0; i < 12; i++) chk = __builtin_fmaf(o[i], 0.0f, chk);
    const bool bad = !(l < 0x1p120f) || !(chk == 0.0f);
    if (__builtin_amdgcn_ballot_w64(bad) != 0) {
      const f32x16 z = {};
      float mx = -3.0e38f;
#pragma unroll 1
      for (int t = 0; t < 64; t++) {
        const char* cp = kv + (t >> 2) * 10240;
        const u32x2 r = *(const u32x2*)(cp + 4096 + ((t & 3) >> 1) * 512 + row * 16 + (t & 1) * 8);
        sa = MFMA8(__builtin_bit_cast(s16x4, r), qa, MFMA(gldf(cp + (t & 3) * 1024 + l16), qm, z));
        mx = max16(sa, mx);
      }
      mx = fmaxf(mx, __shfl_xor(mx, 32));
#pragma unroll
      for (int i = 0; i < 16; i++) negm[i] = -mx;
      o = z;
      const bf16x8 ones = __builtin_bit_cast(bf16x8, u32x4{0x3f803f80u, 0x3f803f80u, 0x3f803f80u, 0x3f803f80u});
#pragma unroll 1
      for (int t = 0; t < 64; t++) {
        const char* cp = kv + (t >> 2) * 10240;
        const u32x2 r = *(const u32x2*)(cp + 4096 + ((t & 3) >> 1) * 512 + row * 16 + (t & 1) * 8);
        sa = MFMA8(__builtin_bit_cast(s16x4, r), qa, MFMA(gldf(cp + (t & 3) * 1024 + l16), qm, negm));
#pragma unroll
        for (int i = 0; i < 16; i++) sa[i] = __builtin_amdgcn_exp2f(sa[i]);
        const char* vp = cp + 5120 + (t & 3) * 1280 + h * 320 + min(row, 19) * 16;
        bf16x8 a0 = gldf(vp), a1 = gldf(vp + 640);
        if (row >= 20) { a0 = ones; a1 = ones; }
        o = SMF(a0, LO8(sa), o);
        o = SMF(a1, HI8(sa), o);
      }
      l = h ? o[8] : o[12];
    }
  }
  {
    const float ri = __builtin_amdgcn_rcpf(l);
    char* op = ws + WS_A2 + ((size_t)qt * KS2 + 112 + 5 * (head >> 2)) * 1024;
    *(u32x4*)(op + (head & 3) * 1024 + l16) =
        u32x4{pk2(o[0] * ri, o[1] * ri), pk2(o[2] * ri, o[3] * ri), pk2(o[4] * ri, o[5] * ri), pk2(o[6] * ri, o[7] * ri)};
    if (h == 0) *(u32x2*)(op + 4096 + ((head & 3) >> 1) * 512 + row * 16 + (head & 1) * 8) =
        u32x2{pk2(o[8] * ri, o[9] * ri), pk2(o[10] * ri, o[11] * ri)};
  }
  asm volatile("s_waitcnt lgkmcnt(0)\n\ts_barrier" ::: "memory");
  g2_reduce<false>(ga, w, lane, tid, mt, ng, nullptr, ws, nullptr);
}

__global__ __launch_bounds__(512, 2) void k3(const char* __restrict__ x, char* __restrict__ ws, char* __restrict__ out) {
  extern __shared__ __attribute__((aligned(256))) char smem[];
  const int tid = threadIdx.x, lane = tid & 63, w = __builtin_amdgcn_readfirstlane(tid >> 6);
  const int xcd = blockIdx.x & 7, jb = blockIdx.x >> 3;
  const int mt = 8 * xcd + (jb >> 2), ng = jb & 3, kq = w & 3, wh = w >> 2;
  const unsigned l16 = lane * 16;
  const int k0 = 112 + kq * 10 + wh, dab = wh ? -1024 : 1024;
  const char* pa = ws + WS_A2 + ((size_t)mt * KS2 + k0) * 1024 + l16;
  const char* pb0 = ws + WS_W2 + ((size_t)(ng * 5 + 2 * wh) * KS2 + k0) * 1024 + l16;
  const char* pb4 = ws + WS_W2 + ((size_t)(ng * 5 + 4) * KS2 + k0) * 1024 + l16;
  G2 g[5];
#pragma unroll
  for (int i = 0; i < 5; i++) {
    g[i].aa = gldf(pa + i * 2048); g[i].b0a = gldf(pb0 + i * 2048); g[i].b1a = gldf(pb0 + KS2 * 1024 + i * 2048);
    g[i].b4a = gldf(pb4 + i * 2048); g[i].ab = gldf(pa + i * 2048 + dab); g[i].b0b = gldf(pb0 + i * 2048 + dab);
    g[i].b1b = gldf(pb0 + KS2 * 1024 + i * 2048 + dab);
  }
  SB();
  f32x16 ga[3] = {};
#pragma unroll
  for (int i = 0; i < 5; i++) {
    ga[0] = MFMA(g[i].b0a, g[i].aa, ga[0]); ga[1] = MFMA(g[i].b1a, g[i].aa, ga[1]); ga[2] = MFMA(g[i].b4a, g[i].aa, ga[2]);
    ga[0] = MFMA(g[i].b0b, g[i].ab, ga[0]); ga[1] = MFMA(g[i].b1b, g[i].ab, ga[1]);
  }
  g2_reduce<true>(ga, w, lane, tid, mt, ng, x, ws, out);
}

extern "C" void kernel_launch(void* const* d_in, const int* in_sizes, int n_in, void* d_out, int out_size, void* d_ws,
                              size_t ws_size, hipStream_t stream) {
  const char* x = (const char*)d_in[0];
  (void)hipFuncSetAttribute((const void*)k1, hipFuncAttributeMaxDynamicSharedMemorySize, K1_LDS);
  (void)hipFuncSetAttribute((const void*)k2, hipFuncAttributeMaxDynamicSharedMemorySize, K2_LDS);
  (void)hipFuncSetAttribute((const void*)k3, hipFuncAttributeMaxDynamicSharedMemorySize, 110592);
  k1<<<256, 512, K1_LDS, stream>>>(x, (const char*)d_in[1], (const char*)d_in[2], (const char*)d_in[4], (const char*)d_in[5],
                                   (const char*)d_in[6], (char*)d_ws);
  k2<<<256, 512, K2_LDS, stream>>>((char*)d_ws);
  k3<<<256, 512, 110592, stream>>>(x, (char*)d_ws, (char*)d_out);
}
```

```cpp
#define K1_NW0 0
#define K1_ASM0 \
  "s_nop 4\n" \
  "s_add_u32 m0, s67, 0\n" \
  "s_nop 0\n" \
  "global_load_lds_dwordx4 v96, s[68:69] offset:0\n" \
  "global_load_lds_dwordx4 v97, s[68:69] offset:1024\n" \
  "global_load_lds_dwordx4 v98, s[68:69] offset:2048\n" \
  "global_load_lds_dwordx4 v99, s[68:69] offset:3072\n" \
  "s_add_u32 m0, s67, 16384\n" \
  "s_add_u32 s68, s68, 128\n" \
  "s_addc_u32 s69, s69, 0\n" \
  "global_load_lds_dwordx4 v96, s[68:69] offset:0\n" \
  "global_load_lds_dwordx4 v97, s[68:69] offset:1024\n" \
  "global_load_lds_dwordx4 v98, s[68:69] offset:2048\n" \
  "global_load_lds_dwordx4 v99, s[68:69] offset:3072\n" \
  "s_add_u32 m0, s67, 32768\n" \
  "s_add_u32 s68, s68, 128\n" \
  "s_addc_u32 s69, s69, 0\n" \
  "global_load_lds_dwordx4 v96, s[68:69] offset:0\n" \
  "global_load_lds_dwordx4 v97, s[68:69] offset:1024\n" \
  "global_load_lds_dwordx4 v98, s[68:69] offset:2048\n" \
  "global_load_lds_dwordx4 v99, s[68:69] offset:3072\n" \
  "s_add_u32 m0, s67, 49152\n" \
  "s_add_u32 s68, s68, 128\n" \
  "s_addc_u32 s69, s69, 0\n" \
  "global_load_lds_dwordx4 v96, s[68:69] offset:0\n" \
  "global_load_lds_dwordx4 v97, s[68:69] offset:1024\n" \
  "global_load_lds_dwordx4 v98, s[68:69] offset:2048\n" \
  "global_load_lds_dwordx4 v99, s[68:69] offset:3072\n"
#define K1_ASM \
  "s_nop 4\n" \
  "v_add_u32_e32 v104, 0x10000, v100\n" \
  "v_add_u32_e32 v108, 0x20000, v100\n" \
  "v_mov_b32_e32 v112, 0\n" \
  "v_add_u32_e32 v105, 0x10000, v101\n" \
  "v_add_u32_e32 v109, 0x20000, v101\n" \
  "v_mov_b32_e32 v113, 0\n" \
  "v_add_u32_e32 v106, 0x10000, v102\n" \
  "v_add_u32_e32 v110, 0x20000, v102\n" \
  "v_mov_b32_e32 v114, 0\n" \
  "v_add_u32_e32 v107, 0x10000, v103\n" \
  "v_add_u32_e32 v111, 0x20000, v103\n" \
  "v_mov_b32_e32 v115, 0\n" \
  "s_waitcnt vmcnt(12)\n" \
  "ds_read_b128 v[0:3], v100 offset:0\n" \
  "ds_read_b128 v[4:7], v101 offset:0\n" \
  "ds_read_b128 v[8:11], v102 offset:0\n" \
  "ds_read_b128 v[12:15], v103 offset:0\n" \
  "s_waitcnt lgkmcnt(0)\n" \
  "s_waitcnt vmcnt(8)\n" \
  "ds_read_b128 v[16:19], v100 offset:16384\n" \
  "ds_read_b128 v[20:23], v101 offset:16384\n" \
  "ds_read_b128 v[24:27], v102 offset:16384\n" \
  "ds_read_b128 v[28:31], v103 offset:16384\n" \
  "s_add_u32 m0, s67, 65536\n" \
  "s_add_u32 s68, s68, 128\n" \
  "s_addc_u32 s69, s69, 0\n" \
  "v_dot2c_f32_bf16_e32 v112, s76, v0\n" \
  "v_dot2c_f32_bf16_e32 v114, v0, v0\n" \
  "v_dot2c_f32_bf16_e32 v113, s76, v1\n" \
  "v_dot2c_f32_bf16_e32 v115, v1, v1\n" \
  "global_load_lds_dwordx4 v96, s[68:69] offset:0\n" \
  "v_dot2c_f32_bf16_e32 v112, s76, v2\n" \
  "v_dot2c_f32_bf16_e32 v114, v2, v2\n" \
  "global_load_lds_dwordx4 v97, s[68:69] offset:1024\n" \
  "v_dot2c_f32_bf16_e32 v113, s76, v3\n" \
  "v_dot2c_f32_bf16_e32 v115, v3, v3\n" \
  "global_load_lds_dwordx4 v98, s[68:69] offset:2048\n" \
  "v_dot2c_f32_bf16_e32 v112, s76, v4\n" \
  "v_dot2c_f32_bf16_e32 v114, v4, v4\n" \
  "v_dot2c_f32_bf16_e32 v113, s76, v5\n" \
  "v_dot2c_f32_bf16_e32 v115, v5, v5\n" \
  "global_load_lds_dwordx4 v99, s[68:69] offset:3072\n" \
  "v_dot2c_f32_bf16_e32 v112, s76, v6\n" \
  "v_dot2c_f32_bf16_e32 v114, v6, v6\n" \
  "v_dot2c_f32_bf16_e32 v113, s76, v7\n" \
  "v_dot2c_f32_bf16_e32 v115, v7, v7\n" \
  "s_add_u32 m0, s67, 81920\n" \
  "s_add_u32 s68, s68, 128\n" \
  "s_addc_u32 s69, s69, 0\n" \
  "v_dot2c_f32_bf16_e32 v112, s76, v8\n" \
  "v_dot2c_f32_bf16_e32 v114, v8, v8\n" \
  "v_dot2c_f32_bf16_e32 v113, s76, v9\n" \
  "v_dot2c_f32_bf16_e32 v115, v9, v9\n" \
  "global_load_lds_dwordx4 v96, s[68:69] offset:0\n" \
  "v_dot2c_f32_bf16_e32 v112, s76, v10\n" \
  "v_dot2c_f32_bf16_e32 v114, v10, v10\n" \
  "global_load_lds_dwordx4 v97, s[68:69] offset:1024\n" \
  "v_dot2c_f32_bf16_e32 v113, s76, v11\n" \
  "v_dot2c_f32_bf16_e32 v115, v11, v11\n" \
  "global_load_lds_dwordx4 v98, s[68:69] offset:2048\n" \
  "v_dot2c_f32_bf16_e32 v112, s76, v12\n" \
  "v_dot2c_f32_bf16_e32 v114, v12, v12\n" \
  "v_dot2c_f32_bf16_e32 v113, s76, v13\n" \
  "v_dot2c_f32_bf16_e32 v115, v13, v13\n" \
  "global_load_lds_dwordx4 v99, s[68:69] offset:3072\n" \
  "v_dot2c_f32_bf16_e32 v112, s76, v14\n" \
  "v_dot2c_f32_bf16_e32 v114, v14, v14\n" \
  "v_dot2c_f32_bf16_e32 v113, s76, v15\n" \
  "v_dot2c_f32_bf16_e32 v115, v15, v15\n" \
  "s_waitcnt lgkmcnt(0)\n" \
  "s_waitcnt vmcnt(12)\n" \
  "ds_read_b128 v[32:35], v100 offset:32768\n" \
  "ds_read_b128 v[36:39], v101 offset:32768\n" \
  "ds_read_b128 v[40:43], v102 offset:32768\n" \
  "ds_read_b128 v[44:47], v103 offset:32768\n" \
  "s_add_u32 m0, s67, 98304\n" \
  "s_add_u32 s68, s68, 128\n" \
  "s_addc_u32 s69, s69, 0\n" \
  "v_dot2c_f32_bf16_e32 v112, s76, v16\n" \
  "v_dot2c_f32_bf16_e32 v114, v16, v16\n" \
  "v_dot2c_f32_bf16_e32 v113, s76, v17\n" \
  "v_dot2c_f32_bf16_e32 v115, v17, v17\n" \
  "v_dot2c_f32_bf16_e32 v112, s76, v18\n" \
  "v_dot2c_f32_bf16_e32 v114, v18, v18\n" \
  "global_load_lds_dwordx4 v96, s[68:69] offset:0\n" \
  "v_dot2c_f32_bf16_e32 v113, s76, v19\n" \
  "v_dot2c_f32_bf16_e32 v115, v19, v19\n" \
  "v_dot2c_f32_bf16_e32 v112, s76, v20\n" \
  "v_dot2c_f32_bf16_e32 v114, v20, v20\n" \
  "v_dot2c_f32_bf16_e32 v113, s76, v21\n" \
  "v_dot2c_f32_bf16_e32 v115, v21, v21\n" \
  "global_load_lds_dwordx4 v97, s[68:69] offset:1024\n" \
  "v_dot2c_f32_bf16_e32 v112, s76, v22\n" \
  "v_dot2c_f32_bf16_e32 v114, v22, v22\n" \
  "v_dot2c_f32_bf16_e32 v113, s76, v23\n" \
  "v_dot2c_f32_bf16_e32 v115, v23, v23\n" \
  "global_load_lds_dwordx4 v98, s[68:69] offset:2048\n" \
  "v_dot2c_f32_bf16_e32 v112, s76, v24\n" \
  "v_dot2c_f32_bf16_e32 v114, v24, v24\n" \
  "v_dot2c_f32_bf16_e32 v113, s76, v25\n" \
  "v_dot2c_f32_bf16_e32 v115, v25, v25\n" \
  "v_dot2c_f32_bf16_e32 v112, s76, v26\n" \
  "v_dot2c_f32_bf16_e32 v114, v26, v26\n" \
  "global_load_lds_dwordx4 v99, s[68:69] offset:3072\n" \
  "v_dot2c_f32_bf16_e32 v113, s76, v27\n" \
  "v_dot2c_f32_bf16_e32 v115, v27, v27\n" \
  "v_dot2c_f32_bf16_e32 v112, s76, v28\n" \
  "v_dot2c_f32_bf16_e32 v114, v28, v28\n" \
  "v_dot2c_f32_bf16_e32 v113, s76, v29\n" \
  "v_dot2c_f32_bf16_e32 v115, v29, v29\n" \
  "v_dot2c_f32_bf16_e32 v112, s76, v30\n" \
  "v_dot2c_f32_bf16_e32 v114, v30, v30\n" \
  "v_dot2c_f32_bf16_e32 v113, s76, v31\n" \
  "v_dot2c_f32_bf16_e32 v115, v31, v31\n" \
  "s_waitcnt lgkmcnt(0)\n" \
  "s_waitcnt vmcnt(12)\n" \
  "ds_read_b128 v[48:51], v100 offset:49152\n" \
  "ds_read_b128 v[52:55], v101 offset:49152\n" \
  "ds_read_b128 v[56:59], v102 offset:49152\n" \
  "ds_read_b128 v[60:63], v103 offset:49152\n" \
  "s_add_u32 m0, s67, 114688\n" \
  "s_add_u32 s68, s68, 128\n" \
  "s_addc_u32 s69, s69, 0\n" \
  "v_dot2c_f32_bf16_e32 v112, s76, v32\n" \
  "v_dot2c_f32_bf16_e32 v114, v32, v32\n" \
  "v_dot2c_f32_bf16_e32 v113, s76, v33\n" \
  "v_dot2c_f32_bf16_e32 v115, v33, v33\n" \
  "v_dot2c_f32_bf16_e32 v112, s76, v34\n" \
  "v_dot2c_f32_bf16_e32 v114, v34, v34\n" \
  "global_load_lds_dwordx4 v96, s[68:69] offset:0\n" \
  "v_dot2c_f32_bf16_e32 v113, s76, v35\n" \
  "v_dot2c_f32_bf16_e32 v115, v35, v35\n" \
  "v_dot2c_f32_bf16_e32 v112, s76, v36\n" \
  "v_dot2c_f32_bf16_e32 v114, v36, v36\n" \
  "v_dot2c_f32_bf16_e32 v113, s76, v37\n" \
  "v_dot2c_f32_bf16_e32 v115, v37, v37\n" \
  "global_load_lds_dwordx4 v97, s[68:69] offset:1024\n" \
  "v_dot2c_f32_bf16_e32 v112, s76, v38\n" \
  "v_dot2c_f32_bf16_e32 v114, v38, v38\n" \
  "v_dot2c_f32_bf16_e32 v113, s76, v39\n" \
  "v_dot2c_f32_bf16_e32 v115, v39, v39\n" \
  "global_load_lds_dwordx4 v98, s[68:69] offset:2048\n" \
  "v_dot2c_f32_bf16_e32 v112, s76, v40\n" \
  "v_dot2c_f32_bf16_e32 v114, v40, v40\n" \
  "v_dot2c_f32_bf16_e32 v113, s76, v41\n" \
  "v_dot2c_f32_bf16_e32 v115, v41, v41\n" \
  "v_dot2c_f32_bf16_e32 v112, s76, v42\n" \
  "v_dot2c_f32_bf16_e32 v114, v42, v42\n" \
  "global_load_lds_dwordx4 v99, s[68:69] offset:3072\n" \
  "v_dot2c_f32_bf16_e32 v113, s76, v43\n" \
  "v_dot2c_f32_bf16_e32 v115, v43, v43\n" \
  "v_dot2c_f32_bf16_e32 v112, s76, v44\n" \
  "v_dot2c_f32_bf16_e32 v114, v44, v44\n" \
  "v_dot2c_f32_bf16_e32 v113, s76, v45\n" \
  "v_dot2c_f32_bf16_e32 v115, v45, v45\n" \
  "v_dot2c_f32_bf16_e32 v112, s76, v46\n" \
  "v_dot2c_f32_bf16_e32 v114, v46, v46\n" \
  "v_dot2c_f32_bf16_e32 v113, s76, v47\n" \
  "v_dot2c_f32_bf16_e32 v115, v47, v47\n" \
  "s_waitcnt lgkmcnt(0)\n" \
  "s_waitcnt vmcnt(12)\n" \
  "ds_read_b128 v[64:67], v104 offset:0\n" \
  "ds_read_b128 v[68:71], v105 offset:0\n" \
  "ds_read_b128 v[72:75], v106 offset:0\n" \
  "ds_read_b128 v[76:79], v107 offset:0\n" \
  "s_add_u32 m0, s67, 131072\n" \
  "s_add_u32 s68, s68, 128\n" \
  "s_addc_u32 s69, s69, 0\n" \
  "v_dot2c_f32_bf16_e32 v112, s76, v48\n" \
  "v_dot2c_f32_bf16_e32 v114, v48, v48\n" \
  "v_dot2c_f32_bf16_e32 v113, s76, v49\n" \
  "v_dot2c_f32_bf16_e32 v115, v49, v49\n" \
  "v_dot2c_f32_bf16_e32 v112, s76, v50\n" \
  "v_dot2c_f32_bf16_e32 v114, v50, v50\n" \
  "global_load_lds_dwordx4 v96, s[68:69] offset:0\n" \
  "v_dot2c_f32_bf16_e32 v113, s76, v51\n" \
  "v_dot2c_f32_bf16_e32 v115, v51, v51\n" \
  "v_dot2c_f32_bf16_e32 v112, s76, v52\n" \
  "v_dot2c_f32_bf16_e32 v114, v52, v52\n" \
  "v_dot2c_f32_bf16_e32 v113, s76, v53\n" \
  "v_dot2c_f32_bf16_e32 v115, v53, v53\n" \
  "global_load_lds_dwordx4 v97, s[68:69] offset:1024\n" \
  "v_dot2c_f32_bf16_e32 v112, s76, v54\n" \
  "v_dot2c_f32_bf16_e32 v114, v54, v54\n" \
  "v_dot2c_f32_bf16_e32 v113, s76, v55\n" \
  "v_dot2c_f32_bf16_e32 v115, v55, v55\n" \
  "global_load_lds_dwordx4 v98, s[68:69] offset:2048\n" \
  "v_dot2c_f32_bf16_e32 v112, s76, v56\n" \
  "v_dot2c_f32_bf16_e32 v114, v56, v56\n" \
  "v_dot2c_f32_bf16_e32 v113, s76, v57\n" \
  "v_dot2c_f32_bf16_e32 v115, v57, v57\n" \
  "v_dot2c_f32_bf16_e32 v112, s76, v58\n" \
  "v_dot2c_f32_bf16_e32 v114, v58, v58\n" \
  "global_load_lds_dwordx4 v99, s[68:69] offset:3072\n" \
  "v_dot2c_f32_bf16_e32 v113, s76, v59\n" \
  "v_dot2c_f32_bf16_e32 v115, v59, v59\n" \
  "v_dot2c_f32_bf16_e32 v112, s76, v60\n" \
  "v_dot2c_f32_bf16_e32 v114, v60, v60\n" \
  "v_dot2c_f32_bf16_e32 v113, s76, v61\n" \
  "v_dot2c_f32_bf16_e32 v115, v61, v61\n" \
  "v_dot2c_f32_bf16_e32 v112, s76, v62\n" \
  "v_dot2c_f32_bf16_e32 v114, v62, v62\n" \
  "v_dot2c_f32_bf16_e32 v113, s76, v63\n" \
  "v_dot2c_f32_bf16_e32 v115, v63, v63\n" \
  "s_waitcnt lgkmcnt(0)\n" \
  "s_waitcnt vmcnt(12)\n" \
  "ds_read_b128 v[80:83], v104 offset:16384\n" \
  "ds_read_b128 v[84:87], v105 offset:16384\n" \
  "ds_read_b128 v[88:91], v106 offset:16384\n" \
  "ds_read_b128 v[92:95], v107 offset:16384\n" \
  "s_add_u32 m0, s67, 147456\n" \
  "s_add_u32 s68, s68, 128\n" \
  "s_addc_u32 s69, s69, 0\n" \
  "v_dot2c_f32_bf16_e32 v112, s76, v64\n" \
  "v_dot2c_f32_bf16_e32 v114, v64, v64\n" \
  "v_dot2c_f32_bf16_e32 v113, s76, v65\n" \
  "v_dot2c_f32_bf16_e32 v115, v65, v65\n" \
  "v_dot2c_f32_bf16_e32 v112, s76, v66\n" \
  "v_dot2c_f32_bf16_e32 v114, v66, v66\n" \
  "global_load_lds_dwordx4 v96, s[68:69] offset:0\n" \
  "v_dot2c_f32_bf16_e32 v113, s76, v67\n" \
  "v_dot2c_f32_bf16_e32 v115, v67, v67\n" \
  "v_dot2c_f32_bf16_e32 v112, s76, v68\n" \
  "v_dot2c_f32_bf16_e32 v114, v68, v68\n" \
  "v_dot2c_f32_bf16_e32 v113, s76, v69\n" \
  "v_dot2c_f32_bf16_e32 v115, v69, v69\n" \
  "global_load_lds_dwordx4 v97, s[68:69] offset:1024\n" \
  "v_dot2c_f32_bf16_e32 v112, s76, v70\n" \
  "v_dot2c_f32_bf16_e32 v114, v70, v70\n" \
  "v_dot2c_f32_bf16_e32 v113, s76, v71\n" \
  "v_dot2c_f32_bf16_e32 v115, v71, v71\n" \
  "global_load_lds_dwordx4 v98, s[68:69] offset:2048\n" \
  "v_dot2c_f32_bf16_e32 v112, s76, v72\n" \
  "v_dot2c_f32_bf16_e32 v114, v72, v72\n" \
  "v_dot2c_f32_bf16_e32 v113, s76, v73\n" \
  "v_dot2c_f32_bf16_e32 v115, v73, v73\n" \
  "v_dot2c_f32_bf16_e32 v112, s76, v74\n" \
  "v_dot2c_f32_bf16_e32 v114, v74, v74\n" \
  "global_load_lds_dwordx4 v99, s[68:69] offset:3072\n" \
  "v_dot2c_f32_bf16_e32 v113, s76, v75\n" \
  "v_dot2c_f32_bf16_e32 v115, v75, v75\n" \
  "v_dot2c_f32_bf16_e32 v112, s76, v76\n" \
  "v_dot2c_f32_bf16_e32 v114, v76, v76\n" \
  "v_dot2c_f32_bf16_e32 v113, s76, v77\n" \
  "v_dot2c_f32_bf16_e32 v115, v77, v77\n" \
  "v_dot2c_f32_bf16_e32 v112, s76, v78\n" \
  "v_dot2c_f32_bf16_e32 v114, v78, v78\n" \
  "v_dot2c_f32_bf16_e32 v113, s76, v79\n" \
  "v_dot2c_f32_bf16_e32 v115, v79, v79\n" \
  "s_waitcnt lgkmcnt(0)\n" \
  "s_waitcnt vmcnt(12)\n" \
  "ds_read_b128 v[160:163], v104 offset:32768\n" \
  "ds_read_b128 v[164:167], v105 offset:32768\n" \
  "ds_read_b128 v[168:171], v106 offset:32768\n" \
  "ds_read_b128 v[172:175], v107 offset:32768\n" \
  "s_add_u32 m0, s67, 0\n" \
  "v_dot2c_f32_bf16_e32 v112, s76, v80\n" \
  "v_dot2c_f32_bf16_e32 v114, v80, v80\n" \
  "v_dot2c_f32_bf16_e32 v113, s76, v81\n" \
  "v_dot2c_f32_bf16_e32 v115, v81, v81\n" \
  "v_dot2c_f32_bf16_e32 v112, s76, v82\n" \
  "v_dot2c_f32_bf16_e32 v114, v82, v82\n" \
  "global_load_lds_dwordx4 v228, s[40:41] offset:0\n" \
  "v_dot2c_f32_bf16_e32 v113, s76, v83\n" \
  "v_dot2c_f32_bf16_e32 v115, v83, v83\n" \
  "v_dot2c_f32_bf16_e32 v112, s76, v84\n" \
  "v_dot2c_f32_bf16_e32 v114, v84, v84\n" \
  "v_dot2c_f32_bf16_e32 v113, s76, v85\n" \
  "v_dot2c_f32_bf16_e32 v115, v85, v85\n" \
  "global_load_lds_dwordx4 v228, s[42:43] offset:1024\n" \
  "v_dot2c_f32_bf16_e32 v112, s76, v86\n" \
  "v_dot2c_f32_bf16_e32 v114, v86, v86\n" \
  "v_dot2c_f32_bf16_e32 v113, s76, v87\n" \
  "v_dot2c_f32_bf16_e32 v115, v87, v87\n" \
  "global_load_lds_dwordx4 v228, s[44:45] offset:2048\n" \
  "v_dot2c_f32_bf16_e32 v112, s76, v88\n" \
  "v_dot2c_f32_bf16_e32 v114, v88, v88\n" \
  "v_dot2c_f32_bf16_e32 v113, s76, v89\n" \
  "v_dot2c_f32_bf16_e32 v115, v89, v89\n" \
  "v_dot2c_f32_bf16_e32 v112, s76, v90\n" \
  "v_dot2c_f32_bf16_e32 v114, v90, v90\n" \
  "global_load_lds_dwordx4 v228, s[46:47] offset:3072\n" \
  "v_dot2c_f32_bf16_e32 v113, s76, v91\n" \
  "v_dot2c_f32_bf16_e32 v115, v91, v91\n" \
  "v_dot2c_f32_bf16_e32 v112, s76, v92\n" \
  "v_dot2c_f32_bf16_e32 v114, v92, v92\n" \
  "v_dot2c_f32_bf16_e32 v113, s76, v93\n" \
  "v_dot2c_f32_bf16_e32 v115, v93, v93\n" \
  "v_add_u32_e32 v228, s64, v228\n" \
  "v_dot2c_f32_bf16_e32 v112, s76, v94\n" \
  "v_dot2c_f32_bf16_e32 v114, v94, v94\n" \
  "v_dot2c_f32_bf16_e32 v113, s76, v95\n" \
  "v_dot2c_f32_bf16_e32 v115, v95, v95\n" \
  "s_waitcnt lgkmcnt(0)\n" \
  "s_waitcnt vmcnt(12)\n" \
  "ds_read_b128 v[176:179], v104 offset:49152\n" \
  "ds_read_b128 v[180:183], v105 offset:49152\n" \
  "ds_read_b128 v[184:187], v106 offset:49152\n" \
  "ds_read_b128 v[188:191], v107 offset:49152\n" \
  "s_add_u32 m0, s67, 16384\n" \
  "v_dot2c_f32_bf16_e32 v112, s76, v160\n" \
  "v_dot2c_f32_bf16_e32 v114, v160, v160\n" \
  "v_dot2c_f32_bf16_e32 v113, s76, v161\n" \
  "v_dot2c_f32_bf16_e32 v115, v161, v161\n" \
  "v_dot2c_f32_bf16_e32 v112, s76, v162\n" \
  "v_dot2c_f32_bf16_e32 v114, v162, v162\n" \
  "global_load_lds_dwordx4 v228, s[40:41] offset:0\n" \
  "v_dot2c_f32_bf16_e32 v113, s76, v163\n" \
  "v_dot2c_f32_bf16_e32 v115, v163, v163\n" \
  "v_dot2c_f32_bf16_e32 v112, s76, v164\n" \
  "v_dot2c_f32_bf16_e32 v114, v164, v164\n" \
  "v_dot2c_f32_bf16_e32 v113, s76, v165\n" \
  "v_dot2c_f32_bf16_e32 v115, v165, v165\n" \
  "global_load_lds_dwordx4 v228, s[42:43] offset:1024\n" \
  "v_dot2c_f32_bf16_e32 v112, s76, v166\n" \
  "v_dot2c_f32_bf16_e32 v114, v166, v166\n" \
  "v_dot2c_f32_bf16_e32 v113, s76, v167\n" \
  "v_dot2c_f32_bf16_e32 v115, v167, v167\n" \
  "global_load_lds_dwordx4 v228, s[44:45] offset:2048\n" \
  "v_dot2c_f32_bf16_e32 v112, s76, v168\n" \
  "v_dot2c_f32_bf16_e32 v114, v168, v168\n" \
  "v_dot2c_f32_bf16_e32 v113, s76, v169\n" \
  "v_dot2c_f32_bf16_e32 v115, v169, v169\n" \
  "v_dot2c_f32_bf16_e32 v112, s76, v170\n" \
  "v_dot2c_f32_bf16_e32 v114, v170, v170\n" \
  "global_load_lds_dwordx4 v228, s[46:47] offset:3072\n" \
  "v_dot2c_f32_bf16_e32 v113, s76, v171\n" \
  "v_dot2c_f32_bf16_e32 v115, v171, v171\n" \
  "v_dot2c_f32_bf16_e32 v112, s76, v172\n" \
  "v_dot2c_f32_bf16_e32 v114, v172, v172\n" \
  "v_dot2c_f32_bf16_e32 v113, s76, v173\n" \
  "v_dot2c_f32_bf16_e32 v115, v173, v173\n" \
  "v_add_u32_e32 v228, s64, v228\n" \
  "v_dot2c_f32_bf16_e32 v112, s76, v174\n" \
  "v_dot2c_f32_bf16_e32 v114, v174, v174\n" \
  "v_dot2c_f32_bf16_e32 v113, s76, v175\n" \
  "v_dot2c_f32_bf16_e32 v115, v175, v175\n" \
  "s_waitcnt lgkmcnt(0)\n" \
  "s_waitcnt vmcnt(12)\n" \
  "ds_read_b128 v[192:195], v108 offset:0\n" \
  "ds_read_b128 v[196:199], v109 offset:0\n" \
  "ds_read_b128 v[200:203], v110 offset:0\n" \
  "ds_read_b128 v[204:207], v111 offset:0\n" \
  "s_add_u32 m0, s67, 32768\n" \
  "v_dot2c_f32_bf16_e32 v112, s76, v176\n" \
  "v_dot2c_f32_bf16_e32 v114, v176, v176\n" \
  "v_dot2c_f32_bf16_e32 v113, s76, v177\n" \
  "v_dot2c_f32_bf16_e32 v115, v177, v177\n" \
  "v_dot2c_f32_bf16_e32 v112, s76, v178\n" \
  "v_dot2c_f32_bf16_e32 v114, v178, v178\n" \
  "global_load_lds_dwordx4 v228, s[40:41] offset:0\n" \
  "v_dot2c_f32_bf16_e32 v113, s76, v179\n" \
  "v_dot2c_f32_bf16_e32 v115, v179, v179\n" \
  "v_dot2c_f32_bf16_e32 v112, s76, v180\n" \
  "v_dot2c_f32_bf16_e32 v114, v180, v180\n" \
  "v_dot2c_f32_bf16_e32 v113, s76, v181\n" \
  "v_dot2c_f32_bf16_e32 v115, v181, v181\n" \
  "global_load_lds_dwordx4 v228, s[42:43] offset:1024\n" \
  "v_dot2c_f32_bf16_e32 v112, s76, v182\n" \
  "v_dot2c_f32_bf16_e32 v114, v182, v182\n" \
  "v_dot2c_f32_bf16_e32 v113, s76, v183\n" \
  "v_dot2c_f32_bf16_e32 v115, v183, v183\n" \
  "global_load_lds_dwordx4 v228, s[44:45] offset:2048\n" \
  "v_dot2c_f32_bf16_e32 v112, s76, v184\n" \
  "v_dot2c_f32_bf16_e32 v114, v184, v184\n" \
  "v_dot2c_f32_bf16_e32 v113, s76, v185\n" \
  "v_dot2c_f32_bf16_e32 v115, v185, v185\n" \
  "v_dot2c_f32_bf16_e32 v112, s76, v186\n" \
  "v_dot2c_f32_bf16_e32 v114, v186, v186\n" \
  "global_load_lds_dwordx4 v228, s[46:47] offset:3072\n" \
  "v_dot2c_f32_bf16_e32 v113, s76, v187\n" \
  "v_dot2c_f32_bf16_e32 v115, v187, v187\n" \
  "v_dot2c_f32_bf16_e32 v112, s76, v188\n" \
  "v_dot2c_f32_bf16_e32 v114, v188, v188\n" \
  "v_dot2c_f32_bf16_e32 v113, s76, v189\n" \
  "v_dot2c_f32_bf16_e32 v115, v189, v189\n" \
  "v_add_u32_e32 v228, s64, v228\n" \
  "v_dot2c_f32_bf16_e32 v112, s76, v190\n" \
  "v_dot2c_f32_bf16_e32 v114, v190, v190\n" \
  "v_dot2c_f32_bf16_e32 v113, s76, v191\n" \
  "v_dot2c_f32_bf16_e32 v115, v191, v191\n" \
  "s_waitcnt lgkmcnt(0)\n" \
  "s_waitcnt vmcnt(12)\n" \
  "ds_read_b128 v[208:211], v108 offset:16384\n" \
  "ds_read_b128 v[212:215], v109 offset:16384\n" \
  "ds_read_b128 v[216:219], v110 offset:16384\n" \
  "ds_read_b128 v[220:223], v111 offset:16384\n" \
  "s_add_u32 m0, s67, 49152\n" \
  "v_dot2c_f32_bf16_e32 v112, s76, v192\n" \
  "v_dot2c_f32_bf16_e32 v114, v192, v192\n" \
  "v_dot2c_f32_bf16_e32 v113, s76, v193\n" \
  "v_dot2c_f32_bf16_e32 v115, v193, v193\n" \
  "v_dot2c_f32_bf16_e32 v112, s76, v194\n" \
  "v_dot2c_f32_bf16_e32 v114, v194, v194\n" \
  "global_load_lds_dwordx4 v228, s[40:41] offset:0\n" \
  "v_dot2c_f32_bf16_e32 v113, s76, v195\n" \
  "v_dot2c_f32_bf16_e32 v115, v195, v195\n" \
  "v_dot2c_f32_bf16_e32 v112, s76, v196\n" \
  "v_dot2c_f32_bf16_e32 v114, v196, v196\n" \
  "v_dot2c_f32_bf16_e32 v113, s76, v197\n" \
  "v_dot2c_f32_bf16_e32 v115, v197, v197\n" \
  "global_load_lds_dwordx4 v228, s[42:43] offset:1024\n" \
  "v_dot2c_f32_bf16_e32 v112, s76, v198\n" \
  "v_dot2c_f32_bf16_e32 v114, v198, v198\n" \
  "v_dot2c_f32_bf16_e32 v113, s76, v199\n" \
  "v_dot2c_f32_bf16_e32 v115, v199, v199\n" \
  "global_load_lds_dwordx4 v228, s[44:45] offset:2048\n" \
  "v_dot2c_f32_bf16_e32 v112, s76, v200\n" \
  "v_dot2c_f32_bf16_e32 v114, v200, v200\n" \
  "v_dot2c_f32_bf16_e32 v113, s76, v201\n" \
  "v_dot2c_f32_bf16_e32 v115, v201, v201\n" \
  "v_dot2c_f32_bf16_e32 v112, s76, v202\n" \
  "v_dot2c_f32_bf16_e32 v114, v202, v202\n" \
  "global_load_lds_dwordx4 v228, s[46:47] offset:3072\n" \
  "v_dot2c_f32_bf16_e32 v113, s76, v203\n" \
  "v_dot2c_f32_bf16_e32 v115, v203, v203\n" \
  "v_dot2c_f32_bf16_e32 v112, s76, v204\n" \
  "v_dot2c_f32_bf16_e32 v114, v204, v204\n" \
  "v_dot2c_f32_bf16_e32 v113, s76, v205\n" \
  "v_dot2c_f32_bf16_e32 v115, v205, v205\n" \
  "v_add_u32_e32 v228, s64, v228\n" \
  "v_dot2c_f32_bf16_e32 v112, s76, v206\n" \
  "v_dot2c_f32_bf16_e32 v114, v206, v206\n" \
  "v_dot2c_f32_bf16_e32 v113, s76, v207\n" \
  "v_dot2c_f32_bf16_e32 v115, v207, v207\n" \
  "s_waitcnt lgkmcnt(0)\n" \
  "s_waitcnt vmcnt(12)\n" \
  "s_barrier\n" \
  "ds_read_b64_tr_b16 a[0:1], v224 offset:0\n" \
  "ds_read_b64_tr_b16 a[2:3], v224 offset:512\n" \
  "ds_read_b64_tr_b16 a[4:5], v224 offset:1024\n" \
  "ds_read_b64_tr_b16 a[6:7], v224 offset:1536\n" \
  "ds_read_b64_tr_b16 a[8:9], v225 offset:0\n" \
  "ds_read_b64_tr_b16 a[10:11], v225 offset:512\n" \
  "ds_read_b64_tr_b16 a[12:13], v225 offset:1024\n" \
  "ds_read_b64_tr_b16 a[14:15], v225 offset:1536\n" \
  "s_add_u32 m0, s67, 65536\n" \
  "v_dot2c_f32_bf16_e32 v112, s76, v208\n" \
  "v_dot2c_f32_bf16_e32 v114, v208, v208\n" \
  "v_dot2c_f32_bf16_e32 v113, s76, v209\n" \
  "v_dot2c_f32_bf16_e32 v115, v209, v209\n" \
  "v_dot2c_f32_bf16_e32 v112, s76, v210\n" \
  "v_dot2c_f32_bf16_e32 v114, v210, v210\n" \
  "global_load_lds_dwordx4 v228, s[40:41] offset:0\n" \
  "v_dot2c_f32_bf16_e32 v113, s76, v211\n" \
  "v_dot2c_f32_bf16_e32 v115, v211, v211\n" \
  "v_dot2c_f32_bf16_e32 v112, s76, v212\n" \
  "v_dot2c_f32_bf16_e32 v114, v212, v212\n" \
  "v_dot2c_f32_bf16_e32 v113, s76, v213\n" \
  "v_dot2c_f32_bf16_e32 v115, v213, v213\n" \
  "global_load_lds_dwordx4 v228, s[42:43] offset:1024\n" \
  "v_dot2c_f32_bf16_e32 v112, s76, v214\n" \
  "v_dot2c_f32_bf16_e32 v114, v214, v214\n" \
  "v_dot2c_f32_bf16_e32 v113, s76, v215\n" \
  "v_dot2c_f32_bf16_e32 v115, v215, v215\n" \
  "global_load_lds_dwordx4 v228, s[44:45] offset:2048\n" \
  "v_dot2c_f32_bf16_e32 v112, s76, v216\n" \
  "v_dot2c_f32_bf16_e32 v114, v216, v216\n" \
  "v_dot2c_f32_bf16_e32 v113, s76, v217\n" \
  "v_dot2c_f32_bf16_e32 v115, v217, v217\n" \
  "v_dot2c_f32_bf16_e32 v112, s76, v218\n" \
  "v_dot2c_f32_bf16_e32 v114, v218, v218\n" \
  "global_load_lds_dwordx4 v228, s[46:47] offset:3072\n" \
  "v_dot2c_f32_bf16_e32 v113, s76, v219\n" \
  "v_dot2c_f32_bf16_e32 v115, v219, v219\n" \
  "v_dot2c_f32_bf16_e32 v112, s76, v220\n" \
  "v_dot2c_f32_bf16_e32 v114, v220, v220\n" \
  "v_dot2c_f32_bf16_e32 v113, s76, v221\n" \
  "v_dot2c_f32_bf16_e32 v115, v221, v221\n" \
  "v_add_u32_e32 v228, s64, v228\n" \
  "v_dot2c_f32_bf16_e32 v112, s76, v222\n" \
  "v_dot2c_f32_bf16_e32 v114, v222, v222\n" \
  "v_dot2c_f32_bf16_e32 v113, s76, v223\n" \
  "v_dot2c_f32_bf16_e32 v115, v223, v223\n" \
  "s_nop 3\n" \
  "v_add_f32_e32 v112, v112, v113\n" \
  "v_add_f32_e32 v114, v114, v115\n" \
  "v_mov_b32_e32 v113, v112\n" \
  "v_mov_b32_e32 v115, v114\n" \
  "s_nop 1\n" \
  "v_permlane32_swap_b32_e32 v112, v113\n" \
  "v_permlane32_swap_b32_e32 v114, v115\n" \
  "s_nop 1\n" \
  "v_add_f32_e32 v112, v112, v113\n" \
  "v_add_f32_e32 v114, v114, v115\n" \
  "v_mul_f32_e32 v112, 0x3acccccd, v112\n" \
  "v_mul_f32_e32 v114, 0x3acccccd, v114\n" \
  "v_fma_f32 v114, -v112, v112, v114\n" \
  "v_max_f32_e32 v114, 0, v114\n" \
  "v_add_f32_e32 v114, 0x3727c5ac, v114\n" \
  "v_rsq_f32_e32 v233, v114\n" \
  "s_nop 0\n" \
  "v_mul_f32_e64 v234, -v112, v233\n" \
  "v_lshlrev_b32_e32 v236, 16, v0\n" \
  "v_and_b32_e32 v237, s78, v0\n" \
  "v_lshlrev_b32_e32 v238, 16, v1\n" \
  "v_and_b32_e32 v239, s78, v1\n" \
  "v_fma_f32 v236, v236, v233, v234\n" \
  "v_fma_f32 v237, v237, v233, v234\n" \
  "v_fma_f32 v238, v238, v233, v234\n" \
  "v_fma_f32 v239, v239, v233, v234\n" \
  "v_cvt_pk_bf16_f32 v0, v236, v237\n" \
  "v_cvt_pk_bf16_f32 v1, v238, v239\n" \
  "v_lshlrev_b32_e32 v236, 16, v2\n" \
  "v_and_b32_e32 v237, s78, v2\n" \
  "v_lshlrev_b32_e32 v238, 16, v3\n" \
  "v_and_b32_e32 v239, s78, v3\n" \
  "v_fma_f32 v236, v236, v233, v234\n" \
  "v_fma_f32 v237, v237, v233, v234\n" \
  "v_fma_f32 v238, v238, v233, v234\n" \
  "v_fma_f32 v239, v239, v233, v234\n" \
  "v_cvt_pk_bf16_f32 v2, v236, v237\n" \
  "v_cvt_pk_bf16_f32 v3, v238, v239\n" \
  "v_lshlrev_b32_e32 v236, 16, v4\n" \
  "v_and_b32_e32 v237, s78, v4\n" \
  "v_lshlrev_b32_e32 v238, 16, v5\n" \
  "v_and_b32_e32 v239, s78, v5\n" \
  "v_fma_f32 v236, v236, v233, v234\n" \
  "v_fma_f32 v237, v237, v233, v234\n" \
  "v_fma_f32 v238, v238, v233, v234\n" \
  "v_fma_f32 v239, v239, v233, v234\n" \
  "v_cvt_pk_bf16_f32 v4, v236, v237\n" \
  "v_cvt_pk_bf16_f32 v5, v238, v239\n" \
  "s_waitcnt lgkmcnt(0)\n" \
  "v_mfma_f32_32x32x16_bf16 v[96:111], a[0:3], v[0:3], 0\n" \
  "s_add_u32 m0, s67, 81920\n" \
  "ds_read_b64_tr_b16 a[16:17], v224 offset:4096\n" \
  "ds_read_b64_tr_b16 a[18:19], v224 offset:4608\n" \
  "ds_read_b64_tr_b16 a[20:21], v224 offset:5120\n" \
  "global_load_lds_dwordx4 v228, s[40:41] offset:0\n" \
  "v_lshlrev_b32_e32 v236, 16, v6\n" \
  "v_and_b32_e32 v237, s78, v6\n" \
  "v_lshlrev_b32_e32 v238, 16, v7\n" \
  "v_mfma_f32_32x32x16_bf16 v[112:127], a[4:7], v[0:3], 0\n" \
  "ds_read_b64_tr_b16 a[22:23], v224 offset:5632\n" \
  "ds_read_b64_tr_b16 a[24:25], v225 offset:4096\n" \
  "ds_read_b64_tr_b16 a[26:27], v225 offset:4608\n" \
  "v_and_b32_e32 v239, s78, v7\n" \
  "v_fma_f32 v236, v236, v233, v234\n" \
  "v_fma_f32 v237, v237, v233, v234\n" \
  "v_fma_f32 v238, v238, v233, v234\n" \
  "v_fma_f32 v239, v239, v233, v234\n" \
  "v_mfma_f32_32x32x16_bf16 v[128:143], a[8:11], v[0:3], 0\n" \
  "ds_read_b64_tr_b16 a[28:29], v225 offset:5120\n" \
  "ds_read_b64_tr_b16 a[30:31], v225 offset:5632\n" \
  "v_cvt_pk_bf16_f32 v6, v236, v237\n" \
  "v_cvt_pk_bf16_f32 v7, v238, v239\n" \
  "v_lshlrev_b32_e32 v236, 16, v8\n" \
  "v_and_b32_e32 v237, s78, v8\n" \
  "v_lshlrev_b32_e32 v238, 16, v9\n" \
  "v_and_b32_e32 v239, s78, v9\n" \
  "v_mfma_f32_32x32x16_bf16 v[144:159], a[12:15], v[0:3], 0\n" \
  "v_fma_f32 v236, v236, v233, v234\n" \
  "v_fma_f32 v237, v237, v233, v234\n" \
  "v_fma_f32 v238, v238, v233, v234\n" \
  "v_fma_f32 v239, v239, v233, v234\n" \
  "v_cvt_pk_bf16_f32 v8, v236, v237\n" \
  "v_cvt_pk_bf16_f32 v9, v238, v239\n" \
  "s_waitcnt lgkmcnt(0)\n" \
  "v_mfma_f32_32x32x16_bf16 v[96:111], a[16:19], v[4:7], v[96:111]\n" \
  "ds_read_b64_tr_b16 a[0:1], v224 offset:8192\n" \
  "ds_read_b64_tr_b16 a[2:3], v224 offset:8704\n" \
  "ds_read_b64_tr_b16 a[4:5], v224 offset:9216\n" \
  "global_load_lds_dwordx4 v228, s[42:43] offset:1024\n" \
  "v_lshlrev_b32_e32 v236, 16, v10\n" \
  "v_and_b32_e32 v237, s78, v10\n" \
  "v_lshlrev_b32_e32 v238, 16, v11\n" \
  "v_and_b32_e32 v239, s78, v11\n" \
  "v_mfma_f32_32x32x16_bf16 v[112:127], a[20:23], v[4:7], v[112:127]\n" \
  "ds_read_b64_tr_b16 a[6:7], v224 offset:9728\n" \
  "ds_read_b64_tr_b16 a[8:9], v225 offset:8192\n" \
  "ds_read_b64_tr_b16 a[10:11], v225 offset:8704\n" \
  "v_fma_f32 v236, v236, v233, v234\n" \
  "v_fma_f32 v237, v237, v233, v234\n" \
  "v_fma_f32 v238, v238, v233, v234\n" \
  "v_fma_f32 v239, v239, v233, v234\n" \
  "v_cvt_pk_bf16_f32 v10, v236, v237\n" \
  "v_mfma_f32_32x32x16_bf16 v[128:143], a[24:27], v[4:7], v[128:143]\n" \
  "ds_read_b64_tr_b16 a[12:13], v225 offset:9216\n" \
  "ds_read_b64_tr_b16 a[14:15], v225 offset:9728\n" \
  "v_cvt_pk_bf16_f32 v11, v238, v239\n" \
  "v_lshlrev_b32_e32 v236, 16, v12\n" \
  "v_and_b32_e32 v237, s78, v12\n" \
  "v_lshlrev_b32_e32 v238, 16, v13\n" \
  "v_and_b32_e32 v239, s78, v13\n" \
  "v_fma_f32 v236, v236, v233, v234\n" \
  "v_mfma_f32_32x32x16_bf16 v[144:159], a[28:31], v[4:7], v[144:159]\n" \
  "v_fma_f32 v237, v237, v233, v234\n" \
  "v_fma_f32 v238, v238, v233, v234\n" \
  "v_fma_f32 v239, v239, v233, v234\n" \
  "v_cvt_pk_bf16_f32 v12, v236, v237\n" \
  "v_cvt_pk_bf16_f32 v13, v238, v239\n" \
  "s_waitcnt lgkmcnt(0)\n" \
  "v_mfma_f32_32x32x16_bf16 v[96:111], a[0:3], v[8:11], v[96:111]\n" \
  "ds_read_b64_tr_b16 a[16:17], v224 offset:12288\n" \
  "ds_read_b64_tr_b16 a[18:19], v224 offset:12800\n" \
  "ds_read_b64_tr_b16 a[20:21], v224 offset:13312\n" \
  "global_load_lds_dwordx4 v228, s[44:45] offset:2048\n" \
  "v_lshlrev_b32_e32 v236, 16, v14\n" \
  "v_and_b32_e32 v237, s78, v14\n" \
  "v_lshlrev_b32_e32 v238, 16, v15\n" \
  "v_and_b32_e32 v239, s78, v15\n" \
  "v_mfma_f32_32x32x16_bf16 v[112:127], a[4:7], v[8:11], v[112:127]\n" \
  "ds_read_b64_tr_b16 a[22:23], v224 offset:13824\n" \
  "ds_read_b64_tr_b16 a[24:25], v225 offset:12288\n" \
  "ds_read_b64_tr_b16 a[26:27], v225 offset:12800\n" \
  "v_fma_f32 v236, v236, v233, v234\n" \
  "v_fma_f32 v237, v237, v233, v234\n" \
  "v_fma_f32 v238, v238, v233, v234\n" \
  "v_fma_f32 v239, v239, v233, v234\n" \
  "v_cvt_pk_bf16_f32 v14, v236, v237\n" \
  "v_mfma_f32_32x32x16_bf16 v[128:143], a[8:11], v[8:11], v[128:143]\n" \
  "ds_read_b64_tr_b16 a[28:29], v225 offset:13312\n" \
  "ds_read_b64_tr_b16 a[30:31], v225 offset:13824\n" \
  "v_cvt_pk_bf16_f32 v15, v238, v239\n" \
  "v_lshlrev_b32_e32 v236, 16, v16\n" \
  "v_and_b32_e32 v237, s78, v16\n" \
  "v_lshlrev_b32_e32 v238, 16, v17\n" \
  "v_and_b32_e32 v239, s78, v17\n" \
  "v_fma_f32 v236, v236, v233, v234\n" \
  "v_mfma_f32_32x32x16_bf16 v[144:159], a[12:15], v[8:11], v[144:159]\n" \
  "v_fma_f32 v237, v237, v233, v234\n" \
  "v_fma_f32 v238, v238, v233, v234\n" \
  "v_fma_f32 v239, v239, v233, v234\n" \
  "v_cvt_pk_bf16_f32 v16, v236, v237\n" \
  "v_cvt_pk_bf16_f32 v17, v238, v239\n" \
  "s_waitcnt lgkmcnt(0)\n" \
  "v_mfma_f32_32x32x16_bf16 v[96:111], a[16:19], v[12:15], v[96:111]\n" \
  "ds_read_b64_tr_b16 a[0:1], v224 offset:16384\n" \
  "ds_read_b64_tr_b16 a[2:3], v224 offset:16896\n" \
  "ds_read_b64_tr_b16 a[4:5], v224 offset:17408\n" \
  "global_load_lds_dwordx4 v228, s[46:47] offset:3072\n" \
  "v_add_u32_e32 v228, s64, v228\n" \
  "v_lshlrev_b32_e32 v236, 16, v18\n" \
  "v_and_b32_e32 v237, s78, v18\n" \
  "v_lshlrev_b32_e32 v238, 16, v19\n" \
  "v_mfma_f32_32x32x16_bf16 v[112:127], a[20:23], v[12:15], v[112:127]\n" \
  "ds_read_b64_tr_b16 a[6:7], v224 offset:17920\n" \
  "ds_read_b64_tr_b16 a[8:9], v225 offset:16384\n" \
  "ds_read_b64_tr_b16 a[10:11], v225 offset:16896\n" \
  "v_and_b32_e32 v239, s78, v19\n" \
  "v_fma_f32 v236, v236, v233, v234\n" \
  "v_fma_f32 v237, v237, v233, v234\n" \
  "v_fma_f32 v238, v238, v233, v234\n" \
  "v_fma_f32 v239, v239, v233, v234\n" \
  "v_mfma_f32_32x32x16_bf16 v[128:143], a[24:27], v[12:15], v[128:143]\n" \
  "ds_read_b64_tr_b16 a[12:13], v225 offset:17408\n" \
  "ds_read_b64_tr_b16 a[14:15], v225 offset:17920\n" \
  "v_cvt_pk_bf16_f32 v18, v236, v237\n" \
  "v_cvt_pk_bf16_f32 v19, v238, v239\n" \
  "v_lshlrev_b32_e32 v236, 16, v20\n" \
  "v_and_b32_e32 v237, s78, v20\n" \
  "v_lshlrev_b32_e32 v238, 16, v21\n" \
  "v_and_b32_e32 v239, s78, v21\n" \
  "v_mfma_f32_32x32x16_bf16 v[144:159], a[28:31], v[12:15], v[144:159]\n" \
  "v_fma_f32 v236, v236, v233, v234\n" \
  "v_fma_f32 v237, v237, v233, v234\n" \
  "v_fma_f32 v238, v238, v233, v234\n" \
  "v_fma_f32 v239, v239, v233, v234\n" \
  "v_cvt_pk_bf16_f32 v20, v236, v237\n" \
  "v_cvt_pk_bf16_f32 v21, v238, v239\n" \
  "s_waitcnt vmcnt(12) lgkmcnt(0)\n" \
  "s_barrier\n" \
  "v_mfma_f32_32x32x16_bf16 v[96:111], a[0:3], v[16:19], v[96:111]\n" \
  "s_add_u32 m0, s67, 98304\n" \
  "ds_read_b64_tr_b16 a[16:17], v224 offset:20480\n" \
  "ds_read_b64_tr_b16 a[18:19], v224 offset:20992\n" \
  "ds_read_b64_tr_b16 a[20:21], v224 offset:21504\n" \
  "global_load_lds_dwordx4 v228, s[40:41] offset:0\n" \
  "v_lshlrev_b32_e32 v236, 16, v22\n" \
  "v_and_b32_e32 v237, s78, v22\n" \
  "v_lshlrev_b32_e32 v238, 16, v23\n" \
  "v_mfma_f32_32x32x16_bf16 v[112:127], a[4:7], v[16:19], v[112:127]\n" \
  "ds_read_b64_tr_b16 a[22:23], v224 offset:22016\n" \
  "ds_read_b64_tr_b16 a[24:25], v225 offset:20480\n" \
  "ds_read_b64_tr_b16 a[26:27], v225 offset:20992\n" \
  "v_and_b32_e32 v239, s78, v23\n" \
  "v_fma_f32 v236, v236, v233, v234\n" \
  "v_fma_f32 v237, v237, v233, v234\n" \
  "v_fma_f32 v238, v238, v233, v234\n" \
  "v_fma_f32 v239, v239, v233, v234\n" \
  "v_mfma_f32_32x32x16_bf16 v[128:143], a[8:11], v[16:19], v[128:143]\n" \
  "ds_read_b64_tr_b16 a[28:29], v225 offset:21504\n" \
  "ds_read_b64_tr_b16 a[30:31], v225 offset:22016\n" \
  "v_cvt_pk_bf16_f32 v22, v236, v237\n" \
  "v_cvt_pk_bf16_f32 v23, v238, v239\n" \
  "v_lshlrev_b32_e32 v236, 16, v24\n" \
  "v_and_b32_e32 v237, s78, v24\n" \
  "v_lshlrev_b32_e32 v238, 16, v25\n" \
  "v_and_b32_e32 v239, s78, v25\n" \
  "v_mfma_f32_32x32x16_bf16 v[144:159], a[12:15], v[16:19], v[144:159]\n" \
  "v_fma_f32 v236, v236, v233, v234\n" \
  "v_fma_f32 v237, v237, v233, v234\n" \
  "v_fma_f32 v238, v238, v233, v234\n" \
  "v_fma_f32 v239, v239, v233, v234\n" \
  "v_cvt_pk_bf16_f32 v24, v236, v237\n" \
  "v_cvt_pk_bf16_f32 v25, v238, v239\n" \
  "s_waitcnt lgkmcnt(0)\n" \
  "v_mfma_f32_32x32x16_bf16 v[96:111], a[16:19], v[20:23], v[96:111]\n" \
  "ds_read_b64_tr_b16 a[0:1], v224 offset:24576\n" \
  "ds_read_b64_tr_b16 a[2:3], v224 offset:25088\n" \
  "ds_read_b64_tr_b16 a[4:5], v224 offset:25600\n" \
  "global_load_lds_dwordx4 v228, s[42:43] offset:1024\n" \
  "v_lshlrev_b32_e32 v236, 16, v26\n" \
  "v_and_b32_e32 v237, s78, v26\n" \
  "v_lshlrev_b32_e32 v238, 16, v27\n" \
  "v_and_b32_e32 v239, s78, v27\n" \
  "v_mfma_f32_32x32x16_bf16 v[112:127], a[20:23], v[20:23], v[112:127]\n" \
  "ds_read_b64_tr_b16 a[6:7], v224 offset:26112\n" \
  "ds_read_b64_tr_b16 a[8:9], v225 offset:24576\n" \
  "ds_read_b64_tr_b16 a[10:11], v225 offset:25088\n" \
  "v_fma_f32 v236, v236, v233, v234\n" \
  "v_fma_f32 v237, v237, v233, v234\n" \
  "v_fma_f32 v238, v238, v233, v234\n" \
  "v_fma_f32 v239, v239, v233, v234\n" \
  "v_cvt_pk_bf16_f32 v26, v236, v237\n" \
  "v_mfma_f32_32x32x16_bf16 v[128:143], a[24:27], v[20:23], v[128:143]\n" \
  "ds_read_b64_tr_b16 a[12:13], v225 offset:25600\n" \
  "ds_read_b64_tr_b16 a[14:15], v225 offset:26112\n" \
  "v_cvt_pk_bf16_f32 v27, v238, v239\n" \
  "v_lshlrev_b32_e32 v236, 16, v28\n" \
  "v_and_b32_e32 v237, s78, v28\n" \
  "v_lshlrev_b32_e32 v238, 16, v29\n" \
  "v_and_b32_e32 v239, s78, v29\n" \
  "v_fma_f32 v236, v236, v233, v234\n" \
  "v_mfma_f32_32x32x16_bf16 v[144:159], a[28:31], v[20:23], v[144:159]\n" \
  "v_fma_f32 v237, v237, v233, v234\n" \
  "v_fma_f32 v238, v238, v233, v234\n" \
  "v_fma_f32 v239, v239, v233, v234\n" \
  "v_cvt_pk_bf16_f32 v28, v236, v237\n" \
  "v_cvt_pk_bf16_f32 v29, v238, v239\n" \
  "s_waitcnt lgkmcnt(0)\n" \
  "v_mfma_f32_32x32x16_bf16 v[96:111], a[0:3], v[24:27], v[96:111]\n" \
  "ds_read_b64_tr_b16 a[16:17], v224 offset:28672\n" \
  "ds_read_b64_tr_b16 a[18:19], v224 offset:29184\n" \
  "ds_read_b64_tr_b16 a[20:21], v224 offset:29696\n" \
  "global_load_lds_dwordx4 v228, s[44:45] offset:2048\n" \
  "v_lshlrev_b32_e32 v236, 16, v30\n" \
  "v_and_b32_e32 v237, s78, v30\n" \
  "v_lshlrev_b32_e32 v238, 16, v31\n" \
  "v_and_b32_e32 v239, s78, v31\n" \
  "v_mfma_f32_32x32x16_bf16 v[112:127], a[4:7], v[24:27], v[112:127]\n" \
  "ds_read_b64_tr_b16 a[22:23], v224 offset:30208\n" \
  "ds_read_b64_tr_b16 a[24:25], v225 offset:28672\n" \
  "ds_read_b64_tr_b16 a[26:27], v225 offset:29184\n" \
  "v_fma_f32 v236, v236, v233, v234\n" \
  "v_fma_f32 v237, v237, v233, v234\n" \
  "v_fma_f32 v238, v238, v233, v234\n" \
  "v_fma_f32 v239, v239, v233, v234\n" \
  "v_cvt_pk_bf16_f32 v30, v236, v237\n" \
  "v_mfma_f32_32x32x16_bf16 v[128:143], a[8:11], v[24:27], v[128:143]\n" \
  "ds_read_b64_tr_b16 a[28:29], v225 offset:29696\n" \
  "ds_read_b64_tr_b16 a[30:31], v225 offset:30208\n" \
  "v_cvt_pk_bf16_f32 v31, v238, v239\n" \
  "v_lshlrev_b32_e32 v236, 16, v32\n" \
  "v_and_b32_e32 v237, s78, v32\n" \
  "v_lshlrev_b32_e32 v238, 16, v33\n" \
  "v_and_b32_e32 v239, s78, v33\n" \
  "v_fma_f32 v236, v236, v233, v234\n" \
  "v_mfma_f32_32x32x16_bf16 v[144:159], a[12:15], v[24:27], v[144:159]\n" \
  "v_fma_f32 v237, v237, v233, v234\n" \
  "v_fma_f32 v238, v238, v233, v234\n" \
  "v_fma_f32 v239, v239, v233, v234\n" \
  "v_cvt_pk_bf16_f32 v32, v236, v237\n" \
  "v_cvt_pk_bf16_f32 v33, v238, v239\n" \
  "s_waitcnt lgkmcnt(0)\n" \
  "v_mfma_f32_32x32x16_bf16 v[96:111], a[16:19], v[28:31], v[96:111]\n" \
  "ds_read_b64_tr_b16 a[0:1], v224 offset:32768\n" \
  "ds_read_b64_tr_b16 a[2:3], v224 offset:33280\n" \
  "ds_read_b64_tr_b16 a[4:5], v224 offset:33792\n" \
  "global_load_lds_dwordx4 v228, s[46:47] offset:3072\n" \
  "v_add_u32_e32 v228, s64, v228\n" \
  "v_lshlrev_b32_e32 v236, 16, v34\n" \
  "v_and_b32_e32 v237, s78, v34\n" \
  "v_lshlrev_b32_e32 v238, 16, v35\n" \
  "v_mfma_f32_32x32x16_bf16 v[112:127], a[20:23], v[28:31], v[112:127]\n" \
  "ds_read_b64_tr_b16 a[6:7], v224 offset:34304\n" \
  "ds_read_b64_tr_b16 a[8:9], v225 offset:32768\n" \
  "ds_read_b64_tr_b16 a[10:11], v225 offset:33280\n" \
  "v_and_b32_e32 v239, s78, v35\n" \
  "v_fma_f32 v236, v236, v233, v234\n" \
  "v_fma_f32 v237, v237, v233, v234\n" \
  "v_fma_f32 v238, v238, v233, v234\n" \
  "v_fma_f32 v239, v239, v233, v234\n" \
  "v_mfma_f32_32x32x16_bf16 v[128:143], a[24:27], v[28:31], v[128:143]\n" \
  "ds_read_b64_tr_b16 a[12:13], v225 offset:33792\n" \
  "ds_read_b64_tr_b16 a[14:15], v225 offset:34304\n" \
  "v_cvt_pk_bf16_f32 v34, v236, v237\n" \
  "v_cvt_pk_bf16_f32 v35, v238, v239\n" \
  "v_lshlrev_b32_e32 v236, 16, v36\n" \
  "v_and_b32_e32 v237, s78, v36\n" \
  "v_lshlrev_b32_e32 v238, 16, v37\n" \
  "v_and_b32_e32 v239, s78, v37\n" \
  "v_mfma_f32_32x32x16_bf16 v[144:159], a[28:31], v[28:31], v[144:159]\n" \
  "v_fma_f32 v236, v236, v233, v234\n" \
  "v_fma_f32 v237, v237, v233, v234\n" \
  "v_fma_f32 v238, v238, v233, v234\n" \
  "v_fma_f32 v239, v239, v233, v234\n" \
  "v_cvt_pk_bf16_f32 v36, v236, v237\n" \
  "v_cvt_pk_bf16_f32 v37, v238, v239\n" \
  "s_waitcnt vmcnt(12) lgkmcnt(0)\n" \
  "s_barrier\n" \
  "v_mfma_f32_32x32x16_bf16 v[96:111], a[0:3], v[32:35], v[96:111]\n" \
  "s_add_u32 m0, s67, 114688\n" \
  "ds_read_b64_tr_b16 a[16:17], v224 offset:36864\n" \
  "ds_read_b64_tr_b16 a[18:19], v224 offset:37376\n" \
  "ds_read_b64_tr_b16 a[20:21], v224 offset:37888\n" \
  "global_load_lds_dwordx4 v228, s[40:41] offset:0\n" \
  "v_lshlrev_b32_e32 v236, 16, v38\n" \
  "v_and_b32_e32 v237, s78, v38\n" \
  "v_lshlrev_b32_e32 v238, 16, v39\n" \
  "v_mfma_f32_32x32x16_bf16 v[112:127], a[4:7], v[32:35], v[112:127]\n" \
  "ds_read_b64_tr_b16 a[22:23], v224 offset:38400\n" \
  "ds_read_b64_tr_b16 a[24:25], v225 offset:36864\n" \
  "ds_read_b64_tr_b16 a[26:27], v225 offset:37376\n" \
  "v_and_b32_e32 v239, s78, v39\n" \
  "v_fma_f32 v236, v236, v233, v234\n" \
  "v_fma_f32 v237, v237, v233, v234\n" \
  "v_fma_f32 v238, v238, v233, v234\n" \
  "v_fma_f32 v239, v239, v233, v234\n" \
  "v_mfma_f32_32x32x16_bf16 v[128:143], a[8:11], v[32:35], v[128:143]\n" \
  "ds_read_b64_tr_b16 a[28:29], v225 offset:37888\n" \
  "ds_read_b64_tr_b16 a[30:31], v225 offset:38400\n" \
  "v_cvt_pk_bf16_f32 v38, v236, v237\n" \
  "v_cvt_pk_bf16_f32 v39, v238, v239\n" \
  "v_lshlrev_b32_e32 v236, 16, v40\n" \
  "v_and_b32_e32 v237, s78, v40\n" \
  "v_lshlrev_b32_e32 v238, 16, v41\n" \
  "v_and_b32_e32 v239, s78, v41\n" \
  "v_mfma_f32_32x32x16_bf16 v[144:159], a[12:15], v[32:35], v[144:159]\n" \
  "v_fma_f32 v236, v236, v233, v234\n" \
  "v_fma_f32 v237, v237, v233, v234\n" \
  "v_fma_f32 v238, v238, v233, v234\n" \
  "v_fma_f32 v239, v239, v233, v234\n" \
  "v_cvt_pk_bf16_f32 v40, v236, v237\n" \
  "v_cvt_pk_bf16_f32 v41, v238, v239\n" \
  "s_waitcnt lgkmcnt(0)\n" \
  "v_mfma_f32_32x32x16_bf16 v[96:111], a[16:19], v[36:39], v[96:111]\n" \
  "ds_read_b64_tr_b16 a[0:1], v224 offset:40960\n" \
  "ds_read_b64_tr_b16 a[2:3], v224 offset:41472\n" \
  "ds_read_b64_tr_b16 a[4:5], v224 offset:41984\n" \
  "global_load_lds_dwordx4 v228, s[42:43] offset:1024\n" \
  "v_lshlrev_b32_e32 v236, 16, v42\n" \
  "v_and_b32_e32 v237, s78, v42\n" \
  "v_lshlrev_b32_e32 v238, 16, v43\n" \
  "v_and_b32_e32 v239, s78, v43\n" \
  "v_mfma_f32_32x32x16_bf16 v[112:127], a[20:23], v[36:39], v[112:127]\n" \
  "ds_read_b64_tr_b16 a[6:7], v224 offset:42496\n" \
  "ds_read_b64_tr_b16 a[8:9], v225 offset:40960\n" \
  "ds_read_b64_tr_b16 a[10:11], v225 offset:41472\n" \
  "v_fma_f32 v236, v236, v233, v234\n" \
  "v_fma_f32 v237, v237, v233, v234\n" \
  "v_fma_f32 v238, v238, v233, v234\n" \
  "v_fma_f32 v239, v239, v233, v234\n" \
  "v_cvt_pk_bf16_f32 v42, v236, v237\n" \
  "v_mfma_f32_32x32x16_bf16 v[128:143], a[24:27], v[36:39], v[128:143]\n" \
  "ds_read_b64_tr_b16 a[12:13], v225 offset:41984\n" \
  "ds_read_b64_tr_b16 a[14:15], v225 offset:42496\n" \
  "v_cvt_pk_bf16_f32 v43, v238, v239\n" \
  "v_lshlrev_b32_e32 v236, 16, v44\n" \
  "v_and_b32_e32 v237, s78, v44\n" \
  "v_lshlrev_b32_e32 v238, 16, v45\n" \
  "v_and_b32_e32 v239, s78, v45\n" \
  "v_fma_f32 v236, v236, v233, v234\n" \
  "v_mfma_f32_32x32x16_bf16 v[144:159], a[28:31], v[36:39], v[144:159]\n" \
  "v_fma_f32 v237, v237, v233, v234\n" \
  "v_fma_f32 v238, v238, v233, v234\n" \
  "v_fma_f32 v239, v239, v233, v234\n" \
  "v_cvt_pk_bf16_f32 v44, v236, v237\n" \
  "v_cvt_pk_bf16_f32 v45, v238, v239\n" \
  "s_waitcnt lgkmcnt(0)\n" \
  "v_mfma_f32_32x32x16_bf16 v[96:111], a[0:3], v[40:43], v[96:111]\n" \
  "ds_read_b64_tr_b16 a[16:17], v224 offset:45056\n" \
  "ds_read_b64_tr_b16 a[18:19], v224 offset:45568\n" \
  "ds_read_b64_tr_b16 a[20:21], v224 offset:46080\n" \
  "global_load_lds_dwordx4 v228, s[44:45] offset:2048\n" \
  "v_lshlrev_b32_e32 v236, 16, v46\n" \
  "v_and_b32_e32 v237, s78, v46\n" \
  "v_lshlrev_b32_e32 v238, 16, v47\n" \
  "v_and_b32_e32 v239, s78, v47\n" \
  "v_mfma_f32_32x32x16_bf16 v[112:127], a[4:7], v[40:43], v[112:127]\n" \
  "ds_read_b64_tr_b16 a[22:23], v224 offset:46592\n" \
  "ds_read_b64_tr_b16 a[24:25], v225 offset:45056\n" \
  "ds_read_b64_tr_b16 a[26:27], v225 offset:45568\n" \
  "v_fma_f32 v236, v236, v233, v234\n" \
  "v_fma_f32 v237, v237, v233, v234\n" \
  "v_fma_f32 v238, v238, v233, v234\n" \
  "v_fma_f32 v239, v239, v233, v234\n" \
  "v_cvt_pk_bf16_f32 v46, v236, v237\n" \
  "v_mfma_f32_32x32x16_bf16 v[128:143], a[8:11], v[40:43], v[128:143]\n" \
  "ds_read_b64_tr_b16 a[28:29], v225 offset:46080\n" \
  "ds_read_b64_tr_b16 a[30:31], v225 offset:46592\n" \
  "v_cvt_pk_bf16_f32 v47, v238, v239\n" \
  "v_lshlrev_b32_e32 v236, 16, v48\n" \
  "v_and_b32_e32 v237, s78, v48\n" \
  "v_lshlrev_b32_e32 v238, 16, v49\n" \
  "v_and_b32_e32 v239, s78, v49\n" \
  "v_fma_f32 v236, v236, v233, v234\n" \
  "v_mfma_f32_32x32x16_bf16 v[144:159], a[12:15], v[40:43], v[144:159]\n" \
  "v_fma_f32 v237, v237, v233, v234\n" \
  "v_fma_f32 v238, v238, v233, v234\n" \
  "v_fma_f32 v239, v239, v233, v234\n" \
  "v_cvt_pk_bf16_f32 v48, v236, v237\n" \
  "v_cvt_pk_bf16_f32 v49, v238, v239\n" \
  "s_waitcnt lgkmcnt(0)\n" \
  "v_mfma_f32_32x32x16_bf16 v[96:111], a[16:19], v[44:47], v[96:111]\n" \
  "ds_read_b64_tr_b16 a[0:1], v224 offset:49152\n" \
  "ds_read_b64_tr_b16 a[2:3], v224 offset:49664\n" \
  "ds_read_b64_tr_b16 a[4:5], v224 offset:50176\n" \
  "global_load_lds_dwordx4 v228, s[46:47] offset:3072\n" \
  "v_add_u32_e32 v228, s64, v228\n" \
  "v_lshlrev_b32_e32 v236, 16, v50\n" \
  "v_and_b32_e32 v237, s78, v50\n" \
  "v_lshlrev_b32_e32 v238, 16, v51\n" \
  "v_mfma_f32_32x32x16_bf16 v[112:127], a[20:23], v[44:47], v[112:127]\n" \
  "ds_read_b64_tr_b16 a[6:7], v224 offset:50688\n" \
  "ds_read_b64_tr_b16 a[8:9], v225 offset:49152\n" \
  "ds_read_b64_tr_b16 a[10:11], v225 offset:49664\n" \
  "v_and_b32_e32 v239, s78, v51\n" \
  "v_fma_f32 v236, v236, v233, v234\n" \
  "v_fma_f32 v237, v237, v233, v234\n" \
  "v_fma_f32 v238, v238, v233, v234\n" \
  "v_fma_f32 v239, v239, v233, v234\n" \
  "v_mfma_f32_32x32x16_bf16 v[128:143], a[24:27], v[44:47], v[128:143]\n" \
  "ds_read_b64_tr_b16 a[12:13], v225 offset:50176\n" \
  "ds_read_b64_tr_b16 a[14:15], v225 offset:50688\n" \
  "v_cvt_pk_bf16_f32 v50, v236, v237\n" \
  "v_cvt_pk_bf16_f32 v51, v238, v239\n" \
  "v_lshlrev_b32_e32 v236, 16, v52\n" \
  "v_and_b32_e32 v237, s78, v52\n" \
  "v_lshlrev_b32_e32 v238, 16, v53\n" \
  "v_and_b32_e32 v239, s78, v53\n" \
  "v_mfma_f32_32x32x16_bf16 v[144:159], a[28:31], v[44:47], v[144:159]\n" \
  "v_fma_f32 v236, v236, v233, v234\n" \
  "v_fma_f32 v237, v237, v233, v234\n" \
  "v_fma_f32 v238, v238, v233, v234\n" \
  "v_fma_f32 v239, v239, v233, v234\n" \
  "v_cvt_pk_bf16_f32 v52, v236, v237\n" \
  "v_cvt_pk_bf16_f32 v53, v238, v239\n" \
  "s_waitcnt vmcnt(12) lgkmcnt(0)\n" \
  "s_barrier\n" \
  "v_mfma_f32_32x32x16_bf16 v[96:111], a[0:3], v[48:51], v[96:111]\n" \
  "s_add_u32 m0, s67, 0\n" \
  "ds_read_b64_tr_b16 a[16:17], v224 offset:53248\n" \
  "ds_read_b64_tr_b16 a[18:19], v224 offset:53760\n" \
  "ds_read_b64_tr_b16 a[20:21], v224 offset:54272\n" \
  "global_load_lds_dwordx4 v228, s[40:41] offset:0\n" \
  "v_lshlrev_b32_e32 v236, 16, v54\n" \
  "v_and_b32_e32 v237, s78, v54\n" \
  "v_lshlrev_b32_e32 v238, 16, v55\n" \
  "v_mfma_f32_32x32x16_bf16 v[112:127], a[4:7], v[48:51], v[112:127]\n" \
  "ds_read_b64_tr_b16 a[22:23], v224 offset:54784\n" \
  "ds_read_b64_tr_b16 a[24:25], v225 offset:53248\n" \
  "ds_read_b64_tr_b16 a[26:27], v225 offset:53760\n" \
  "v_and_b32_e32 v239, s78, v55\n" \
  "v_fma_f32 v236, v236, v233, v234\n" \
  "v_fma_f32 v237, v237, v233, v234\n" \
  "v_fma_f32 v238, v238, v233, v234\n" \
  "v_fma_f32 v239, v239, v233, v234\n" \
  "v_mfma_f32_32x32x16_bf16 v[128:143], a[8:11], v[48:51], v[128:143]\n" \
  "ds_read_b64_tr_b16 a[28:29], v225 offset:54272\n" \
  "ds_read_b64_tr_b16 a[30:31], v225 offset:54784\n" \
  "v_cvt_pk_bf16_f32 v54, v236, v237\n" \
  "v_cvt_pk_bf16_f32 v55, v238, v239\n" \
  "v_lshlrev_b32_e32 v236, 16, v56\n" \
  "v_and_b32_e32 v237, s78, v56\n" \
  "v_lshlrev_b32_e32 v238, 16, v57\n" \
  "v_and_b32_e32 v239, s78, v57\n" \
  "v_mfma_f32_32x32x16_bf16 v[144:159], a[12:15], v[48:51], v[144:159]\n" \
  "v_fma_f32 v236, v236, v233, v234\n" \
  "v_fma_f32 v237, v237, v233, v234\n" \
  "v_fma_f32 v238, v238, v233, v234\n" \
  "v_fma_f32 v239, v239, v233, v234\n" \
  "v_cvt_pk_bf16_f32 v56, v236, v237\n" \
  "v_cvt_pk_bf16_f32 v57, v238, v239\n" \
  "s_waitcnt lgkmcnt(0)\n" \
  "v_mfma_f32_32x32x16_bf16 v[96:111], a[16:19], v[52:55], v[96:111]\n" \
  "ds_read_b64_tr_b16 a[0:1], v224 offset:57344\n" \
  "ds_read_b64_tr_b16 a[2:3], v224 offset:57856\n" \
  "ds_read_b64_tr_b16 a[4:5], v224 offset:58368\n" \
  "global_load_lds_dwordx4 v228, s[42:43] offset:1024\n" \
  "v_lshlrev_b32_e32 v236, 16, v58\n" \
  "v_and_b32_e32 v237, s78, v58\n" \
  "v_lshlrev_b32_e32 v238, 16, v59\n" \
  "v_and_b32_e32 v239, s78, v59\n" \
  "v_mfma_f32_32x32x16_bf16 v[112:127], a[20:23], v[52:55], v[112:127]\n" \
  "ds_read_b64_tr_b16 a[6:7], v224 offset:58880\n" \
  "ds_read_b64_tr_b16 a[8:9], v225 offset:57344\n" \
  "ds_read_b64_tr_b16 a[10:11], v225 offset:57856\n" \
  "v_fma_f32 v236, v236, v233, v234\n" \
  "v_fma_f32 v237, v237, v233, v234\n" \
  "v_fma_f32 v238, v238, v233, v234\n" \
  "v_fma_f32 v239, v239, v233, v234\n" \
  "v_cvt_pk_bf16_f32 v58, v236, v237\n" \
  "v_mfma_f32_32x32x16_bf16 v[128:143], a[24:27], v[52:55], v[128:143]\n" \
  "ds_read_b64_tr_b16 a[12:13], v225 offset:58368\n" \
  "ds_read_b64_tr_b16 a[14:15], v225 offset:58880\n" \
  "v_cvt_pk_bf16_f32 v59, v238, v239\n" \
  "v_lshlrev_b32_e32 v236, 16, v60\n" \
  "v_and_b32_e32 v237, s78, v60\n" \
  "v_lshlrev_b32_e32 v238, 16, v61\n" \
  "v_and_b32_e32 v239, s78, v61\n" \
  "v_fma_f32 v236, v236, v233, v234\n" \
  "v_mfma_f32_32x32x16_bf16 v[144:159], a[28:31], v[52:55], v[144:159]\n" \
  "v_fma_f32 v237, v237, v233, v234\n" \
  "v_fma_f32 v238, v238, v233, v234\n" \
  "v_fma_f32 v239, v239, v233, v234\n" \
  "v_cvt_pk_bf16_f32 v60, v236, v237\n" \
  "v_cvt_pk_bf16_f32 v61, v238, v239\n" \
  "s_waitcnt lgkmcnt(0)\n" \
  "v_mfma_f32_32x32x16_bf16 v[96:111], a[0:3], v[56:59], v[96:111]\n" \
  "ds_read_b64_tr_b16 a[16:17], v224 offset:61440\n" \
  "ds_read_b64_tr_b16 a[18:19], v224 offset:61952\n" \
  "ds_read_b64_tr_b16 a[20:21], v224 offset:62464\n" \
  "global_load_lds_dwordx4 v228, s[44:45] offset:2048\n" \
  "v_lshlrev_b32_e32 v236, 16, v62\n" \
  "v_and_b32_e32 v237, s78, v62\n" \
  "v_lshlrev_b32_e32 v238, 16, v63\n" \
  "v_and_b32_e32 v239, s78, v63\n" \
  "v_mfma_f32_32x32x16_bf16 v[112:127], a[4:7], v[56:59], v[112:127]\n" \
  "ds_read_b64_tr_b16 a[22:23], v224 offset:62976\n" \
  "ds_read_b64_tr_b16 a[24:25], v225 offset:61440\n" \
  "ds_read_b64_tr_b16 a[26:27], v225 offset:61952\n" \
  "v_fma_f32 v236, v236, v233, v234\n" \
  "v_fma_f32 v237, v237, v233, v234\n" \
  "v_fma_f32 v238, v238, v233, v234\n" \
  "v_fma_f32 v239, v239, v233, v234\n" \
  "v_cvt_pk_bf16_f32 v62, v236, v237\n" \
  "v_mfma_f32_32x32x16_bf16 v[128:143], a[8:11], v[56:59], v[128:143]\n" \
  "ds_read_b64_tr_b16 a[28:29], v225 offset:62464\n" \
  "ds_read_b64_tr_b16 a[30:31], v225 offset:62976\n" \
  "v_cvt_pk_bf16_f32 v63, v238, v239\n" \
  "v_lshlrev_b32_e32 v236, 16, v64\n" \
  "v_and_b32_e32 v237, s78, v64\n" \
  "v_lshlrev_b32_e32 v238, 16, v65\n" \
  "v_and_b32_e32 v239, s78, v65\n" \
  "v_fma_f32 v236, v236, v233, v234\n" \
  "v_mfma_f32_32x32x16_bf16 v[144:159], a[12:15], v[56:59], v[144:159]\n" \
  "v_fma_f32 v237, v237, v233, v234\n" \
  "v_fma_f32 v238, v238, v233, v234\n" \
  "v_fma_f32 v239, v239, v233, v234\n" \
  "v_cvt_pk_bf16_f32 v64, v236, v237\n" \
  "v_cvt_pk_bf16_f32 v65, v238, v239\n" \
  "s_waitcnt lgkmcnt(0)\n" \
  "v_mfma_f32_32x32x16_bf16 v[96:111], a[16:19], v[60:63], v[96:111]\n" \
  "ds_read_b64_tr_b16 a[0:1], v226 offset:0\n" \
  "ds_read_b64_tr_b16 a[2:3], v226 offset:512\n" \
  "ds_read_b64_tr_b16 a[4:5], v226 offset:1024\n" \
  "global_load_lds_dwordx4 v228, s[46:47] offset:3072\n" \
  "v_add_u32_e32 v228, s64, v228\n" \
  "v_lshlrev_b32_e32 v236, 16, v66\n" \
  "v_and_b32_e32 v237, s78, v66\n" \
  "v_lshlrev_b32_e32 v238, 16, v67\n" \
  "v_mfma_f32_32x32x16_bf16 v[112:127], a[20:23], v[60:63], v[112:127]\n" \
  "ds_read_b64_tr_b16 a[6:7], v226 offset:1536\n" \
  "ds_read_b64_tr_b16 a[8:9], v227 offset:0\n" \
  "ds_read_b64_tr_b16 a[10:11], v227 offset:512\n" \
  "v_and_b32_e32 v239, s78, v67\n" \
  "v_fma_f32 v236, v236, v233, v234\n" \
  "v_fma_f32 v237, v237, v233, v234\n" \
  "v_fma_f32 v238, v238, v233, v234\n" \
  "v_fma_f32 v239, v239, v233, v234\n" \
  "v_mfma_f32_32x32x16_bf16 v[128:143], a[24:27], v[60:63], v[128:143]\n" \
  "ds_read_b64_tr_b16 a[12:13], v227 offset:1024\n" \
  "ds_read_b64_tr_b16 a[14:15], v227 offset:1536\n" \
  "v_cvt_pk_bf16_f32 v66, v236, v237\n" \
  "v_cvt_pk_bf16_f32 v67, v238, v239\n" \
  "v_lshlrev_b32_e32 v236, 16, v68\n" \
  "v_and_b32_e32 v237, s78, v68\n" \
  "v_lshlrev_b32_e32 v238, 16, v69\n" \
  "v_and_b32_e32 v239, s78, v69\n" \
  "v_mfma_f32_32x32x16_bf16 v[144:159], a[28:31], v[60:63], v[144:159]\n" \
  "v_fma_f32 v236, v236, v233, v234\n" \
  "v_fma_f32 v237, v237, v233, v234\n" \
  "v_fma_f32 v238, v238, v233, v234\n" \
  "v_fma_f32 v239, v239, v233, v234\n" \
  "v_cvt_pk_bf16_f32 v68, v236, v237\n" \
  "v_cvt_pk_bf16_f32 v69, v238, v239\n" \
  "s_waitcnt vmcnt(12) lgkmcnt(0)\n" \
  "s_barrier\n" \
  "v_mfma_f32_32x32x16_bf16 v[96:111], a[0:3], v[64:67], v[96:111]\n" \
  "s_add_u32 m0, s67, 16384\n" \
  "ds_read_b64_tr_b16 a[16:17], v226 offset:4096\n" \
  "ds_read_b64_tr_b16 a[18:19], v226 offset:4608\n" \
  "ds_read_b64_tr_b16 a[20:21], v226 offset:5120\n" \
  "global_load_lds_dwordx4 v228, s[40:41] offset:0\n" \
  "v_lshlrev_b32_e32 v236, 16, v70\n" \
  "v_and_b32_e32 v237, s78, v70\n" \
  "v_lshlrev_b32_e32 v238, 16, v71\n" \
  "v_mfma_f32_32x32x16_bf16 v[112:127], a[4:7], v[64:67], v[112:127]\n" \
  "ds_read_b64_tr_b16 a[22:23], v226 offset:5632\n" \
  "ds_read_b64_tr_b16 a[24:25], v227 offset:4096\n" \
  "ds_read_b64_tr_b16 a[26:27], v227 offset:4608\n" \
  "v_and_b32_e32 v239, s78, v71\n" \
  "v_fma_f32 v236, v236, v233, v234\n" \
  "v_fma_f32 v237, v237, v233, v234\n" \
  "v_fma_f32 v238, v238, v233, v234\n" \
  "v_fma_f32 v239, v239, v233, v234\n" \
  "v_mfma_f32_32x32x16_bf16 v[128:143], a[8:11], v[64:67], v[128:143]\n" \
  "ds_read_b64_tr_b16 a[28:29], v227 offset:5120\n" \
  "ds_read_b64_tr_b16 a[30:31], v227 offset:5632\n" \
  "v_cvt_pk_bf16_f32 v70, v236, v237\n" \
  "v_cvt_pk_bf16_f32 v71, v238, v239\n" \
  "v_lshlrev_b32_e32 v236, 16, v72\n" \
  "v_and_b32_e32 v237, s78, v72\n" \
  "v_lshlrev_b32_e32 v238, 16, v73\n" \
  "v_and_b32_e32 v239, s78, v73\n" \
  "v_mfma_f32_32x32x16_bf16 v[144:159], a[12:15], v[64:67], v[144:159]\n" \
  "v_fma_f32 v236, v236, v233, v234\n" \
  "v_fma_f32 v237, v237, v233, v234\n" \
  "v_fma_f32 v238, v238, v233, v234\n" \
  "v_fma_f32 v239, v239, v233, v234\n" \
  "v_cvt_pk_bf16_f32 v72, v236, v237\n" \
  "v_cvt_pk_bf16_f32 v73, v238, v239\n" \
  "s_waitcnt lgkmcnt(0)\n" \
  "v_mfma_f32_32x32x16_bf16 v[96:111], a[16:19], v[68:71], v[96:111]\n" \
  "ds_read_b64_tr_b16 a[0:1], v226 offset:8192\n" \
  "ds_read_b64_tr_b16 a[2:3], v226 offset:8704\n" \
  "ds_read_b64_tr_b16 a[4:5], v226 offset:9216\n" \
  "global_load_lds_dwordx4 v228, s[42:43] offset:1024\n" \
  "v_lshlrev_b32_e32 v236, 16, v74\n" \
  "v_and_b32_e32 v237, s78, v74\n" \
  "v_lshlrev_b32_e32 v238, 16, v75\n" \
  "v_and_b32_e32 v239, s78, v75\n" \
  "v_mfma_f32_32x32x16_bf16 v[112:127], a[20:23], v[68:71], v[112:127]\n" \
  "ds_read_b64_tr_b16 a[6:7], v226 offset:9728\n" \
  "ds_read_b64_tr_b16 a[8:9], v227 offset:8192\n" \
  "ds_read_b64_tr_b16 a[10:11], v227 offset:8704\n" \
  "v_fma_f32 v236, v236, v233, v234\n" \
  "v_fma_f32 v237, v237, v233, v234\n" \
  "v_fma_f32 v238, v238, v233, v234\n" \
  "v_fma_f32 v239, v239, v233, v234\n" \
  "v_cvt_pk_bf16_f32 v74, v236, v237\n" \
  "v_mfma_f32_32x32x16_bf16 v[128:143], a[24:27], v[68:71], v[128:143]\n" \
  "ds_read_b64_tr_b16 a[12:13], v227 offset:9216\n" \
  "ds_read_b64_tr_b16 a[14:15], v227 offset:9728\n" \
  "v_cvt_pk_bf16_f32 v75, v238, v239\n" \
  "v_lshlrev_b32_e32 v236, 16, v76\n" \
  "v_and_b32_e32 v237, s78, v76\n" \
  "v_lshlrev_b32_e32 v238, 16, v77\n" \
  "v_and_b32_e32 v239, s78, v77\n" \
  "v_fma_f32 v236, v236, v233, v234\n" \
  "v_mfma_f32_32x32x16_bf16 v[144:159], a[28:31], v[68:71], v[144:159]\n" \
  "v_fma_f32 v237, v237, v233, v234\n" \
  "v_fma_f32 v238, v238, v233, v234\n" \
  "v_fma_f32 v239, v239, v233, v234\n" \
  "v_cvt_pk_bf16_f32 v76, v236, v237\n" \
  "v_cvt_pk_bf16_f32 v77, v238, v239\n" \
  "s_waitcnt lgkmcnt(0)\n" \
  "v_mfma_f32_32x32x16_bf16 v[96:111], a[0:3], v[72:75], v[96:111]\n" \
  "ds_read_b64_tr_b16 a[16:17], v226 offset:12288\n" \
  "ds_read_b64_tr_b16 a[18:19], v226 offset:12800\n" \
  "ds_read_b64_tr_b16 a[20:21], v226 offset:13312\n" \
  "global_load_lds_dwordx4 v228, s[44:45] offset:2048\n" \
  "v_lshlrev_b32_e32 v236, 16, v78\n" \
  "v_and_b32_e32 v237, s78, v78\n" \
  "v_lshlrev_b32_e32 v238, 16, v79\n" \
  "v_and_b32_e32 v239, s78, v79\n" \
  "v_mfma_f32_32x32x16_bf16 v[112:127], a[4:7], v[72:75], v[112:127]\n" \
  "ds_read_b64_tr_b16 a[22:23], v226 offset:13824\n" \
  "ds_read_b64_tr_b16 a[24:25], v227 offset:12288\n" \
  "ds_read_b64_tr_b16 a[26:27], v227 offset:12800\n" \
  "v_fma_f32 v236, v236, v233, v234\n" \
  "v_fma_f32 v237, v237, v233, v234\n" \
  "v_fma_f32 v238, v238, v233, v234\n" \
  "v_fma_f32 v239, v239, v233, v234\n" \
  "v_cvt_pk_bf16_f32 v78, v236, v237\n" \
  "v_mfma_f32_32x32x16_bf16 v[128:143], a[8:11], v[72:75], v[128:143]\n" \
  "ds_read_b64_tr_b16 a[28:29], v227 offset:13312\n" \
  "ds_read_b64_tr_b16 a[30:31], v227 offset:13824\n" \
  "v_cvt_pk_bf16_f32 v79, v238, v239\n" \
  "v_lshlrev_b32_e32 v236, 16, v80\n" \
  "v_and_b32_e32 v237, s78, v80\n" \
  "v_lshlrev_b32_e32 v238, 16, v81\n" \
  "v_and_b32_e32 v239, s78, v81\n" \
  "v_fma_f32 v236, v236, v233, v234\n" \
  "v_mfma_f32_32x32x16_bf16 v[144:159], a[12:15], v[72:75], v[144:159]\n" \
  "v_fma_f32 v237, v237, v233, v234\n" \
  "v_fma_f32 v238, v238, v233, v234\n" \
  "v_fma_f32 v239, v239, v233, v234\n" \
  "v_cvt_pk_bf16_f32 v80, v236, v237\n" \
  "v_cvt_pk_bf16_f32 v81, v238, v239\n" \
  "s_waitcnt lgkmcnt(0)\n" \
  "v_mfma_f32_32x32x16_bf16 v[96:111], a[16:19], v[76:79], v[96:111]\n" \
  "ds_read_b64_tr_b16 a[0:1], v226 offset:16384\n" \
  "ds_read_b64_tr_b16 a[2:3], v226 offset:16896\n" \
  "ds_read_b64_tr_b16 a[4:5], v226 offset:17408\n" \
  "global_load_lds_dwordx4 v228, s[46:47] offset:3072\n" \
  "v_add_u32_e32 v228, s64, v228\n" \
  "v_lshlrev_b32_e32 v236, 16, v82\n" \
  "v_and_b32_e32 v237, s78, v82\n" \
  "v_lshlrev_b32_e32 v238, 16, v83\n" \
  "v_mfma_f32_32x32x16_bf16 v[112:127], a[20:23], v[76:79], v[112:127]\n" \
  "ds_read_b64_tr_b16 a[6:7], v226 offset:17920\n" \
  "ds_read_b64_tr_b16 a[8:9], v227 offset:16384\n" \
  "ds_read_b64_tr_b16 a[10:11], v227 offset:16896\n" \
  "v_and_b32_e32 v239, s78, v83\n" \
  "v_fma_f32 v236, v236, v233, v234\n" \
  "v_fma_f32 v237, v237, v233, v234\n" \
  "v_fma_f32 v238, v238, v233, v234\n" \
  "v_fma_f32 v239, v239, v233, v234\n" \
  "v_mfma_f32_32x32x16_bf16 v[128:143], a[24:27], v[76:79], v[128:143]\n" \
  "ds_read_b64_tr_b16 a[12:13], v227 offset:17408\n" \
  "ds_read_b64_tr_b16 a[14:15], v227 offset:17920\n" \
  "v_cvt_pk_bf16_f32 v82, v236, v237\n" \
  "v_cvt_pk_bf16_f32 v83, v238, v239\n" \
  "v_lshlrev_b32_e32 v236, 16, v84\n" \
  "v_and_b32_e32 v237, s78, v84\n" \
  "v_lshlrev_b32_e32 v238, 16, v85\n" \
  "v_and_b32_e32 v239, s78, v85\n" \
  "v_mfma_f32_32x32x16_bf16 v[144:159], a[28:31], v[76:79], v[144:159]\n" \
  "v_fma_f32 v236, v236, v233, v234\n" \
  "v_fma_f32 v237, v237, v233, v234\n" \
  "v_fma_f32 v238, v238, v233, v234\n" \
  "v_fma_f32 v239, v239, v233, v234\n" \
  "v_cvt_pk_bf16_f32 v84, v236, v237\n" \
  "v_cvt_pk_bf16_f32 v85, v238, v239\n" \
  "s_waitcnt vmcnt(12) lgkmcnt(0)\n" \
  "s_barrier\n" \
  "v_mfma_f32_32x32x16_bf16 v[96:111], a[0:3], v[80:83], v[96:111]\n" \
  "s_add_u32 m0, s67, 32768\n" \
  "ds_read_b64_tr_b16 a[16:17], v226 offset:20480\n" \
  "ds_read_b64_tr_b16 a[18:19], v226 offset:20992\n" \
  "ds_read_b64_tr_b16 a[20:21], v226 offset:21504\n" \
  "global_load_lds_dwordx4 v229, s[48:49] offset:0\n" \
  "v_lshlrev_b32_e32 v236, 16, v86\n" \
  "v_and_b32_e32 v237, s78, v86\n" \
  "v_lshlrev_b32_e32 v238, 16, v87\n" \
  "v_mfma_f32_32x32x16_bf16 v[112:127], a[4:7], v[80:83], v[112:127]\n" \
  "ds_read_b64_tr_b16 a[22:23], v226 offset:22016\n" \
  "ds_read_b64_tr_b16 a[24:25], v227 offset:20480\n" \
  "ds_read_b64_tr_b16 a[26:27], v227 offset:20992\n" \
  "v_and_b32_e32 v239, s78, v87\n" \
  "v_fma_f32 v236, v236, v233, v234\n" \
  "v_fma_f32 v237, v237, v233, v234\n" \
  "v_fma_f32 v238, v238, v233, v234\n" \
  "v_fma_f32 v239, v239, v233, v234\n" \
  "v_mfma_f32_32x32x16_bf16 v[128:143], a[8:11], v[80:83], v[128:143]\n" \
  "ds_read_b64_tr_b16 a[28:29], v227 offset:21504\n" \
  "ds_read_b64_tr_b16 a[30:31], v227 offset:22016\n" \
  "v_cvt_pk_bf16_f32 v86, v236, v237\n" \
  "v_cvt_pk_bf16_f32 v87, v238, v239\n" \
  "v_lshlrev_b32_e32 v236, 16, v88\n" \
  "v_and_b32_e32 v237, s78, v88\n" \
  "v_lshlrev_b32_e32 v238, 16, v89\n" \
  "v_and_b32_e32 v239, s78, v89\n" \
  "v_mfma_f32_32x32x16_bf16 v[144:159], a[12:15], v[80:83], v[144:159]\n" \
  "v_fma_f32 v236, v236, v233, v234\n" \
  "v_fma_f32 v237, v237, v233, v234\n" \
  "v_fma_f32 v238, v238, v233, v234\n" \
  "v_fma_f32 v239, v239, v233, v234\n" \
  "v_cvt_pk_bf16_f32 v88, v236, v237\n" \
  "v_cvt_pk_bf16_f32 v89, v238, v239\n" \
  "s_waitcnt lgkmcnt(0)\n" \
  "v_mfma_f32_32x32x16_bf16 v[96:111], a[16:19], v[84:87], v[96:111]\n" \
  "ds_read_b64_tr_b16 a[0:1], v226 offset:24576\n" \
  "ds_read_b64_tr_b16 a[2:3], v226 offset:25088\n" \
  "ds_read_b64_tr_b16 a[4:5], v226 offset:25600\n" \
  "global_load_lds_dwordx4 v229, s[50:51] offset:1024\n" \
  "v_lshlrev_b32_e32 v236, 16, v90\n" \
  "v_and_b32_e32 v237, s78, v90\n" \
  "v_lshlrev_b32_e32 v238, 16, v91\n" \
  "v_and_b32_e32 v239, s78, v91\n" \
  "v_mfma_f32_32x32x16_bf16 v[112:127], a[20:23], v[84:87], v[112:127]\n" \
  "ds_read_b64_tr_b16 a[6:7], v226 offset:26112\n" \
  "ds_read_b64_tr_b16 a[8:9], v227 offset:24576\n" \
  "ds_read_b64_tr_b16 a[10:11], v227 offset:25088\n" \
  "v_fma_f32 v236, v236, v233, v234\n" \
  "v_fma_f32 v237, v237, v233, v234\n" \
  "v_fma_f32 v238, v238, v233, v234\n" \
  "v_fma_f32 v239, v239, v233, v234\n" \
  "v_cvt_pk_bf16_f32 v90, v236, v237\n" \
  "v_mfma_f32_32x32x16_bf16 v[128:143], a[24:27], v[84:87], v[128:143]\n" \
  "ds_read_b64_tr_b16 a[12:13], v227 offset:25600\n" \
  "ds_read_b64_tr_b16 a[14:15], v227 offset:26112\n" \
  "v_cvt_pk_bf16_f32 v91, v238, v239\n" \
  "v_lshlrev_b32_e32 v236, 16, v92\n" \
  "v_and_b32_e32 v237, s78, v92\n" \
  "v_lshlrev_b32_e32 v238, 16, v93\n" \
  "v_and_b32_e32 v239, s78, v93\n" \
  "v_fma_f32 v236, v236, v233, v234\n" \
  "v_mfma_f32_32x32x16_bf16 v[144:159], a[28:31], v[84:87], v[144:159]\n" \
  "v_fma_f32 v237, v237, v233, v234\n" \
  "v_fma_f32 v238, v238, v233, v234\n" \
  "v_fma_f32 v239, v239, v233, v234\n" \
  "v_cvt_pk_bf16_f32 v92, v236, v237\n" \
  "v_cvt_pk_bf16_f32 v93, v238, v239\n" \
  "s_waitcnt lgkmcnt(0)\n" \
  "v_mfma_f32_32x32x16_bf16 v[96:111], a[0:3], v[88:91], v[96:111]\n" \
  "ds_read_b64_tr_b16 a[16:17], v226 offset:28672\n" \
  "ds_read_b64_tr_b16 a[18:19], v226 offset:29184\n" \
  "ds_read_b64_tr_b16 a[20:21], v226 offset:29696\n" \
  "global_load_lds_dwordx4 v229, s[52:53] offset:2048\n" \
  "v_lshlrev_b32_e32 v236, 16, v94\n" \
  "v_and_b32_e32 v237, s78, v94\n" \
  "v_lshlrev_b32_e32 v238, 16, v95\n" \
  "v_and_b32_e32 v239, s78, v95\n" \
  "v_mfma_f32_32x32x16_bf16 v[112:127], a[4:7], v[88:91], v[112:127]\n" \
  "ds_read_b64_tr_b16 a[22:23], v226 offset:30208\n" \
  "ds_read_b64_tr_b16 a[24:25], v227 offset:28672\n" \
  "ds_read_b64_tr_b16 a[26:27], v227 offset:29184\n" \
  "v_fma_f32 v236, v236, v233, v234\n" \
  "v_fma_f32 v237, v237, v233, v234\n" \
  "v_fma_f32 v238, v238, v233, v234\n" \
  "v_fma_f32 v239, v239, v233, v234\n" \
  "v_cvt_pk_bf16_f32 v94, v236, v237\n" \
  "v_mfma_f32_32x32x16_bf16 v[128:143], a[8:11], v[88:91], v[128:143]\n" \
  "ds_read_b64_tr_b16 a[28:29], v227 offset:29696\n" \
  "ds_read_b64_tr_b16 a[30:31], v227 offset:30208\n" \
  "v_cvt_pk_bf16_f32 v95, v238, v239\n" \
  "v_lshlrev_b32_e32 v236, 16, v160\n" \
  "v_and_b32_e32 v237, s78, v160\n" \
  "v_lshlrev_b32_e32 v238, 16, v161\n" \
  "v_and_b32_e32 v239, s78, v161\n" \
  "v_fma_f32 v236, v236, v233, v234\n" \
  "v_mfma_f32_32x32x16_bf16 v[144:159], a[12:15], v[88:91], v[144:159]\n" \
  "v_fma_f32 v237, v237, v233, v234\n" \
  "v_fma_f32 v238, v238, v233, v234\n" \
  "v_fma_f32 v239, v239, v233, v234\n" \
  "v_cvt_pk_bf16_f32 v160, v236, v237\n" \
  "v_cvt_pk_bf16_f32 v161, v238, v239\n" \
  "v_accvgpr_write_b32 a32, v160\n" \
  "v_accvgpr_write_b32 a33, v161\n" \
  "s_waitcnt lgkmcnt(0)\n" \
  "v_mfma_f32_32x32x16_bf16 v[96:111], a[16:19], v[92:95], v[96:111]\n" \
  "ds_read_b64_tr_b16 a[0:1], v226 offset:32768\n" \
  "ds_read_b64_tr_b16 a[2:3], v226 offset:33280\n" \
  "ds_read_b64_tr_b16 a[4:5], v226 offset:33792\n" \
  "global_load_lds_dwordx4 v229, s[54:55] offset:3072\n" \
  "v_add_u32_e32 v229, s65, v229\n" \
  "v_lshlrev_b32_e32 v236, 16, v162\n" \
  "v_and_b32_e32 v237, s78, v162\n" \
  "v_lshlrev_b32_e32 v238, 16, v163\n" \
  "v_and_b32_e32 v239, s78, v163\n" \
  "v_mfma_f32_32x32x16_bf16 v[112:127], a[20:23], v[92:95], v[112:127]\n" \
  "ds_read_b64_tr_b16 a[6:7], v226 offset:34304\n" \
  "ds_read_b64_tr_b16 a[8:9], v227 offset:32768\n" \
  "ds_read_b64_tr_b16 a[10:11], v227 offset:33280\n" \
  "v_fma_f32 v236, v236, v233, v234\n" \
  "v_fma_f32 v237, v237, v233, v234\n" \
  "v_fma_f32 v238, v238, v233, v234\n" \
  "v_fma_f32 v239, v239, v233, v234\n" \
  "v_cvt_pk_bf16_f32 v162, v236, v237\n" \
  "v_cvt_pk_bf16_f32 v163, v238, v239\n" \
  "v_mfma_f32_32x32x16_bf16 v[128:143], a[24:27], v[92:95], v[128:143]\n" \
  "ds_read_b64_tr_b16 a[12:13], v227 offset:33792\n" \
  "ds_read_b64_tr_b16 a[14:15], v227 offset:34304\n" \
  "v_accvgpr_write_b32 a34, v162\n" \
  "v_accvgpr_write_b32 a35, v163\n" \
  "v_lshlrev_b32_e32 v236, 16, v164\n" \
  "v_and_b32_e32 v237, s78, v164\n" \
  "v_lshlrev_b32_e32 v238, 16, v165\n" \
  "v_and_b32_e32 v239, s78, v165\n" \
  "v_fma_f32 v236, v236, v233, v234\n" \
  "v_mfma_f32_32x32x16_bf16 v[144:159], a[28:31], v[92:95], v[144:159]\n" \
  "v_fma_f32 v237, v237, v233, v234\n" \
  "v_fma_f32 v238, v238, v233, v234\n" \
  "v_fma_f32 v239, v239, v233, v234\n" \
  "v_cvt_pk_bf16_f32 v164, v236, v237\n" \
  "v_cvt_pk_bf16_f32 v165, v238, v239\n" \
  "v_accvgpr_write_b32 a36, v164\n" \
  "v_accvgpr_write_b32 a37, v165\n" \
  "s_waitcnt vmcnt(12) lgkmcnt(0)\n" \
  "s_barrier\n" \
  "v_mfma_f32_32x32x16_bf16 v[96:111], a[0:3], a[32:35], v[96:111]\n" \
  "s_add_u32 m0, s67, 49152\n" \
  "ds_read_b64_tr_b16 a[16:17], v226 offset:36864\n" \
  "ds_read_b64_tr_b16 a[18:19], v226 offset:37376\n" \
  "ds_read_b64_tr_b16 a[20:21], v226 offset:37888\n" \
  "global_load_lds_dwordx4 v229, s[48:49] offset:0\n" \
  "v_lshlrev_b32_e32 v236, 16, v166\n" \
  "v_and_b32_e32 v237, s78, v166\n" \
  "v_lshlrev_b32_e32 v238, 16, v167\n" \
  "v_and_b32_e32 v239, s78, v167\n" \
  "v_mfma_f32_32x32x16_bf16 v[112:127], a[4:7], a[32:35], v[112:127]\n" \
  "ds_read_b64_tr_b16 a[22:23], v226 offset:38400\n" \
  "ds_read_b64_tr_b16 a[24:25], v227 offset:36864\n" \
  "ds_read_b64_tr_b16 a[26:27], v227 offset:37376\n" \
  "v_fma_f32 v236, v236, v233, v234\n" \
  "v_fma_f32 v237, v237, v233, v234\n" \
  "v_fma_f32 v238, v238, v233, v234\n" \
  "v_fma_f32 v239, v239, v233, v234\n" \
  "v_cvt_pk_bf16_f32 v166, v236, v237\n" \
  "v_cvt_pk_bf16_f32 v167, v238, v239\n" \
  "v_mfma_f32_32x32x16_bf16 v[128:143], a[8:11], a[32:35], v[128:143]\n" \
  "ds_read_b64_tr_b16 a[28:29], v227 offset:37888\n" \
  "ds_read_b64_tr_b16 a[30:31], v227 offset:38400\n" \
  "v_accvgpr_write_b32 a38, v166\n" \
  "v_accvgpr_write_b32 a39, v167\n" \
  "v_lshlrev_b32_e32 v236, 16, v168\n" \
  "v_and_b32_e32 v237, s78, v168\n" \
  "v_lshlrev_b32_e32 v238, 16, v169\n" \
  "v_and_b32_e32 v239, s78, v169\n" \
  "v_fma_f32 v236, v236, v233, v234\n" \
  "v_mfma_f32_32x32x16_bf16 v[144:159], a[12:15], a[32:35], v[144:159]\n" \
  "v_fma_f32 v237, v237, v233, v234\n" \
  "v_fma_f32 v238, v238, v233, v234\n" \
  "v_fma_f32 v239, v239, v233, v234\n" \
  "v_cvt_pk_bf16_f32 v168, v236, v237\n" \
  "v_cvt_pk_bf16_f32 v169, v238, v239\n" \
  "v_accvgpr_write_b32 a40, v168\n" \
  "v_accvgpr_write_b32 a41, v169\n" \
  "s_waitcnt lgkmcnt(0)\n" \
  "v_mfma_f32_32x32x16_bf16 v[96:111], a[16:19], a[36:39], v[96:111]\n" \
  "ds_read_b64_tr_b16 a[0:1], v226 offset:40960\n" \
  "ds_read_b64_tr_b16 a[2:3], v226 offset:41472\n" \
  "ds_read_b64_tr_b16 a[4:5], v226 offset:41984\n" \
  "global_load_lds_dwordx4 v229, s[50:51] offset:1024\n" \
  "v_lshlrev_b32_e32 v236, 16, v170\n" \
  "v_and_b32_e32 v237, s78, v170\n" \
  "v_lshlrev_b32_e32 v238, 16, v171\n" \
  "v_and_b32_e32 v239, s78, v171\n" \
  "v_fma_f32 v236, v236, v233, v234\n" \
  "v_mfma_f32_32x32x16_bf16 v[112:127], a[20:23], a[36:39], v[112:127]\n" \
  "ds_read_b64_tr_b16 a[6:7], v226 offset:42496\n" \
  "ds_read_b64_tr_b16 a[8:9], v227 offset:40960\n" \
  "ds_read_b64_tr_b16 a[10:11], v227 offset:41472\n" \
  "v_fma_f32 v237, v237, v233, v234\n" \
  "v_fma_f32 v238, v238, v233, v234\n" \
  "v_fma_f32 v239, v239, v233, v234\n" \
  "v_cvt_pk_bf16_f32 v170, v236, v237\n" \
  "v_cvt_pk_bf16_f32 v171, v238, v239\n" \
  "v_accvgpr_write_b32 a42, v170\n" \
  "v_mfma_f32_32x32x16_bf16 v[128:143], a[24:27], a[36:39], v[128:143]\n" \
  "ds_read_b64_tr_b16 a[12:13], v227 offset:41984\n" \
  "ds_read_b64_tr_b16 a[14:15], v227 offset:42496\n" \
  "v_accvgpr_write_b32 a43, v171\n" \
  "v_lshlrev_b32_e32 v236, 16, v172\n" \
  "v_and_b32_e32 v237, s78, v172\n" \
  "v_lshlrev_b32_e32 v238, 16, v173\n" \
  "v_and_b32_e32 v239, s78, v173\n" \
  "v_fma_f32 v236, v236, v233, v234\n" \
  "v_fma_f32 v237, v237, v233, v234\n" \
  "v_mfma_f32_32x32x16_bf16 v[144:159], a[28:31], a[36:39], v[144:159]\n" \
  "v_fma_f32 v238, v238, v233, v234\n" \
  "v_fma_f32 v239, v239, v233, v234\n" \
  "v_cvt_pk_bf16_f32 v172, v236, v237\n" \
  "v_cvt_pk_bf16_f32 v173, v238, v239\n" \
  "v_accvgpr_write_b32 a44, v172\n" \
  "v_accvgpr_write_b32 a45, v173\n" \
  "s_waitcnt lgkmcnt(0)\n" \
  "v_mfma_f32_32x32x16_bf16 v[96:111], a[0:3], a[40:43], v[96:111]\n" \
  "ds_read_b64_tr_b16 a[16:17], v226 offset:45056\n" \
  "ds_read_b64_tr_b16 a[18:19], v226 offset:45568\n" \
  "ds_read_b64_tr_b16 a[20:21], v226 offset:46080\n" \
  "global_load_lds_dwordx4 v229, s[52:53] offset:2048\n" \
  "v_lshlrev_b32_e32 v236, 16, v174\n" \
  "v_and_b32_e32 v237, s78, v174\n" \
  "v_lshlrev_b32_e32 v238, 16, v175\n" \
  "v_and_b32_e32 v239, s78, v175\n" \
  "v_fma_f32 v236, v236, v233, v234\n" \
  "v_mfma_f32_32x32x16_bf16 v[112:127], a[4:7], a[40:43], v[112:127]\n" \
  "ds_read_b64_tr_b16 a[22:23], v226 offset:46592\n" \
  "ds_read_b64_tr_b16 a[24:25], v227 offset:45056\n" \
  "ds_read_b64_tr_b16 a[26:27], v227 offset:45568\n" \
  "v_fma_f32 v237, v237, v233, v234\n" \
  "v_fma_f32 v238, v238, v233, v234\n" \
  "v_fma_f32 v239, v239, v233, v234\n" \
  "v_cvt_pk_bf16_f32 v174, v236, v237\n" \
  "v_cvt_pk_bf16_f32 v175, v238, v239\n" \
  "v_accvgpr_write_b32 a46, v174\n" \
  "v_mfma_f32_32x32x16_bf16 v[128:143], a[8:11], a[40:43], v[128:143]\n" \
  "ds_read_b64_tr_b16 a[28:29], v227 offset:46080\n" \
  "ds_read_b64_tr_b16 a[30:31], v227 offset:46592\n" \
  "v_accvgpr_write_b32 a47, v175\n" \
  "v_lshlrev_b32_e32 v236, 16, v176\n" \
  "v_and_b32_e32 v237, s78, v176\n" \
  "v_lshlrev_b32_e32 v238, 16, v177\n" \
  "v_and_b32_e32 v239, s78, v177\n" \
  "v_fma_f32 v236, v236, v233, v234\n" \
  "v_fma_f32 v237, v237, v233, v234\n" \
  "v_mfma_f32_32x32x16_bf16 v[144:159], a[12:15], a[40:43], v[144:159]\n" \
  "v_fma_f32 v238, v238, v233, v234\n" \
  "v_fma_f32 v239, v239, v233, v234\n" \
  "v_cvt_pk_bf16_f32 v176, v236, v237\n" \
  "v_cvt_pk_bf16_f32 v177, v238, v239\n" \
  "v_accvgpr_write_b32 a48, v176\n" \
  "v_accvgpr_write_b32 a49, v177\n" \
  "s_waitcnt lgkmcnt(0)\n" \
  "v_mfma_f32_32x32x16_bf16 v[96:111], a[16:19], a[44:47], v[96:111]\n" \
  "ds_read_b64_tr_b16 a[0:1], v226 offset:49152\n" \
  "ds_read_b64_tr_b16 a[2:3], v226 offset:49664\n" \
  "ds_read_b64_tr_b16 a[4:5], v226 offset:50176\n" \
  "global_load_lds_dwordx4 v229, s[54:55] offset:3072\n" \
  "v_add_u32_e32 v229, s65, v229\n" \
  "v_lshlrev_b32_e32 v236, 16, v178\n" \
  "v_and_b32_e32 v237, s78, v178\n" \
  "v_lshlrev_b32_e32 v238, 16, v179\n" \
  "v_and_b32_e32 v239, s78, v179\n" \
  "v_mfma_f32_32x32x16_bf16 v[112:127], a[20:23], a[44:47], v[112:127]\n" \
  "ds_read_b64_tr_b16 a[6:7], v226 offset:50688\n" \
  "ds_read_b64_tr_b16 a[8:9], v227 offset:49152\n" \
  "ds_read_b64_tr_b16 a[10:11], v227 offset:49664\n" \
  "v_fma_f32 v236, v236, v233, v234\n" \
  "v_fma_f32 v237, v237, v233, v234\n" \
  "v_fma_f32 v238, v238, v233, v234\n" \
  "v_fma_f32 v239, v239, v233, v234\n" \
  "v_cvt_pk_bf16_f32 v178, v236, v237\n" \
  "v_cvt_pk_bf16_f32 v179, v238, v239\n" \
  "v_mfma_f32_32x32x16_bf16 v[128:143], a[24:27], a[44:47], v[128:143]\n" \
  "ds_read_b64_tr_b16 a[12:13], v227 offset:50176\n" \
  "ds_read_b64_tr_b16 a[14:15], v227 offset:50688\n" \
  "v_accvgpr_write_b32 a50, v178\n" \
  "v_accvgpr_write_b32 a51, v179\n" \
  "v_lshlrev_b32_e32 v236, 16, v180\n" \
  "v_and_b32_e32 v237, s78, v180\n" \
  "v_lshlrev_b32_e32 v238, 16, v181\n" \
  "v_and_b32_e32 v239, s78, v181\n" \
  "v_fma_f32 v236, v236, v233, v234\n" \
  "v_mfma_f32_32x32x16_bf16 v[144:159], a[28:31], a[44:47], v[144:159]\n" \
  "v_fma_f32 v237, v237, v233, v234\n" \
  "v_fma_f32 v238, v238, v233, v234\n" \
  "v_fma_f32 v239, v239, v233, v234\n" \
  "v_cvt_pk_bf16_f32 v180, v236, v237\n" \
  "v_cvt_pk_bf16_f32 v181, v238, v239\n" \
  "v_accvgpr_write_b32 a52, v180\n" \
  "v_accvgpr_write_b32 a53, v181\n" \
  "s_waitcnt vmcnt(12) lgkmcnt(0)\n" \
  "s_barrier\n" \
  "v_mfma_f32_32x32x16_bf16 v[96:111], a[0:3], a[48:51], v[96:111]\n" \
  "s_add_u32 m0, s67, 65536\n" \
  "ds_read_b64_tr_b16 a[16:17], v226 offset:53248\n" \
  "ds_read_b64_tr_b16 a[18:19], v226 offset:53760\n" \
  "ds_read_b64_tr_b16 a[20:21], v226 offset:54272\n" \
  "global_load_lds_dwordx4 v229, s[48:49] offset:0\n" \
  "v_lshlrev_b32_e32 v236, 16, v182\n" \
  "v_and_b32_e32 v237, s78, v182\n" \
  "v_lshlrev_b32_e32 v238, 16, v183\n" \
  "v_and_b32_e32 v239, s78, v183\n" \
  "v_mfma_f32_32x32x16_bf16 v[112:127], a[4:7], a[48:51], v[112:127]\n" \
  "ds_read_b64_tr_b16 a[22:23], v226 offset:54784\n" \
  "ds_read_b64_tr_b16 a[24:25], v227 offset:53248\n" \
  "ds_read_b64_tr_b16 a[26:27], v227 offset:53760\n" \
  "v_fma_f32 v236, v236, v233, v234\n" \
  "v_fma_f32 v237, v237, v233, v234\n" \
  "v_fma_f32 v238, v238, v233, v234\n" \
  "v_fma_f32 v239, v239, v233, v234\n" \
  "v_cvt_pk_bf16_f32 v182, v236, v237\n" \
  "v_cvt_pk_bf16_f32 v183, v238, v239\n" \
  "v_mfma_f32_32x32x16_bf16 v[128:143], a[8:11], a[48:51], v[128:143]\n" \
  "ds_read_b64_tr_b16 a[28:29], v227 offset:54272\n" \
  "ds_read_b64_tr_b16 a[30:31], v227 offset:54784\n" \
  "v_accvgpr_write_b32 a54, v182\n" \
  "v_accvgpr_write_b32 a55, v183\n" \
  "v_lshlrev_b32_e32 v236, 16, v184\n" \
  "v_and_b32_e32 v237, s78, v184\n" \
  "v_lshlrev_b32_e32 v238, 16, v185\n" \
  "v_and_b32_e32 v239, s78, v185\n" \
  "v_fma_f32 v236, v236, v233, v234\n" \
  "v_mfma_f32_32x32x16_bf16 v[144:159], a[12:15], a[48:51], v[144:159]\n" \
  "v_fma_f32 v237, v237, v233, v234\n" \
  "v_fma_f32 v238, v238, v233, v234\n" \
  "v_fma_f32 v239, v239, v233, v234\n" \
  "v_cvt_pk_bf16_f32 v184, v236, v237\n" \
  "v_cvt_pk_bf16_f32 v185, v238, v239\n" \
  "v_accvgpr_write_b32 a56, v184\n" \
  "v_accvgpr_write_b32 a57, v185\n" \
  "s_waitcnt lgkmcnt(0)\n" \
  "v_mfma_f32_32x32x16_bf16 v[96:111], a[16:19], a[52:55], v[96:111]\n" \
  "ds_read_b64_tr_b16 a[0:1], v226 offset:57344\n" \
  "ds_read_b64_tr_b16 a[2:3], v226 offset:57856\n" \
  "ds_read_b64_tr_b16 a[4:5], v226 offset:58368\n" \
  "global_load_lds_dwordx4 v229, s[50:51] offset:1024\n" \
  "v_lshlrev_b32_e32 v236, 16, v186\n" \
  "v_and_b32_e32 v237, s78, v186\n" \
  "v_lshlrev_b32_e32 v238, 16, v187\n" \
  "v_and_b32_e32 v239, s78, v187\n" \
  "v_fma_f32 v236, v236, v233, v234\n" \
  "v_mfma_f32_32x32x16_bf16 v[112:127], a[20:23], a[52:55], v[112:127]\n" \
  "ds_read_b64_tr_b16 a[6:7], v226 offset:58880\n" \
  "ds_read_b64_tr_b16 a[8:9], v227 offset:57344\n" \
  "ds_read_b64_tr_b16 a[10:11], v227 offset:57856\n" \
  "v_fma_f32 v237, v237, v233, v234\n" \
  "v_fma_f32 v238, v238, v233, v234\n" \
  "v_fma_f32 v239, v239, v233, v234\n" \
  "v_cvt_pk_bf16_f32 v186, v236, v237\n" \
  "v_cvt_pk_bf16_f32 v187, v238, v239\n" \
  "v_accvgpr_write_b32 a58, v186\n" \
  "v_mfma_f32_32x32x16_bf16 v[128:143], a[24:27], a[52:55], v[128:143]\n" \
  "ds_read_b64_tr_b16 a[12:13], v227 offset:58368\n" \
  "ds_read_b64_tr_b16 a[14:15], v227 offset:58880\n" \
  "v_accvgpr_write_b32 a59, v187\n" \
  "v_lshlrev_b32_e32 v236, 16, v188\n" \
  "v_and_b32_e32 v237, s78, v188\n" \
  "v_lshlrev_b32_e32 v238, 16, v189\n" \
  "v_and_b32_e32 v239, s78, v189\n" \
  "v_fma_f32 v236, v236, v233, v234\n" \
  "v_fma_f32 v237, v237, v233, v234\n" \
  "v_mfma_f32_32x32x16_bf16 v[144:159], a[28:31], a[52:55], v[144:159]\n" \
  "v_fma_f32 v238, v238, v233, v234\n" \
  "v_fma_f32 v239, v239, v233, v234\n" \
  "v_cvt_pk_bf16_f32 v188, v236, v237\n" \
  "v_cvt_pk_bf16_f32 v189, v238, v239\n" \
  "v_accvgpr_write_b32 a60, v188\n" \
  "v_accvgpr_write_b32 a61, v189\n" \
  "s_waitcnt lgkmcnt(0)\n" \
  "v_mfma_f32_32x32x16_bf16 v[96:111], a[0:3], a[56:59], v[96:111]\n" \
  "ds_read_b64_tr_b16 a[16:17], v226 offset:61440\n" \
  "ds_read_b64_tr_b16 a[18:19], v226 offset:61952\n" \
  "ds_read_b64_tr_b16 a[20:21], v226 offset:62464\n" \
  "global_load_lds_dwordx4 v229, s[52:53] offset:2048\n" \
  "v_lshlrev_b32_e32 v236, 16, v190\n" \
  "v_and_b32_e32 v237, s78, v190\n" \
  "v_lshlrev_b32_e32 v238, 16, v191\n" \
  "v_and_b32_e32 v239, s78, v191\n" \
  "v_fma_f32 v236, v236, v233, v234\n" \
  "v_mfma_f32_32x32x16_bf16 v[112:127], a[4:7], a[56:59], v[112:127]\n" \
  "ds_read_b64_tr_b16 a[22:23], v226 offset:62976\n" \
  "ds_read_b64_tr_b16 a[24:25], v227 offset:61440\n" \
  "ds_read_b64_tr_b16 a[26:27], v227 offset:61952\n" \
  "v_fma_f32 v237, v237, v233, v234\n" \
  "v_fma_f32 v238, v238, v233, v234\n" \
  "v_fma_f32 v239, v239, v233, v234\n" \
  "v_cvt_pk_bf16_f32 v190, v236, v237\n" \
  "v_cvt_pk_bf16_f32 v191, v238, v239\n" \
  "v_accvgpr_write_b32 a62, v190\n" \
  "v_mfma_f32_32x32x16_bf16 v[128:143], a[8:11], a[56:59], v[128:143]\n" \
  "ds_read_b64_tr_b16 a[28:29], v227 offset:62464\n" \
  "ds_read_b64_tr_b16 a[30:31], v227 offset:62976\n" \
  "v_accvgpr_write_b32 a63, v191\n" \
  "v_lshlrev_b32_e32 v236, 16, v192\n" \
  "v_and_b32_e32 v237, s78, v192\n" \
  "v_lshlrev_b32_e32 v238, 16, v193\n" \
  "v_and_b32_e32 v239, s78, v193\n" \
  "v_fma_f32 v236, v236, v233, v234\n" \
  "v_fma_f32 v237, v237, v233, v234\n" \
  "v_mfma_f32_32x32x16_bf16 v[144:159], a[12:15], a[56:59], v[144:159]\n" \
  "v_fma_f32 v238, v238, v233, v234\n" \
  "v_fma_f32 v239, v239, v233, v234\n" \
  "v_cvt_pk_bf16_f32 v192, v236, v237\n" \
  "v_cvt_pk_bf16_f32 v193, v238, v239\n" \
  "v_accvgpr_write_b32 a64, v192\n" \
  "v_accvgpr_write_b32 a65, v193\n" \
  "s_waitcnt lgkmcnt(0)\n" \
  "v_mfma_f32_32x32x16_bf16 v[96:111], a[16:19], a[60:63], v[96:111]\n" \
  "ds_read_b64_tr_b16 a[0:1], v224 offset:0\n" \
  "ds_read_b64_tr_b16 a[2:3], v224 offset:512\n" \
  "ds_read_b64_tr_b16 a[4:5], v224 offset:1024\n" \
  "global_load_lds_dwordx4 v229, s[54:55] offset:3072\n" \
  "v_add_u32_e32 v229, s65, v229\n" \
  "v_lshlrev_b32_e32 v236, 16, v194\n" \
  "v_and_b32_e32 v237, s78, v194\n" \
  "v_lshlrev_b32_e32 v238, 16, v195\n" \
  "v_and_b32_e32 v239, s78, v195\n" \
  "v_mfma_f32_32x32x16_bf16 v[112:127], a[20:23], a[60:63], v[112:127]\n" \
  "ds_read_b64_tr_b16 a[6:7], v224 offset:1536\n" \
  "ds_read_b64_tr_b16 a[8:9], v225 offset:0\n" \
  "ds_read_b64_tr_b16 a[10:11], v225 offset:512\n" \
  "v_fma_f32 v236, v236, v233, v234\n" \
  "v_fma_f32 v237, v237, v233, v234\n" \
  "v_fma_f32 v238, v238, v233, v234\n" \
  "v_fma_f32 v239, v239, v233, v234\n" \
  "v_cvt_pk_bf16_f32 v194, v236, v237\n" \
  "v_cvt_pk_bf16_f32 v195, v238, v239\n" \
  "v_mfma_f32_32x32x16_bf16 v[128:143], a[24:27], a[60:63], v[128:143]\n" \
  "ds_read_b64_tr_b16 a[12:13], v225 offset:1024\n" \
  "ds_read_b64_tr_b16 a[14:15], v225 offset:1536\n" \
  "v_accvgpr_write_b32 a66, v194\n" \
  "v_accvgpr_write_b32 a67, v195\n" \
  "v_lshlrev_b32_e32 v236, 16, v196\n" \
  "v_and_b32_e32 v237, s78, v196\n" \
  "v_lshlrev_b32_e32 v238, 16, v197\n" \
  "v_and_b32_e32 v239, s78, v197\n" \
  "v_fma_f32 v236, v236, v233, v234\n" \
  "v_mfma_f32_32x32x16_bf16 v[144:159], a[28:31], a[60:63], v[144:159]\n" \
  "v_fma_f32 v237, v237, v233, v234\n" \
  "v_fma_f32 v238, v238, v233, v234\n" \
  "v_fma_f32 v239, v239, v233, v234\n" \
  "v_cvt_pk_bf16_f32 v196, v236, v237\n" \
  "v_cvt_pk_bf16_f32 v197, v238, v239\n" \
  "v_accvgpr_write_b32 a68, v196\n" \
  "v_accvgpr_write_b32 a69, v197\n" \
  "s_waitcnt vmcnt(12) lgkmcnt(0)\n" \
  "s_barrier\n" \
  "v_mfma_f32_32x32x16_bf16 v[96:111], a[0:3], a[64:67], v[96:111]\n" \
  "s_add_u32 m0, s67, 81920\n" \
  "ds_read_b64_tr_b16 a[16:17], v224 offset:4096\n" \
  "ds_read_b64_tr_b16 a[18:19], v224 offset:4608\n" \
  "ds_read_b64_tr_b16 a[20:21], v224 offset:5120\n" \
  "global_load_lds_dwordx4 v229, s[48:49] offset:0\n" \
  "v_lshlrev_b32_e32 v236, 16, v198\n" \
  "v_and_b32_e32 v237, s78, v198\n" \
  "v_lshlrev_b32_e32 v238, 16, v199\n" \
  "v_and_b32_e32 v239, s78, v199\n" \
  "v_mfma_f32_32x32x16_bf16 v[112:127], a[4:7], a[64:67], v[112:127]\n" \
  "ds_read_b64_tr_b16 a[22:23], v224 offset:5632\n" \
  "ds_read_b64_tr_b16 a[24:25], v225 offset:4096\n" \
  "ds_read_b64_tr_b16 a[26:27], v225 offset:4608\n" \
  "v_fma_f32 v236, v236, v233, v234\n" \
  "v_fma_f32 v237, v237, v233, v234\n" \
  "v_fma_f32 v238, v238, v233, v234\n" \
  "v_fma_f32 v239, v239, v233, v234\n" \
  "v_cvt_pk_bf16_f32 v198, v236, v237\n" \
  "v_cvt_pk_bf16_f32 v199, v238, v239\n" \
  "v_mfma_f32_32x32x16_bf16 v[128:143], a[8:11], a[64:67], v[128:143]\n" \
  "ds_read_b64_tr_b16 a[28:29], v225 offset:5120\n" \
  "ds_read_b64_tr_b16 a[30:31], v225 offset:5632\n" \
  "v_accvgpr_write_b32 a70, v198\n" \
  "v_accvgpr_write_b32 a71, v199\n" \
  "v_lshlrev_b32_e32 v236, 16, v200\n" \
  "v_and_b32_e32 v237, s78, v200\n" \
  "v_lshlrev_b32_e32 v238, 16, v201\n" \
  "v_and_b32_e32 v239, s78, v201\n" \
  "v_fma_f32 v236, v236, v233, v234\n" \
  "v_mfma_f32_32x32x16_bf16 v[144:159], a[12:15], a[64:67], v[144:159]\n" \
  "v_fma_f32 v237, v237, v233, v234\n" \
  "v_fma_f32 v238, v238, v233, v234\n" \
  "v_fma_f32 v239, v239, v233, v234\n" \
  "v_cvt_pk_bf16_f32 v200, v236, v237\n" \
  "v_cvt_pk_bf16_f32 v201, v238, v239\n" \
  "v_accvgpr_write_b32 a72, v200\n" \
  "v_accvgpr_write_b32 a73, v201\n" \
  "s_waitcnt lgkmcnt(0)\n" \
  "v_mfma_f32_32x32x16_bf16 v[96:111], a[16:19], a[68:71], v[96:111]\n" \
  "ds_read_b64_tr_b16 a[0:1], v224 offset:8192\n" \
  "ds_read_b64_tr_b16 a[2:3], v224 offset:8704\n" \
  "ds_read_b64_tr_b16 a[4:5], v224 offset:9216\n" \
  "global_load_lds_dwordx4 v229, s[50:51] offset:1024\n" \
  "v_lshlrev_b32_e32 v236, 16, v202\n" \
  "v_and_b32_e32 v237, s78, v202\n" \
  "v_lshlrev_b32_e32 v238, 16, v203\n" \
  "v_and_b32_e32 v239, s78, v203\n" \
  "v_fma_f32 v236, v236, v233, v234\n" \
  "v_mfma_f32_32x32x16_bf16 v[112:127], a[20:23], a[68:71], v[112:127]\n" \
  "ds_read_b64_tr_b16 a[6:7], v224 offset:9728\n" \
  "ds_read_b64_tr_b16 a[8:9], v225 offset:8192\n" \
  "ds_read_b64_tr_b16 a[10:11], v225 offset:8704\n" \
  "v_fma_f32 v237, v237, v233, v234\n" \
  "v_fma_f32 v238, v238, v233, v234\n" \
  "v_fma_f32 v239, v239, v233, v234\n" \
  "v_cvt_pk_bf16_f32 v202, v236, v237\n" \
  "v_cvt_pk_bf16_f32 v203, v238, v239\n" \
  "v_accvgpr_write_b32 a74, v202\n" \
  "v_mfma_f32_32x32x16_bf16 v[128:143], a[24:27], a[68:71], v[128:143]\n" \
  "ds_read_b64_tr_b16 a[12:13], v225 offset:9216\n" \
  "ds_read_b64_tr_b16 a[14:15], v225 offset:9728\n" \
  "v_accvgpr_write_b32 a75, v203\n" \
  "v_lshlrev_b32_e32 v236, 16, v204\n" \
  "v_and_b32_e32 v237, s78, v204\n" \
  "v_lshlrev_b32_e32 v238, 16, v205\n" \
  "v_and_b32_e32 v239, s78, v205\n" \
  "v_fma_f32 v236, v236, v233, v234\n" \
  "v_fma_f32 v237, v237, v233, v234\n" \
  "v_mfma_f32_32x32x16_bf16 v[144:159], a[28:31], a[68:71], v[144:159]\n" \
  "v_fma_f32 v238, v238, v233, v234\n" \
  "v_fma_f32 v239, v239, v233, v234\n" \
  "v_cvt_pk_bf16_f32 v204, v236, v237\n" \
  "v_cvt_pk_bf16_f32 v205, v238, v239\n" \
  "v_accvgpr_write_b32 a76, v204\n" \
  "v_accvgpr_write_b32 a77, v205\n" \
  "s_waitcnt lgkmcnt(0)\n" \
  "v_mfma_f32_32x32x16_bf16 v[96:111], a[0:3], a[72:75], v[96:111]\n" \
  "ds_read_b64_tr_b16 a[16:17], v224 offset:12288\n" \
  "ds_read_b64_tr_b16 a[18:19], v224 offset:12800\n" \
  "ds_read_b64_tr_b16 a[20:21], v224 offset:13312\n" \
  "global_load_lds_dwordx4 v229, s[52:53] offset:2048\n" \
  "v_lshlrev_b32_e32 v236, 16, v206\n" \
  "v_and_b32_e32 v237, s78, v206\n" \
  "v_lshlrev_b32_e32 v238, 16, v207\n" \
  "v_and_b32_e32 v239, s78, v207\n" \
  "v_fma_f32 v236, v236, v233, v234\n" \
  "v_mfma_f32_32x32x16_bf16 v[112:127], a[4:7], a[72:75], v[112:127]\n" \
  "ds_read_b64_tr_b16 a[22:23], v224 offset:13824\n" \
  "ds_read_b64_tr_b16 a[24:25], v225 offset:12288\n" \
  "ds_read_b64_tr_b16 a[26:27], v225 offset:12800\n" \
  "v_fma_f32 v237, v237, v233, v234\n" \
  "v_fma_f32 v238, v238, v233, v234\n" \
  "v_fma_f32 v239, v239, v233, v234\n" \
  "v_cvt_pk_bf16_f32 v206, v236, v237\n" \
  "v_cvt_pk_bf16_f32 v207, v238, v239\n" \
  "v_accvgpr_write_b32 a78, v206\n" \
  "v_mfma_f32_32x32x16_bf16 v[128:143], a[8:11], a[72:75], v[128:143]\n" \
  "ds_read_b64_tr_b16 a[28:29], v225 offset:13312\n" \
  "ds_read_b64_tr_b16 a[30:31], v225 offset:13824\n" \
  "v_accvgpr_write_b32 a79, v207\n" \
  "v_lshlrev_b32_e32 v236, 16, v208\n" \
  "v_and_b32_e32 v237, s78, v208\n" \
  "v_lshlrev_b32_e32 v238, 16, v209\n" \
  "v_and_b32_e32 v239, s78, v209\n" \
  "v_fma_f32 v236, v236, v233, v234\n" \
  "v_fma_f32 v237, v237, v233, v234\n" \
  "v_mfma_f32_32x32x16_bf16 v[144:159], a[12:15], a[72:75], v[144:159]\n" \
  "v_fma_f32 v238, v238, v233, v234\n" \
  "v_fma_f32 v239, v239, v233, v234\n" \
  "v_cvt_pk_bf16_f32 v208, v236, v237\n" \
  "v_cvt_pk_bf16_f32 v209, v238, v239\n" \
  "v_accvgpr_write_b32 a80, v208\n" \
  "v_accvgpr_write_b32 a81, v209\n" \
  "s_waitcnt lgkmcnt(0)\n" \
  "v_mfma_f32_32x32x16_bf16 v[96:111], a[16:19], a[76:79], v[96:111]\n" \
  "ds_read_b64_tr_b16 a[0:1], v224 offset:16384\n" \
  "ds_read_b64_tr_b16 a[2:3], v224 offset:16896\n" \
  "ds_read_b64_tr_b16 a[4:5], v224 offset:17408\n" \
  "global_load_lds_dwordx4 v229, s[54:55] offset:3072\n" \
  "v_add_u32_e32 v229, s65, v229\n" \
  "v_lshlrev_b32_e32 v236, 16, v210\n" \
  "v_and_b32_e32 v237, s78, v210\n" \
  "v_lshlrev_b32_e32 v238, 16, v211\n" \
  "v_and_b32_e32 v239, s78, v211\n" \
  "v_mfma_f32_32x32x16_bf16 v[112:127], a[20:23], a[76:79], v[112:127]\n" \
  "ds_read_b64_tr_b16 a[6:7], v224 offset:17920\n" \
  "ds_read_b64_tr_b16 a[8:9], v225 offset:16384\n" \
  "ds_read_b64_tr_b16 a[10:11], v225 offset:16896\n" \
  "v_fma_f32 v236, v236, v233, v234\n" \
  "v_fma_f32 v237, v237, v233, v234\n" \
  "v_fma_f32 v238, v238, v233, v234\n" \
  "v_fma_f32 v239, v239, v233, v234\n" \
  "v_cvt_pk_bf16_f32 v210, v236, v237\n" \
  "v_cvt_pk_bf16_f32 v211, v238, v239\n" \
  "v_mfma_f32_32x32x16_bf16 v[128:143], a[24:27], a[76:79], v[128:143]\n" \
  "ds_read_b64_tr_b16 a[12:13], v225 offset:17408\n" \
  "ds_read_b64_tr_b16 a[14:15], v225 offset:17920\n" \
  "v_accvgpr_write_b32 a82, v210\n" \
  "v_accvgpr_write_b32 a83, v211\n" \
  "v_lshlrev_b32_e32 v236, 16, v212\n" \
  "v_and_b32_e32 v237, s78, v212\n" \
  "v_lshlrev_b32_e32 v238, 16, v213\n" \
  "v_and_b32_e32 v239, s78, v213\n" \
  "v_fma_f32 v236, v236, v233, v234\n" \
  "v_mfma_f32_32x32x16_bf16 v[144:159], a[28:31], a[76:79], v[144:159]\n" \
  "v_fma_f32 v237, v237, v233, v234\n" \
  "v_fma_f32 v238, v238, v233, v234\n" \
  "v_fma_f32 v239, v239, v233, v234\n" \
  "v_cvt_pk_bf16_f32 v212, v236, v237\n" \
  "v_cvt_pk_bf16_f32 v213, v238, v239\n" \
  "v_accvgpr_write_b32 a84, v212\n" \
  "v_accvgpr_write_b32 a85, v213\n" \
  "s_waitcnt vmcnt(12) lgkmcnt(0)\n" \
  "s_barrier\n" \
  "v_mfma_f32_32x32x16_bf16 v[96:111], a[0:3], a[80:83], v[96:111]\n" \
  "s_add_u32 m0, s67, 98304\n" \
  "ds_read_b64_tr_b16 a[16:17], v224 offset:20480\n" \
  "ds_read_b64_tr_b16 a[18:19], v224 offset:20992\n" \
  "ds_read_b64_tr_b16 a[20:21], v224 offset:21504\n" \
  "global_load_lds_dwordx4 v229, s[48:49] offset:0\n" \
  "v_lshlrev_b32_e32 v236, 16, v214\n" \
  "v_and_b32_e32 v237, s78, v214\n" \
  "v_lshlrev_b32_e32 v238, 16, v215\n" \
  "v_and_b32_e32 v239, s78, v215\n" \
  "v_mfma_f32_32x32x16_bf16 v[112:127], a[4:7], a[80:83], v[112:127]\n" \
  "ds_read_b64_tr_b16 a[22:23], v224 offset:22016\n" \
  "ds_read_b64_tr_b16 a[24:25], v225 offset:20480\n" \
  "ds_read_b64_tr_b16 a[26:27], v225 offset:20992\n" \
  "v_fma_f32 v236, v236, v233, v234\n" \
  "v_fma_f32 v237, v237, v233, v234\n" \
  "v_fma_f32 v238, v238, v233, v234\n" \
  "v_fma_f32 v239, v239, v233, v234\n" \
  "v_cvt_pk_bf16_f32 v214, v236, v237\n" \
  "v_cvt_pk_bf16_f32 v215, v238, v239\n" \
  "v_mfma_f32_32x32x16_bf16 v[128:143], a[8:11], a[80:83], v[128:143]\n" \
  "ds_read_b64_tr_b16 a[28:29], v225 offset:21504\n" \
  "ds_read_b64_tr_b16 a[30:31], v225 offset:22016\n" \
  "v_accvgpr_write_b32 a86, v214\n" \
  "v_accvgpr_write_b32 a87, v215\n" \
  "v_lshlrev_b32_e32 v236, 16, v216\n" \
  "v_and_b32_e32 v237, s78, v216\n" \
  "v_lshlrev_b32_e32 v238, 16, v217\n" \
  "v_and_b32_e32 v239, s78, v217\n" \
  "v_fma_f32 v236, v236, v233, v234\n" \
  "v_mfma_f32_32x32x16_bf16 v[144:159], a[12:15], a[80:83], v[144:159]\n" \
  "v_fma_f32 v237, v237, v233, v234\n" \
  "v_fma_f32 v238, v238, v233, v234\n" \
  "v_fma_f32 v239, v239, v233, v234\n" \
  "v_cvt_pk_bf16_f32 v216, v236, v237\n" \
  "v_cvt_pk_bf16_f32 v217, v238, v239\n" \
  "v_accvgpr_write_b32 a88, v216\n" \
  "v_accvgpr_write_b32 a89, v217\n" \
  "s_waitcnt lgkmcnt(0)\n" \
  "v_mfma_f32_32x32x16_bf16 v[96:111], a[16:19], a[84:87], v[96:111]\n" \
  "ds_read_b64_tr_b16 a[0:1], v224 offset:24576\n" \
  "ds_read_b64_tr_b16 a[2:3], v224 offset:25088\n" \
  "ds_read_b64_tr_b16 a[4:5], v224 offset:25600\n" \
  "global_load_lds_dwordx4 v229, s[50:51] offset:1024\n" \
  "v_lshlrev_b32_e32 v236, 16, v218\n" \
  "v_and_b32_e32 v237, s78, v218\n" \
  "v_lshlrev_b32_e32 v238, 16, v219\n" \
  "v_and_b32_e32 v239, s78, v219\n" \
  "v_fma_f32 v236, v236, v233, v234\n" \
  "v_mfma_f32_32x32x16_bf16 v[112:127], a[20:23], a[84:87], v[112:127]\n" \
  "ds_read_b64_tr_b16 a[6:7], v224 offset:26112\n" \
  "ds_read_b64_tr_b16 a[8:9], v225 offset:24576\n" \
  "ds_read_b64_tr_b16 a[10:11], v225 offset:25088\n" \
  "v_fma_f32 v237, v237, v233, v234\n" \
  "v_fma_f32 v238, v238, v233, v234\n" \
  "v_fma_f32 v239, v239, v233, v234\n" \
  "v_cvt_pk_bf16_f32 v218, v236, v237\n" \
  "v_cvt_pk_bf16_f32 v219, v238, v239\n" \
  "v_accvgpr_write_b32 a90, v218\n" \
  "v_mfma_f32_32x32x16_bf16 v[128:143], a[24:27], a[84:87], v[128:143]\n" \
  "ds_read_b64_tr_b16 a[12:13], v225 offset:25600\n" \
  "ds_read_b64_tr_b16 a[14:15], v225 offset:26112\n" \
  "v_accvgpr_write_b32 a91, v219\n" \
  "v_lshlrev_b32_e32 v236, 16, v220\n" \
  "v_and_b32_e32 v237, s78, v220\n" \
  "v_lshlrev_b32_e32 v238, 16, v221\n" \
  "v_and_b32_e32 v239, s78, v221\n" \
  "v_fma_f32 v236, v236, v233, v234\n" \
  "v_fma_f32 v237, v237, v233, v234\n" \
  "v_mfma_f32_32x32x16_bf16 v[144:159], a[28:31], a[84:87], v[144:159]\n" \
  "v_fma_f32 v238, v238, v233, v234\n" \
  "v_fma_f32 v239, v239, v233, v234\n" \
  "v_cvt_pk_bf16_f32 v220, v236, v237\n" \
  "v_cvt_pk_bf16_f32 v221, v238, v239\n" \
  "v_accvgpr_write_b32 a92, v220\n" \
  "v_accvgpr_write_b32 a93, v221\n" \
  "s_waitcnt lgkmcnt(0)\n" \
  "v_mfma_f32_32x32x16_bf16 v[96:111], a[0:3], a[88:91], v[96:111]\n" \
  "ds_read_b64_tr_b16 a[16:17], v224 offset:28672\n" \
  "ds_read_b64_tr_b16 a[18:19], v224 offset:29184\n" \
  "ds_read_b64_tr_b16 a[20:21], v224 offset:29696\n" \
  "global_load_lds_dwordx4 v229, s[52:53] offset:2048\n" \
  "v_lshlrev_b32_e32 v236, 16, v222\n" \
  "v_and_b32_e32 v237, s78, v222\n" \
  "v_mfma_f32_32x32x16_bf16 v[112:127], a[4:7], a[88:91], v[112:127]\n" \
  "ds_read_b64_tr_b16 a[22:23], v224 offset:30208\n" \
  "ds_read_b64_tr_b16 a[24:25], v225 offset:28672\n" \
  "ds_read_b64_tr_b16 a[26:27], v225 offset:29184\n" \
  "v_lshlrev_b32_e32 v238, 16, v223\n" \
  "v_and_b32_e32 v239, s78, v223\n" \
  "v_fma_f32 v236, v236, v233, v234\n" \
  "v_mfma_f32_32x32x16_bf16 v[128:143], a[8:11], a[88:91], v[128:143]\n" \
  "ds_read_b64_tr_b16 a[28:29], v225 offset:29696\n" \
  "ds_read_b64_tr_b16 a[30:31], v225 offset:30208\n" \
  "v_fma_f32 v237, v237, v233, v234\n" \
  "v_fma_f32 v238, v238, v233, v234\n" \
  "v_fma_f32 v239, v239, v233, v234\n" \
  "v_cvt_pk_bf16_f32 v222, v236, v237\n" \
  "v_mfma_f32_32x32x16_bf16 v[144:159], a[12:15], a[88:91], v[144:159]\n" \
  "v_cvt_pk_bf16_f32 v223, v238, v239\n" \
  "v_accvgpr_write_b32 a94, v222\n" \
  "v_accvgpr_write_b32 a95, v223\n" \
  "s_waitcnt lgkmcnt(0)\n" \
  "s_nop 0\n" \
  "v_mfma_f32_32x32x16_bf16 v[96:111], a[16:19], a[92:95], v[96:111]\n" \
  "ds_read_b64_tr_b16 a[0:1], v224 offset:32768\n" \
  "ds_read_b64_tr_b16 a[2:3], v224 offset:33280\n" \
  "ds_read_b64_tr_b16 a[4:5], v224 offset:33792\n" \
  "v_mfma_f32_32x32x16_bf16 v[112:127], a[20:23], a[92:95], v[112:127]\n" \
  "ds_read_b64_tr_b16 a[6:7], v224 offset:34304\n" \
  "ds_read_b64_tr_b16 a[8:9], v225 offset:32768\n" \
  "ds_read_b64_tr_b16 a[10:11], v225 offset:33280\n" \
  "v_mfma_f32_32x32x16_bf16 v[128:143], a[24:27], a[92:95], v[128:143]\n" \
  "ds_read_b64_tr_b16 a[12:13], v225 offset:33792\n" \
  "ds_read_b64_tr_b16 a[14:15], v225 offset:34304\n" \
  "global_load_lds_dwordx4 v229, s[54:55] offset:3072\n" \
  "v_mfma_f32_32x32x16_bf16 v[144:159], a[28:31], a[92:95], v[144:159]\n" \
  "v_add_u32_e32 v229, s65, v229\n" \
  "s_waitcnt vmcnt(12) lgkmcnt(0)\n" \
  "s_barrier\n" \
  "v_mfma_f32_32x32x16_bf16 v[160:175], a[0:3], v[0:3], 0\n" \
  "s_add_u32 m0, s67, 114688\n" \
  "ds_read_b64_tr_b16 a[16:17], v224 offset:36864\n" \
  "ds_read_b64_tr_b16 a[18:19], v224 offset:37376\n" \
  "ds_read_b64_tr_b16 a[20:21], v224 offset:37888\n" \
  "v_mfma_f32_32x32x16_bf16 v[176:191], a[4:7], v[0:3], 0\n" \
  "ds_read_b64_tr_b16 a[22:23], v224 offset:38400\n" \
  "ds_read_b64_tr_b16 a[24:25], v225 offset:36864\n" \
  "ds_read_b64_tr_b16 a[26:27], v225 offset:37376\n" \
  "v_mfma_f32_32x32x16_bf16 v[192:207], a[8:11], v[0:3], 0\n" \
  "ds_read_b64_tr_b16 a[28:29], v225 offset:37888\n" \
  "ds_read_b64_tr_b16 a[30:31], v225 offset:38400\n" \
  "global_load_lds_dwordx4 v229, s[48:49] offset:0\n" \
  "v_mfma_f32_32x32x16_bf16 v[208:223], a[12:15], v[0:3], 0\n" \
  "s_waitcnt lgkmcnt(0)\n" \
  "v_mfma_f32_32x32x16_bf16 v[160:175], a[16:19], v[4:7], v[160:175]\n" \
  "ds_read_b64_tr_b16 a[0:1], v224 offset:40960\n" \
  "ds_read_b64_tr_b16 a[2:3], v224 offset:41472\n" \
  "ds_read_b64_tr_b16 a[4:5], v224 offset:41984\n" \
  "global_load_lds_dwordx4 v229, s[50:51] offset:1024\n" \
  "v_mfma_f32_32x32x16_bf16 v[176:191], a[20:23], v[4:7], v[176:191]\n" \
  "ds_read_b64_tr_b16 a[6:7], v224 offset:42496\n" \
  "ds_read_b64_tr_b16 a[8:9], v225 offset:40960\n" \
  "ds_read_b64_tr_b16 a[10:11], v225 offset:41472\n" \
  "v_mul_f32_e32 v236, v96, v96\n" \
  "v_mfma_f32_32x32x16_bf16 v[192:207], a[24:27], v[4:7], v[192:207]\n" \
  "ds_read_b64_tr_b16 a[12:13], v225 offset:41984\n" \
  "ds_read_b64_tr_b16 a[14:15], v225 offset:42496\n" \
  "v_mul_f32_e32 v237, v97, v97\n" \
  "v_mul_f32_e32 v238, v98, v98\n" \
  "v_mfma_f32_32x32x16_bf16 v[208:223], a[28:31], v[4:7], v[208:223]\n" \
  "v_mul_f32_e32 v239, v99, v99\n" \
  "v_fma_f32 v236, v236, v232, s77\n" \
  "v_fma_f32 v237, v237, v232, s77\n" \
  "v_fma_f32 v238, v238, v232, s77\n" \
  "s_waitcnt lgkmcnt(0)\n" \
  "v_mfma_f32_32x32x16_bf16 v[160:175], a[0:3], v[8:11], v[160:175]\n" \
  "ds_read_b64_tr_b16 a[16:17], v224 offset:45056\n" \
  "ds_read_b64_tr_b16 a[18:19], v224 offset:45568\n" \
  "ds_read_b64_tr_b16 a[20:21], v224 offset:46080\n" \
  "global_load_lds_dwordx4 v229, s[52:53] offset:2048\n" \
  "v_mfma_f32_32x32x16_bf16 v[176:191], a[4:7], v[8:11], v[176:191]\n" \
  "ds_read_b64_tr_b16 a[22:23], v224 offset:46592\n" \
  "ds_read_b64_tr_b16 a[24:25], v225 offset:45056\n" \
  "ds_read_b64_tr_b16 a[26:27], v225 offset:45568\n" \
  "v_fma_f32 v239, v239, v232, s77\n" \
  "v_mfma_f32_32x32x16_bf16 v[192:207], a[8:11], v[8:11], v[192:207]\n" \
  "ds_read_b64_tr_b16 a[28:29], v225 offset:46080\n" \
  "ds_read_b64_tr_b16 a[30:31], v225 offset:46592\n" \
  "v_mul_f32_e32 v236, v236, v96\n" \
  "v_mul_f32_e32 v237, v237, v97\n" \
  "v_mfma_f32_32x32x16_bf16 v[208:223], a[12:15], v[8:11], v[208:223]\n" \
  "v_mul_f32_e32 v238, v238, v98\n" \
  "v_mul_f32_e32 v239, v239, v99\n" \
  "v_exp_f32_e32 v236, v236\n" \
  "v_mul_f32_e32 v96, v96, v112\n" \
  "s_waitcnt lgkmcnt(0)\n" \
  "v_mfma_f32_32x32x16_bf16 v[160:175], a[16:19], v[12:15], v[160:175]\n" \
  "ds_read_b64_tr_b16 a[0:1], v224 offset:49152\n" \
  "ds_read_b64_tr_b16 a[2:3], v224 offset:49664\n" \
  "ds_read_b64_tr_b16 a[4:5], v224 offset:50176\n" \
  "global_load_lds_dwordx4 v229, s[54:55] offset:3072\n" \
  "v_add_u32_e32 v229, s65, v229\n" \
  "v_mfma_f32_32x32x16_bf16 v[176:191], a[20:23], v[12:15], v[176:191]\n" \
  "ds_read_b64_tr_b16 a[6:7], v224 offset:50688\n" \
  "ds_read_b64_tr_b16 a[8:9], v225 offset:49152\n" \
  "ds_read_b64_tr_b16 a[10:11], v225 offset:49664\n" \
  "v_exp_f32_e32 v237, v237\n" \
  "v_mul_f32_e32 v97, v97, v113\n" \
  "v_mfma_f32_32x32x16_bf16 v[192:207], a[24:27], v[12:15], v[192:207]\n" \
  "ds_read_b64_tr_b16 a[12:13], v225 offset:50176\n" \
  "ds_read_b64_tr_b16 a[14:15], v225 offset:50688\n" \
  "v_exp_f32_e32 v238, v238\n" \
  "v_mul_f32_e32 v98, v98, v114\n" \
  "v_exp_f32_e32 v239, v239\n" \
  "v_mfma_f32_32x32x16_bf16 v[208:223], a[28:31], v[12:15], v[208:223]\n" \
  "v_mul_f32_e32 v99, v99, v115\n" \
  "v_add_f32_e32 v236, 1.0, v236\n" \
  "s_waitcnt vmcnt(12) lgkmcnt(0)\n" \
  "s_barrier\n" \
  "v_mfma_f32_32x32x16_bf16 v[160:175], a[0:3], v[16:19], v[160:175]\n" \
  "s_add_u32 m0, s67, 0\n" \
  "ds_read_b64_tr_b16 a[16:17], v224 offset:53248\n" \
  "ds_read_b64_tr_b16 a[18:19], v224 offset:53760\n" \
  "ds_read_b64_tr_b16 a[20:21], v224 offset:54272\n" \
  "global_load_lds_dwordx4 v229, s[48:49] offset:0\n" \
  "v_mfma_f32_32x32x16_bf16 v[176:191], a[4:7], v[16:19], v[176:191]\n" \
  "ds_read_b64_tr_b16 a[22:23], v224 offset:54784\n" \
  "ds_read_b64_tr_b16 a[24:25], v225 offset:53248\n" \
  "ds_read_b64_tr_b16 a[26:27], v225 offset:53760\n" \
  "v_add_f32_e32 v237, 1.0, v237\n" \
  "v_rcp_f32_e32 v236, v236\n" \
  "v_mfma_f32_32x32x16_bf16 v[192:207], a[8:11], v[16:19], v[192:207]\n" \
  "ds_read_b64_tr_b16 a[28:29], v225 offset:54272\n" \
  "ds_read_b64_tr_b16 a[30:31], v225 offset:54784\n" \
  "v_add_f32_e32 v238, 1.0, v238\n" \
  "v_rcp_f32_e32 v237, v237\n" \
  "v_add_f32_e32 v239, 1.0, v239\n" \
  "v_mfma_f32_32x32x16_bf16 v[208:223], a[12:15], v[16:19], v[208:223]\n" \
  "v_rcp_f32_e32 v238, v238\n" \
  "v_mul_f32_e32 v96, v96, v236\n" \
  "s_waitcnt lgkmcnt(0)\n" \
  "v_mfma_f32_32x32x16_bf16 v[160:175], a[16:19], v[20:23], v[160:175]\n" \
  "ds_read_b64_tr_b16 a[0:1], v224 offset:57344\n" \
  "ds_read_b64_tr_b16 a[2:3], v224 offset:57856\n" \
  "ds_read_b64_tr_b16 a[4:5], v224 offset:58368\n" \
  "global_load_lds_dwordx4 v229, s[50:51] offset:1024\n" \
  "v_mfma_f32_32x32x16_bf16 v[176:191], a[20:23], v[20:23], v[176:191]\n" \
  "ds_read_b64_tr_b16 a[6:7], v224 offset:58880\n" \
  "ds_read_b64_tr_b16 a[8:9], v225 offset:57344\n" \
  "ds_read_b64_tr_b16 a[10:11], v225 offset:57856\n" \
  "v_rcp_f32_e32 v239, v239\n" \
  "v_mfma_f32_32x32x16_bf16 v[192:207], a[24:27], v[20:23], v[192:207]\n" \
  "ds_read_b64_tr_b16 a[12:13], v225 offset:58368\n" \
  "ds_read_b64_tr_b16 a[14:15], v225 offset:58880\n" \
  "v_mul_f32_e32 v97, v97, v237\n" \
  "v_mul_f32_e32 v98, v98, v238\n" \
  "v_mfma_f32_32x32x16_bf16 v[208:223], a[28:31], v[20:23], v[208:223]\n" \
  "v_mul_f32_e32 v99, v99, v239\n" \
  "v_cvt_pk_bf16_f32 v112, v96, v97\n" \
  "v_cvt_pk_bf16_f32 v113, v98, v99\n" \
  "v_mul_f32_e32 v240, v100, v100\n" \
  "s_waitcnt lgkmcnt(0)\n" \
  "v_mfma_f32_32x32x16_bf16 v[160:175], a[0:3], v[24:27], v[160:175]\n" \
  "ds_read_b64_tr_b16 a[16:17], v224 offset:61440\n" \
  "ds_read_b64_tr_b16 a[18:19], v224 offset:61952\n" \
  "ds_read_b64_tr_b16 a[20:21], v224 offset:62464\n" \
  "global_load_lds_dwordx4 v229, s[52:53] offset:2048\n" \
  "v_mfma_f32_32x32x16_bf16 v[176:191], a[4:7], v[24:27], v[176:191]\n" \
  "ds_read_b64_tr_b16 a[22:23], v224 offset:62976\n" \
  "ds_read_b64_tr_b16 a[24:25], v225 offset:61440\n" \
  "ds_read_b64_tr_b16 a[26:27], v225 offset:61952\n" \
  "v_mul_f32_e32 v241, v101, v101\n" \
  "v_mfma_f32_32x32x16_bf16 v[192:207], a[8:11], v[24:27], v[192:207]\n" \
  "ds_read_b64_tr_b16 a[28:29], v225 offset:62464\n" \
  "ds_read_b64_tr_b16 a[30:31], v225 offset:62976\n" \
  "v_mul_f32_e32 v242, v102, v102\n" \
  "v_mul_f32_e32 v243, v103, v103\n" \
  "v_mfma_f32_32x32x16_bf16 v[208:223], a[12:15], v[24:27], v[208:223]\n" \
  "v_fma_f32 v240, v240, v232, s77\n" \
  "v_fma_f32 v241, v241, v232, s77\n" \
  "v_fma_f32 v242, v242, v232, s77\n" \
  "v_fma_f32 v243, v243, v232, s77\n" \
  "s_waitcnt lgkmcnt(0)\n" \
  "v_mfma_f32_32x32x16_bf16 v[160:175], a[16:19], v[28:31], v[160:175]\n" \
  "ds_read_b64_tr_b16 a[0:1], v226 offset:0\n" \
  "ds_read_b64_tr_b16 a[2:3], v226 offset:512\n" \
  "ds_read_b64_tr_b16 a[4:5], v226 offset:1024\n" \
  "global_load_lds_dwordx4 v229, s[54:55] offset:3072\n" \
  "v_add_u32_e32 v229, s65, v229\n" \
  "v_mfma_f32_32x32x16_bf16 v[176:191], a[20:23], v[28:31], v[176:191]\n" \
  "ds_read_b64_tr_b16 a[6:7], v226 offset:1536\n" \
  "ds_read_b64_tr_b16 a[8:9], v227 offset:0\n" \
  "ds_read_b64_tr_b16 a[10:11], v227 offset:512\n" \
  "v_mul_f32_e32 v240, v240, v100\n" \
  "v_mul_f32_e32 v241, v241, v101\n" \
  "v_mfma_f32_32x32x16_bf16 v[192:207], a[24:27], v[28:31], v[192:207]\n" \
  "ds_read_b64_tr_b16 a[12:13], v227 offset:1024\n" \
  "ds_read_b64_tr_b16 a[14:15], v227 offset:1536\n" \
  "v_mul_f32_e32 v242, v242, v102\n" \
  "v_mul_f32_e32 v243, v243, v103\n" \
  "v_exp_f32_e32 v240, v240\n" \
  "v_mfma_f32_32x32x16_bf16 v[208:223], a[28:31], v[28:31], v[208:223]\n" \
  "v_mul_f32_e32 v100, v100, v116\n" \
  "v_exp_f32_e32 v241, v241\n" \
  "s_waitcnt vmcnt(12) lgkmcnt(0)\n" \
  "s_barrier\n" \
  "v_mfma_f32_32x32x16_bf16 v[160:175], a[0:3], v[32:35], v[160:175]\n" \
  "s_add_u32 m0, s67, 16384\n" \
  "ds_read_b64_tr_b16 a[16:17], v226 offset:4096\n" \
  "ds_read_b64_tr_b16 a[18:19], v226 offset:4608\n" \
  "ds_read_b64_tr_b16 a[20:21], v226 offset:5120\n" \
  "global_load_lds_dwordx4 v229, s[48:49] offset:0\n" \
  "v_mfma_f32_32x32x16_bf16 v[176:191], a[4:7], v[32:35], v[176:191]\n" \
  "ds_read_b64_tr_b16 a[22:23], v226 offset:5632\n" \
  "ds_read_b64_tr_b16 a[24:25], v227 offset:4096\n" \
  "ds_read_b64_tr_b16 a[26:27], v227 offset:4608\n" \
  "v_mul_f32_e32 v101, v101, v117\n" \
  "v_exp_f32_e32 v242, v242\n" \
  "v_mfma_f32_32x32x16_bf16 v[192:207], a[8:11], v[32:35], v[192:207]\n" \
  "ds_read_b64_tr_b16 a[28:29], v227 offset:5120\n" \
  "ds_read_b64_tr_b16 a[30:31], v227 offset:5632\n" \
  "v_mul_f32_e32 v102, v102, v118\n" \
  "v_exp_f32_e32 v243, v243\n" \
  "v_mul_f32_e32 v103, v103, v119\n" \
  "v_mfma_f32_32x32x16_bf16 v[208:223], a[12:15], v[32:35], v[208:223]\n" \
  "v_add_f32_e32 v240, 1.0, v240\n" \
  "v_add_f32_e32 v241, 1.0, v241\n" \
  "s_waitcnt lgkmcnt(0)\n" \
  "v_mfma_f32_32x32x16_bf16 v[160:175], a[16:19], v[36:39], v[160:175]\n" \
  "ds_read_b64_tr_b16 a[0:1], v226 offset:8192\n" \
  "ds_read_b64_tr_b16 a[2:3], v226 offset:8704\n" \
  "ds_read_b64_tr_b16 a[4:5], v226 offset:9216\n" \
  "global_load_lds_dwordx4 v229, s[50:51] offset:1024\n" \
  "v_mfma_f32_32x32x16_bf16 v[176:191], a[20:23], v[36:39], v[176:191]\n" \
  "ds_read_b64_tr_b16 a[6:7], v226 offset:9728\n" \
  "ds_read_b64_tr_b16 a[8:9], v227 offset:8192\n" \
  "ds_read_b64_tr_b16 a[10:11], v227 offset:8704\n" \
  "v_rcp_f32_e32 v240, v240\n" \
  "v_mfma_f32_32x32x16_bf16 v[192:207], a[24:27], v[36:39], v[192:207]\n" \
  "ds_read_b64_tr_b16 a[12:13], v227 offset:9216\n" \
  "ds_read_b64_tr_b16 a[14:15], v227 offset:9728\n" \
  "v_add_f32_e32 v242, 1.0, v242\n" \
  "v_rcp_f32_e32 v241, v241\n" \
  "v_mfma_f32_32x32x16_bf16 v[208:223], a[28:31], v[36:39], v[208:223]\n" \
  "v_add_f32_e32 v243, 1.0, v243\n" \
  "v_rcp_f32_e32 v242, v242\n" \
  "v_mul_f32_e32 v100, v100, v240\n" \
  "v_rcp_f32_e32 v243, v243\n" \
  "s_waitcnt lgkmcnt(0)\n" \
  "v_mfma_f32_32x32x16_bf16 v[160:175], a[0:3], v[40:43], v[160:175]\n" \
  "ds_read_b64_tr_b16 a[16:17], v226 offset:12288\n" \
  "ds_read_b64_tr_b16 a[18:19], v226 offset:12800\n" \
  "ds_read_b64_tr_b16 a[20:21], v226 offset:13312\n" \
  "global_load_lds_dwordx4 v229, s[52:53] offset:2048\n" \
  "v_mfma_f32_32x32x16_bf16 v[176:191], a[4:7], v[40:43], v[176:191]\n" \
  "ds_read_b64_tr_b16 a[22:23], v226 offset:13824\n" \
  "ds_read_b64_tr_b16 a[24:25], v227 offset:12288\n" \
  "ds_read_b64_tr_b16 a[26:27], v227 offset:12800\n" \
  "v_mul_f32_e32 v101, v101, v241\n" \
  "v_mfma_f32_32x32x16_bf16 v[192:207], a[8:11], v[40:43], v[192:207]\n" \
  "ds_read_b64_tr_b16 a[28:29], v227 offset:13312\n" \
  "ds_read_b64_tr_b16 a[30:31], v227 offset:13824\n" \
  "v_mul_f32_e32 v102, v102, v242\n" \
  "v_mul_f32_e32 v103, v103, v243\n" \
  "v_mfma_f32_32x32x16_bf16 v[208:223], a[12:15], v[40:43], v[208:223]\n" \
  "v_cvt_pk_bf16_f32 v114, v100, v101\n" \
  "v_cvt_pk_bf16_f32 v115, v102, v103\n" \
  "global_store_dwordx4 v231, v[112:115], s[72:73] offset:0 sc1\n" \
  "v_mul_f32_e32 v236, v104, v104\n" \
  "s_waitcnt lgkmcnt(0)\n" \
  "v_mfma_f32_32x32x16_bf16 v[160:175], a[16:19], v[44:47], v[160:175]\n" \
  "ds_read_b64_tr_b16 a[0:1], v226 offset:16384\n" \
  "ds_read_b64_tr_b16 a[2:3], v226 offset:16896\n" \
  "ds_read_b64_tr_b16 a[4:5], v226 offset:17408\n" \
  "global_load_lds_dwordx4 v229, s[54:55] offset:3072\n" \
  "v_add_u32_e32 v229, s65, v229\n" \
  "v_mfma_f32_32x32x16_bf16 v[176:191], a[20:23], v[44:47], v[176:191]\n" \
  "ds_read_b64_tr_b16 a[6:7], v226 offset:17920\n" \
  "ds_read_b64_tr_b16 a[8:9], v227 offset:16384\n" \
  "ds_read_b64_tr_b16 a[10:11], v227 offset:16896\n" \
  "v_mul_f32_e32 v237, v105, v105\n" \
  "v_mul_f32_e32 v238, v106, v106\n" \
  "v_mfma_f32_32x32x16_bf16 v[192:207], a[24:27], v[44:47], v[192:207]\n" \
  "ds_read_b64_tr_b16 a[12:13], v227 offset:17408\n" \
  "ds_read_b64_tr_b16 a[14:15], v227 offset:17920\n" \
  "v_mul_f32_e32 v239, v107, v107\n" \
  "v_fma_f32 v236, v236, v232, s77\n" \
  "v_fma_f32 v237, v237, v232, s77\n" \
  "v_mfma_f32_32x32x16_bf16 v[208:223], a[28:31], v[44:47], v[208:223]\n" \
  "v_fma_f32 v238, v238, v232, s77\n" \
  "v_fma_f32 v239, v239, v232, s77\n" \
  "s_waitcnt vmcnt(13) lgkmcnt(0)\n" \
  "s_barrier\n" \
  "v_mfma_f32_32x32x16_bf16 v[160:175], a[0:3], v[48:51], v[160:175]\n" \
  "s_add_u32 m0, s67, 32768\n" \
  "ds_read_b64_tr_b16 a[16:17], v226 offset:20480\n" \
  "ds_read_b64_tr_b16 a[18:19], v226 offset:20992\n" \
  "ds_read_b64_tr_b16 a[20:21], v226 offset:21504\n" \
  "global_load_lds_dwordx4 v229, s[48:49] offset:0\n" \
  "v_mfma_f32_32x32x16_bf16 v[176:191], a[4:7], v[48:51], v[176:191]\n" \
  "ds_read_b64_tr_b16 a[22:23], v226 offset:22016\n" \
  "ds_read_b64_tr_b16 a[24:25], v227 offset:20480\n" \
  "ds_read_b64_tr_b16 a[26:27], v227 offset:20992\n" \
  "v_mul_f32_e32 v236, v236, v104\n" \
  "v_mul_f32_e32 v237, v237, v105\n" \
  "v_mfma_f32_32x32x16_bf16 v[192:207], a[8:11], v[48:51], v[192:207]\n" \
  "ds_read_b64_tr_b16 a[28:29], v227 offset:21504\n" \
  "ds_read_b64_tr_b16 a[30:31], v227 offset:22016\n" \
  "v_mul_f32_e32 v238, v238, v106\n" \
  "v_mul_f32_e32 v239, v239, v107\n" \
  "v_exp_f32_e32 v236, v236\n" \
  "v_mfma_f32_32x32x16_bf16 v[208:223], a[12:15], v[48:51], v[208:223]\n" \
  "v_mul_f32_e32 v104, v104, v120\n" \
  "v_exp_f32_e32 v237, v237\n" \
  "s_waitcnt lgkmcnt(0)\n" \
  "v_mfma_f32_32x32x16_bf16 v[160:175], a[16:19], v[52:55], v[160:175]\n" \
  "ds_read_b64_tr_b16 a[0:1], v226 offset:24576\n" \
  "ds_read_b64_tr_b16 a[2:3], v226 offset:25088\n" \
  "ds_read_b64_tr_b16 a[4:5], v226 offset:25600\n" \
  "global_load_lds_dwordx4 v229, s[50:51] offset:1024\n" \
  "v_mul_f32_e32 v105, v105, v121\n" \
  "v_mfma_f32_32x32x16_bf16 v[176:191], a[20:23], v[52:55], v[176:191]\n" \
  "ds_read_b64_tr_b16 a[6:7], v226 offset:26112\n" \
  "ds_read_b64_tr_b16 a[8:9], v227 offset:24576\n" \
  "ds_read_b64_tr_b16 a[10:11], v227 offset:25088\n" \
  "v_exp_f32_e32 v238, v238\n" \
  "v_mul_f32_e32 v106, v106, v122\n" \
  "v_mfma_f32_32x32x16_bf16 v[192:207], a[24:27], v[52:55], v[192:207]\n" \
  "ds_read_b64_tr_b16 a[12:13], v227 offset:25600\n" \
  "ds_read_b64_tr_b16 a[14:15], v227 offset:26112\n" \
  "v_exp_f32_e32 v239, v239\n" \
  "v_mul_f32_e32 v107, v107, v123\n" \
  "v_add_f32_e32 v236, 1.0, v236\n" \
  "v_mfma_f32_32x32x16_bf16 v[208:223], a[28:31], v[52:55], v[208:223]\n" \
  "v_add_f32_e32 v237, 1.0, v237\n" \
  "v_rcp_f32_e32 v236, v236\n" \
  "s_waitcnt lgkmcnt(0)\n" \
  "v_mfma_f32_32x32x16_bf16 v[160:175], a[0:3], v[56:59], v[160:175]\n" \
  "ds_read_b64_tr_b16 a[16:17], v226 offset:28672\n" \
  "ds_read_b64_tr_b16 a[18:19], v226 offset:29184\n" \
  "ds_read_b64_tr_b16 a[20:21], v226 offset:29696\n" \
  "global_load_lds_dwordx4 v229, s[52:53] offset:2048\n" \
  "v_mfma_f32_32x32x16_bf16 v[176:191], a[4:7], v[56:59], v[176:191]\n" \
  "ds_read_b64_tr_b16 a[22:23], v226 offset:30208\n" \
  "ds_read_b64_tr_b16 a[24:25], v227 offset:28672\n" \
  "ds_read_b64_tr_b16 a[26:27], v227 offset:29184\n" \
  "v_add_f32_e32 v238, 1.0, v238\n" \
  "v_mfma_f32_32x32x16_bf16 v[192:207], a[8:11], v[56:59], v[192:207]\n" \
  "ds_read_b64_tr_b16 a[28:29], v227 offset:29696\n" \
  "ds_read_b64_tr_b16 a[30:31], v227 offset:30208\n" \
  "v_rcp_f32_e32 v237, v237\n" \
  "v_add_f32_e32 v239, 1.0, v239\n" \
  "v_mfma_f32_32x32x16_bf16 v[208:223], a[12:15], v[56:59], v[208:223]\n" \
  "v_rcp_f32_e32 v238, v238\n" \
  "v_mul_f32_e32 v104, v104, v236\n" \
  "v_rcp_f32_e32 v239, v239\n" \
  "v_mul_f32_e32 v105, v105, v237\n" \
  "s_waitcnt lgkmcnt(0)\n" \
  "v_mfma_f32_32x32x16_bf16 v[160:175], a[16:19], v[60:63], v[160:175]\n" \
  "ds_read_b64_tr_b16 a[0:1], v226 offset:32768\n" \
  "ds_read_b64_tr_b16 a[2:3], v226 offset:33280\n" \
  "ds_read_b64_tr_b16 a[4:5], v226 offset:33792\n" \
  "global_load_lds_dwordx4 v229, s[54:55] offset:3072\n" \
  "v_add_u32_e32 v229, s65, v229\n" \
  "v_mfma_f32_32x32x16_bf16 v[176:191], a[20:23], v[60:63], v[176:191]\n" \
  "ds_read_b64_tr_b16 a[6:7], v226 offset:34304\n" \
  "ds_read_b64_tr_b16 a[8:9], v227 offset:32768\n" \
  "ds_read_b64_tr_b16 a[10:11], v227 offset:33280\n" \
  "v_mul_f32_e32 v106, v106, v238\n" \
  "v_mul_f32_e32 v107, v107, v239\n" \
  "v_mfma_f32_32x32x16_bf16 v[192:207], a[24:27], v[60:63], v[192:207]\n" \
  "ds_read_b64_tr_b16 a[12:13], v227 offset:33792\n" \
  "ds_read_b64_tr_b16 a[14:15], v227 offset:34304\n" \
  "v_cvt_pk_bf16_f32 v116, v104, v105\n" \
  "v_cvt_pk_bf16_f32 v117, v106, v107\n" \
  "v_mul_f32_e32 v240, v108, v108\n" \
  "v_mfma_f32_32x32x16_bf16 v[208:223], a[28:31], v[60:63], v[208:223]\n" \
  "v_mul_f32_e32 v241, v109, v109\n" \
  "v_mul_f32_e32 v242, v110, v110\n" \
  "s_waitcnt vmcnt(13) lgkmcnt(0)\n" \
  "s_barrier\n" \
  "v_mfma_f32_32x32x16_bf16 v[160:175], a[0:3], v[64:67], v[160:175]\n" \
  "s_add_u32 m0, s67, 49152\n" \
  "ds_read_b64_tr_b16 a[16:17], v226 offset:36864\n" \
  "ds_read_b64_tr_b16 a[18:19], v226 offset:37376\n" \
  "ds_read_b64_tr_b16 a[20:21], v226 offset:37888\n" \
  "global_load_lds_dwordx4 v229, s[48:49] offset:0\n" \
  "v_mfma_f32_32x32x16_bf16 v[176:191], a[4:7], v[64:67], v[176:191]\n" \
  "ds_read_b64_tr_b16 a[22:23], v226 offset:38400\n" \
  "ds_read_b64_tr_b16 a[24:25], v227 offset:36864\n" \
  "ds_read_b64_tr_b16 a[26:27], v227 offset:37376\n" \
  "v_mul_f32_e32 v243, v111, v111\n" \
  "v_fma_f32 v240, v240, v232, s77\n" \
  "v_mfma_f32_32x32x16_bf16 v[192:207], a[8:11], v[64:67], v[192:207]\n" \
  "ds_read_b64_tr_b16 a[28:29], v227 offset:37888\n" \
  "ds_read_b64_tr_b16 a[30:31], v227 offset:38400\n" \
  "v_fma_f32 v241, v241, v232, s77\n" \
  "v_fma_f32 v242, v242, v232, s77\n" \
  "v_fma_f32 v243, v243, v232, s77\n" \
  "v_mfma_f32_32x32x16_bf16 v[208:223], a[12:15], v[64:67], v[208:223]\n" \
  "v_mul_f32_e32 v240, v240, v108\n" \
  "v_mul_f32_e32 v241, v241, v109\n" \
  "s_waitcnt lgkmcnt(0)\n" \
  "v_mfma_f32_32x32x16_bf16 v[160:175], a[16:19], v[68:71], v[160:175]\n" \
  "ds_read_b64_tr_b16 a[0:1], v226 offset:40960\n" \
  "ds_read_b64_tr_b16 a[2:3], v226 offset:41472\n" \
  "ds_read_b64_tr_b16 a[4:5], v226 offset:41984\n" \
  "global_load_lds_dwordx4 v229, s[50:51] offset:1024\n" \
  "v_mfma_f32_32x32x16_bf16 v[176:191], a[20:23], v[68:71], v[176:191]\n" \
  "ds_read_b64_tr_b16 a[6:7], v226 offset:42496\n" \
  "ds_read_b64_tr_b16 a[8:9], v227 offset:40960\n" \
  "ds_read_b64_tr_b16 a[10:11], v227 offset:41472\n" \
  "v_mul_f32_e32 v242, v242, v110\n" \
  "v_mfma_f32_32x32x16_bf16 v[192:207], a[24:27], v[68:71], v[192:207]\n" \
  "ds_read_b64_tr_b16 a[12:13], v227 offset:41984\n" \
  "ds_read_b64_tr_b16 a[14:15], v227 offset:42496\n" \
  "v_mul_f32_e32 v243, v243, v111\n" \
  "v_exp_f32_e32 v240, v240\n" \
  "v_mfma_f32_32x32x16_bf16 v[208:223], a[28:31], v[68:71], v[208:223]\n" \
  "v_mul_f32_e32 v108, v108, v124\n" \
  "v_exp_f32_e32 v241, v241\n" \
  "v_mul_f32_e32 v109, v109, v125\n" \
  "v_exp_f32_e32 v242, v242\n" \
  "s_waitcnt lgkmcnt(0)\n" \
  "v_mfma_f32_32x32x16_bf16 v[160:175], a[0:3], v[72:75], v[160:175]\n" \
  "ds_read_b64_tr_b16 a[16:17], v226 offset:45056\n" \
  "ds_read_b64_tr_b16 a[18:19], v226 offset:45568\n" \
  "ds_read_b64_tr_b16 a[20:21], v226 offset:46080\n" \
  "global_load_lds_dwordx4 v229, s[52:53] offset:2048\n" \
  "v_mfma_f32_32x32x16_bf16 v[176:191], a[4:7], v[72:75], v[176:191]\n" \
  "ds_read_b64_tr_b16 a[22:23], v226 offset:46592\n" \
  "ds_read_b64_tr_b16 a[24:25], v227 offset:45056\n" \
  "ds_read_b64_tr_b16 a[26:27], v227 offset:45568\n" \
  "v_mul_f32_e32 v110, v110, v126\n" \
  "v_mfma_f32_32x32x16_bf16 v[192:207], a[8:11], v[72:75], v[192:207]\n" \
  "ds_read_b64_tr_b16 a[28:29], v227 offset:46080\n" \
  "ds_read_b64_tr_b16 a[30:31], v227 offset:46592\n" \
  "v_exp_f32_e32 v243, v243\n" \
  "v_mul_f32_e32 v111, v111, v127\n" \
  "v_mfma_f32_32x32x16_bf16 v[208:223], a[12:15], v[72:75], v[208:223]\n" \
  "v_add_f32_e32 v240, 1.0, v240\n" \
  "v_add_f32_e32 v241, 1.0, v241\n" \
  "v_rcp_f32_e32 v240, v240\n" \
  "v_add_f32_e32 v242, 1.0, v242\n" \
  "s_waitcnt lgkmcnt(0)\n" \
  "v_mfma_f32_32x32x16_bf16 v[160:175], a[16:19], v[76:79], v[160:175]\n" \
  "ds_read_b64_tr_b16 a[0:1], v226 offset:49152\n" \
  "ds_read_b64_tr_b16 a[2:3], v226 offset:49664\n" \
  "ds_read_b64_tr_b16 a[4:5], v226 offset:50176\n" \
  "global_load_lds_dwordx4 v229, s[54:55] offset:3072\n" \
  "v_add_u32_e32 v229, s65, v229\n" \
  "v_mfma_f32_32x32x16_bf16 v[176:191], a[20:23], v[76:79], v[176:191]\n" \
  "ds_read_b64_tr_b16 a[6:7], v226 offset:50688\n" \
  "ds_read_b64_tr_b16 a[8:9], v227 offset:49152\n" \
  "ds_read_b64_tr_b16 a[10:11], v227 offset:49664\n" \
  "v_rcp_f32_e32 v241, v241\n" \
  "v_add_f32_e32 v243, 1.0, v243\n" \
  "v_mfma_f32_32x32x16_bf16 v[192:207], a[24:27], v[76:79], v[192:207]\n" \
  "ds_read_b64_tr_b16 a[12:13], v227 offset:50176\n" \
  "ds_read_b64_tr_b16 a[14:15], v227 offset:50688\n" \
  "v_rcp_f32_e32 v242, v242\n" \
  "v_mul_f32_e32 v108, v108, v240\n" \
  "v_rcp_f32_e32 v243, v243\n" \
  "v_mfma_f32_32x32x16_bf16 v[208:223], a[28:31], v[76:79], v[208:223]\n" \
  "v_mul_f32_e32 v109, v109, v241\n" \
  "v_mul_f32_e32 v110, v110, v242\n" \
  "s_waitcnt vmcnt(13) lgkmcnt(0)\n" \
  "s_barrier\n" \
  "v_mfma_f32_32x32x16_bf16 v[160:175], a[0:3], v[80:83], v[160:175]\n" \
  "s_add_u32 m0, s67, 65536\n" \
  "ds_read_b64_tr_b16 a[16:17], v226 offset:53248\n" \
  "ds_read_b64_tr_b16 a[18:19], v226 offset:53760\n" \
  "ds_read_b64_tr_b16 a[20:21], v226 offset:54272\n" \
  "global_load_lds_dwordx4 v230, s[56:57] offset:0\n" \
  "v_mfma_f32_32x32x16_bf16 v[176:191], a[4:7], v[80:83], v[176:191]\n" \
  "ds_read_b64_tr_b16 a[22:23], v226 offset:54784\n" \
  "ds_read_b64_tr_b16 a[24:25], v227 offset:53248\n" \
  "ds_read_b64_tr_b16 a[26:27], v227 offset:53760\n" \
  "v_mul_f32_e32 v111, v111, v243\n" \
  "v_cvt_pk_bf16_f32 v118, v108, v109\n" \
  "v_mfma_f32_32x32x16_bf16 v[192:207], a[8:11], v[80:83], v[192:207]\n" \
  "ds_read_b64_tr_b16 a[28:29], v227 offset:54272\n" \
  "ds_read_b64_tr_b16 a[30:31], v227 offset:54784\n" \
  "v_cvt_pk_bf16_f32 v119, v110, v111\n" \
  "global_store_dwordx4 v231, v[116:119], s[72:73] offset:1024 sc1\n" \
  "v_mul_f32_e32 v236, v128, v128\n" \
  "v_mfma_f32_32x32x16_bf16 v[208:223], a[12:15], v[80:83], v[208:223]\n" \
  "v_mul_f32_e32 v237, v129, v129\n" \
  "v_mul_f32_e32 v238, v130, v130\n" \
  "s_waitcnt lgkmcnt(0)\n" \
  "v_mfma_f32_32x32x16_bf16 v[160:175], a[16:19], v[84:87], v[160:175]\n" \
  "ds_read_b64_tr_b16 a[0:1], v226 offset:57344\n" \
  "ds_read_b64_tr_b16 a[2:3], v226 offset:57856\n" \
  "ds_read_b64_tr_b16 a[4:5], v226 offset:58368\n" \
  "global_load_lds_dwordx4 v230, s[58:59] offset:1024\n" \
  "v_mfma_f32_32x32x16_bf16 v[176:191], a[20:23], v[84:87], v[176:191]\n" \
  "ds_read_b64_tr_b16 a[6:7], v226 offset:58880\n" \
  "ds_read_b64_tr_b16 a[8:9], v227 offset:57344\n" \
  "ds_read_b64_tr_b16 a[10:11], v227 offset:57856\n" \
  "v_mul_f32_e32 v239, v131, v131\n" \
  "v_mfma_f32_32x32x16_bf16 v[192:207], a[24:27], v[84:87], v[192:207]\n" \
  "ds_read_b64_tr_b16 a[12:13], v227 offset:58368\n" \
  "ds_read_b64_tr_b16 a[14:15], v227 offset:58880\n" \
  "v_fma_f32 v236, v236, v232, s77\n" \
  "v_fma_f32 v237, v237, v232, s77\n" \
  "v_mfma_f32_32x32x16_bf16 v[208:223], a[28:31], v[84:87], v[208:223]\n" \
  "v_fma_f32 v238, v238, v232, s77\n" \
  "v_fma_f32 v239, v239, v232, s77\n" \
  "v_mul_f32_e32 v236, v236, v128\n" \
  "v_mul_f32_e32 v237, v237, v129\n" \
  "s_waitcnt lgkmcnt(0)\n" \
  "v_mfma_f32_32x32x16_bf16 v[160:175], a[0:3], v[88:91], v[160:175]\n" \
  "ds_read_b64_tr_b16 a[16:17], v226 offset:61440\n" \
  "ds_read_b64_tr_b16 a[18:19], v226 offset:61952\n" \
  "ds_read_b64_tr_b16 a[20:21], v226 offset:62464\n" \
  "global_load_lds_dwordx4 v230, s[60:61] offset:2048\n" \
  "v_mfma_f32_32x32x16_bf16 v[176:191], a[4:7], v[88:91], v[176:191]\n" \
  "ds_read_b64_tr_b16 a[22:23], v226 offset:62976\n" \
  "ds_read_b64_tr_b16 a[24:25], v227 offset:61440\n" \
  "ds_read_b64_tr_b16 a[26:27], v227 offset:61952\n" \
  "v_mul_f32_e32 v238, v238, v130\n" \
  "v_mfma_f32_32x32x16_bf16 v[192:207], a[8:11], v[88:91], v[192:207]\n" \
  "ds_read_b64_tr_b16 a[28:29], v227 offset:62464\n" \
  "ds_read_b64_tr_b16 a[30:31], v227 offset:62976\n" \
  "v_mul_f32_e32 v239, v239, v131\n" \
  "v_exp_f32_e32 v236, v236\n" \
  "v_mfma_f32_32x32x16_bf16 v[208:223], a[12:15], v[88:91], v[208:223]\n" \
  "v_mul_f32_e32 v128, v128, v144\n" \
  "v_exp_f32_e32 v237, v237\n" \
  "v_mul_f32_e32 v129, v129, v145\n" \
  "v_exp_f32_e32 v238, v238\n" \
  "s_waitcnt lgkmcnt(0)\n" \
  "v_mfma_f32_32x32x16_bf16 v[160:175], a[16:19], v[92:95], v[160:175]\n" \
  "ds_read_b64_tr_b16 a[0:1], v224 offset:0\n" \
  "ds_read_b64_tr_b16 a[2:3], v224 offset:512\n" \
  "ds_read_b64_tr_b16 a[4:5], v224 offset:1024\n" \
  "global_load_lds_dwordx4 v230, s[62:63] offset:3072\n" \
  "v_add_u32_e32 v230, s66, v230\n" \
  "v_mfma_f32_32x32x16_bf16 v[176:191], a[20:23], v[92:95], v[176:191]\n" \
  "ds_read_b64_tr_b16 a[6:7], v224 offset:1536\n" \
  "ds_read_b64_tr_b16 a[8:9], v225 offset:0\n" \
  "ds_read_b64_tr_b16 a[10:11], v225 offset:512\n" \
  "v_mul_f32_e32 v130, v130, v146\n" \
  "v_exp_f32_e32 v239, v239\n" \
  "v_mfma_f32_32x32x16_bf16 v[192:207], a[24:27], v[92:95], v[192:207]\n" \
  "ds_read_b64_tr_b16 a[12:13], v225 offset:1024\n" \
  "ds_read_b64_tr_b16 a[14:15], v225 offset:1536\n" \
  "v_mul_f32_e32 v131, v131, v147\n" \
  "v_add_f32_e32 v236, 1.0, v236\n" \
  "v_add_f32_e32 v237, 1.0, v237\n" \
  "v_mfma_f32_32x32x16_bf16 v[208:223], a[28:31], v[92:95], v[208:223]\n" \
  "v_rcp_f32_e32 v236, v236\n" \
  "v_add_f32_e32 v238, 1.0, v238\n" \
  "s_waitcnt vmcnt(13) lgkmcnt(0)\n" \
  "s_barrier\n" \
  "v_mfma_f32_32x32x16_bf16 v[160:175], a[0:3], a[32:35], v[160:175]\n" \
  "s_add_u32 m0, s67, 81920\n" \
  "ds_read_b64_tr_b16 a[16:17], v224 offset:4096\n" \
  "ds_read_b64_tr_b16 a[18:19], v224 offset:4608\n" \
  "ds_read_b64_tr_b16 a[20:21], v224 offset:5120\n" \
  "global_load_lds_dwordx4 v230, s[56:57] offset:0\n" \
  "v_mfma_f32_32x32x16_bf16 v[176:191], a[4:7], a[32:35], v[176:191]\n" \
  "ds_read_b64_tr_b16 a[22:23], v224 offset:5632\n" \
  "ds_read_b64_tr_b16 a[24:25], v225 offset:4096\n" \
  "ds_read_b64_tr_b16 a[26:27], v225 offset:4608\n" \
  "v_rcp_f32_e32 v237, v237\n" \
  "v_add_f32_e32 v239, 1.0, v239\n" \
  "v_mfma_f32_32x32x16_bf16 v[192:207], a[8:11], a[32:35], v[192:207]\n" \
  "ds_read_b64_tr_b16 a[28:29], v225 offset:5120\n" \
  "ds_read_b64_tr_b16 a[30:31], v225 offset:5632\n" \
  "v_rcp_f32_e32 v238, v238\n" \
  "v_mul_f32_e32 v128, v128, v236\n" \
  "v_rcp_f32_e32 v239, v239\n" \
  "v_mfma_f32_32x32x16_bf16 v[208:223], a[12:15], a[32:35], v[208:223]\n" \
  "v_mul_f32_e32 v129, v129, v237\n" \
  "v_mul_f32_e32 v130, v130, v238\n" \
  "s_waitcnt lgkmcnt(0)\n" \
  "v_mfma_f32_32x32x16_bf16 v[160:175], a[16:19], a[36:39], v[160:175]\n" \
  "ds_read_b64_tr_b16 a[0:1], v224 offset:8192\n" \
  "ds_read_b64_tr_b16 a[2:3], v224 offset:8704\n" \
  "ds_read_b64_tr_b16 a[4:5], v224 offset:9216\n" \
  "global_load_lds_dwordx4 v230, s[58:59] offset:1024\n" \
  "v_mfma_f32_32x32x16_bf16 v[176:191], a[20:23], a[36:39], v[176:191]\n" \
  "ds_read_b64_tr_b16 a[6:7], v224 offset:9728\n" \
  "ds_read_b64_tr_b16 a[8:9], v225 offset:8192\n" \
  "ds_read_b64_tr_b16 a[10:11], v225 offset:8704\n" \
  "v_mul_f32_e32 v131, v131, v239\n" \
  "v_mfma_f32_32x32x16_bf16 v[192:207], a[24:27], a[36:39], v[192:207]\n" \
  "ds_read_b64_tr_b16 a[12:13], v225 offset:9216\n" \
  "ds_read_b64_tr_b16 a[14:15], v225 offset:9728\n" \
  "v_cvt_pk_bf16_f32 v144, v128, v129\n" \
  "v_cvt_pk_bf16_f32 v145, v130, v131\n" \
  "v_mfma_f32_32x32x16_bf16 v[208:223], a[28:31], a[36:39], v[208:223]\n" \
  "v_mul_f32_e32 v240, v132, v132\n" \
  "v_mul_f32_e32 v241, v133, v133\n" \
  "v_mul_f32_e32 v242, v134, v134\n" \
  "v_mul_f32_e32 v243, v135, v135\n" \
  "s_waitcnt lgkmcnt(0)\n" \
  "v_mfma_f32_32x32x16_bf16 v[160:175], a[0:3], a[40:43], v[160:175]\n" \
  "ds_read_b64_tr_b16 a[16:17], v224 offset:12288\n" \
  "ds_read_b64_tr_b16 a[18:19], v224 offset:12800\n" \
  "ds_read_b64_tr_b16 a[20:21], v224 offset:13312\n" \
  "global_load_lds_dwordx4 v230, s[60:61] offset:2048\n" \
  "v_fma_f32 v240, v240, v232, s77\n" \
  "v_mfma_f32_32x32x16_bf16 v[176:191], a[4:7], a[40:43], v[176:191]\n" \
  "ds_read_b64_tr_b16 a[22:23], v224 offset:13824\n" \
  "ds_read_b64_tr_b16 a[24:25], v225 offset:12288\n" \
  "ds_read_b64_tr_b16 a[26:27], v225 offset:12800\n" \
  "v_fma_f32 v241, v241, v232, s77\n" \
  "v_fma_f32 v242, v242, v232, s77\n" \
  "v_mfma_f32_32x32x16_bf16 v[192:207], a[8:11], a[40:43], v[192:207]\n" \
  "ds_read_b64_tr_b16 a[28:29], v225 offset:13312\n" \
  "ds_read_b64_tr_b16 a[30:31], v225 offset:13824\n" \
  "v_fma_f32 v243, v243, v232, s77\n" \
  "v_mul_f32_e32 v240, v240, v132\n" \
  "v_mul_f32_e32 v241, v241, v133\n" \
  "v_mfma_f32_32x32x16_bf16 v[208:223], a[12:15], a[40:43], v[208:223]\n" \
  "v_mul_f32_e32 v242, v242, v134\n" \
  "v_mul_f32_e32 v243, v243, v135\n" \
  "s_waitcnt lgkmcnt(0)\n" \
  "v_mfma_f32_32x32x16_bf16 v[160:175], a[16:19], a[44:47], v[160:175]\n" \
  "ds_read_b64_tr_b16 a[0:1], v224 offset:16384\n" \
  "ds_read_b64_tr_b16 a[2:3], v224 offset:16896\n" \
  "ds_read_b64_tr_b16 a[4:5], v224 offset:17408\n" \
  "global_load_lds_dwordx4 v230, s[62:63] offset:3072\n" \
  "v_add_u32_e32 v230, s66, v230\n" \
  "v_mfma_f32_32x32x16_bf16 v[176:191], a[20:23], a[44:47], v[176:191]\n" \
  "ds_read_b64_tr_b16 a[6:7], v224 offset:17920\n" \
  "ds_read_b64_tr_b16 a[8:9], v225 offset:16384\n" \
  "ds_read_b64_tr_b16 a[10:11], v225 offset:16896\n" \
  "v_exp_f32_e32 v240, v240\n" \
  "v_mul_f32_e32 v132, v132, v148\n" \
  "v_mfma_f32_32x32x16_bf16 v[192:207], a[24:27], a[44:47], v[192:207]\n" \
  "ds_read_b64_tr_b16 a[12:13], v225 offset:17408\n" \
  "ds_read_b64_tr_b16 a[14:15], v225 offset:17920\n" \
  "v_exp_f32_e32 v241, v241\n" \
  "v_mul_f32_e32 v133, v133, v149\n" \
  "v_exp_f32_e32 v242, v242\n" \
  "v_mfma_f32_32x32x16_bf16 v[208:223], a[28:31], a[44:47], v[208:223]\n" \
  "v_mul_f32_e32 v134, v134, v150\n" \
  "v_exp_f32_e32 v243, v243\n" \
  "s_waitcnt vmcnt(13) lgkmcnt(0)\n" \
  "s_barrier\n" \
  "v_mfma_f32_32x32x16_bf16 v[160:175], a[0:3], a[48:51], v[160:175]\n" \
  "s_add_u32 m0, s67, 98304\n" \
  "ds_read_b64_tr_b16 a[16:17], v224 offset:20480\n" \
  "ds_read_b64_tr_b16 a[18:19], v224 offset:20992\n" \
  "ds_read_b64_tr_b16 a[20:21], v224 offset:21504\n" \
  "global_load_lds_dwordx4 v230, s[56:57] offset:0\n" \
  "v_mfma_f32_32x32x16_bf16 v[176:191], a[4:7], a[48:51], v[176:191]\n" \
  "ds_read_b64_tr_b16 a[22:23], v224 offset:22016\n" \
  "ds_read_b64_tr_b16 a[24:25], v225 offset:20480\n" \
  "ds_read_b64_tr_b16 a[26:27], v225 offset:20992\n" \
  "v_mul_f32_e32 v135, v135, v151\n" \
  "v_add_f32_e32 v240, 1.0, v240\n" \
  "v_mfma_f32_32x32x16_bf16 v[192:207], a[8:11], a[48:51], v[192:207]\n" \
  "ds_read_b64_tr_b16 a[28:29], v225 offset:21504\n" \
  "ds_read_b64_tr_b16 a[30:31], v225 offset:22016\n" \
  "v_add_f32_e32 v241, 1.0, v241\n" \
  "v_rcp_f32_e32 v240, v240\n" \
  "v_add_f32_e32 v242, 1.0, v242\n" \
  "v_mfma_f32_32x32x16_bf16 v[208:223], a[12:15], a[48:51], v[208:223]\n" \
  "v_rcp_f32_e32 v241, v241\n" \
  "v_add_f32_e32 v243, 1.0, v243\n" \
  "s_waitcnt lgkmcnt(0)\n" \
  "v_mfma_f32_32x32x16_bf16 v[160:175], a[16:19], a[52:55], v[160:175]\n" \
  "ds_read_b64_tr_b16 a[0:1], v224 offset:24576\n" \
  "ds_read_b64_tr_b16 a[2:3], v224 offset:25088\n" \
  "ds_read_b64_tr_b16 a[4:5], v224 offset:25600\n" \
  "global_load_lds_dwordx4 v230, s[58:59] offset:1024\n" \
  "v_mfma_f32_32x32x16_bf16 v[176:191], a[20:23], a[52:55], v[176:191]\n" \
  "ds_read_b64_tr_b16 a[6:7], v224 offset:26112\n" \
  "ds_read_b64_tr_b16 a[8:9], v225 offset:24576\n" \
  "ds_read_b64_tr_b16 a[10:11], v225 offset:25088\n" \
  "v_rcp_f32_e32 v242, v242\n" \
  "v_mfma_f32_32x32x16_bf16 v[192:207], a[24:27], a[52:55], v[192:207]\n" \
  "ds_read_b64_tr_b16 a[12:13], v225 offset:25600\n" \
  "ds_read_b64_tr_b16 a[14:15], v225 offset:26112\n" \
  "v_mul_f32_e32 v132, v132, v240\n" \
  "v_rcp_f32_e32 v243, v243\n" \
  "v_mfma_f32_32x32x16_bf16 v[208:223], a[28:31], a[52:55], v[208:223]\n" \
  "v_mul_f32_e32 v133, v133, v241\n" \
  "v_mul_f32_e32 v134, v134, v242\n" \
  "v_mul_f32_e32 v135, v135, v243\n" \
  "v_cvt_pk_bf16_f32 v146, v132, v133\n" \
  "s_waitcnt lgkmcnt(0)\n" \
  "v_mfma_f32_32x32x16_bf16 v[160:175], a[0:3], a[56:59], v[160:175]\n" \
  "ds_read_b64_tr_b16 a[16:17], v224 offset:28672\n" \
  "ds_read_b64_tr_b16 a[18:19], v224 offset:29184\n" \
  "ds_read_b64_tr_b16 a[20:21], v224 offset:29696\n" \
  "global_load_lds_dwordx4 v230, s[60:61] offset:2048\n" \
  "v_mfma_f32_32x32x16_bf16 v[176:191], a[4:7], a[56:59], v[176:191]\n" \
  "ds_read_b64_tr_b16 a[22:23], v224 offset:30208\n" \
  "ds_read_b64_tr_b16 a[24:25], v225 offset:28672\n" \
  "ds_read_b64_tr_b16 a[26:27], v225 offset:29184\n" \
  "v_cvt_pk_bf16_f32 v147, v134, v135\n" \
  "v_mfma_f32_32x32x16_bf16 v[192:207], a[8:11], a[56:59], v[192:207]\n" \
  "ds_read_b64_tr_b16 a[28:29], v225 offset:29696\n" \
  "ds_read_b64_tr_b16 a[30:31], v225 offset:30208\n" \
  "global_store_dwordx4 v231, v[144:147], s[72:73] offset:2048 sc1\n" \
  "v_mul_f32_e32 v236, v136, v136\n" \
  "v_mfma_f32_32x32x16_bf16 v[208:223], a[12:15], a[56:59], v[208:223]\n" \
  "v_mul_f32_e32 v237, v137, v137\n" \
  "v_mul_f32_e32 v238, v138, v138\n" \
  "v_mul_f32_e32 v239, v139, v139\n" \
  "v_fma_f32 v236, v236, v232, s77\n" \
  "s_waitcnt lgkmcnt(0)\n" \
  "v_mfma_f32_32x32x16_bf16 v[160:175], a[16:19], a[60:63], v[160:175]\n" \
  "ds_read_b64_tr_b16 a[0:1], v224 offset:32768\n" \
  "ds_read_b64_tr_b16 a[2:3], v224 offset:33280\n" \
  "ds_read_b64_tr_b16 a[4:5], v224 offset:33792\n" \
  "global_load_lds_dwordx4 v230, s[62:63] offset:3072\n" \
  "v_add_u32_e32 v230, s66, v230\n" \
  "v_mfma_f32_32x32x16_bf16 v[176:191], a[20:23], a[60:63], v[176:191]\n" \
  "ds_read_b64_tr_b16 a[6:7], v224 offset:34304\n" \
  "ds_read_b64_tr_b16 a[8:9], v225 offset:32768\n" \
  "ds_read_b64_tr_b16 a[10:11], v225 offset:33280\n" \
  "v_fma_f32 v237, v237, v232, s77\n" \
  "v_fma_f32 v238, v238, v232, s77\n" \
  "v_mfma_f32_32x32x16_bf16 v[192:207], a[24:27], a[60:63], v[192:207]\n" \
  "ds_read_b64_tr_b16 a[12:13], v225 offset:33792\n" \
  "ds_read_b64_tr_b16 a[14:15], v225 offset:34304\n" \
  "v_fma_f32 v239, v239, v232, s77\n" \
  "v_mul_f32_e32 v236, v236, v136\n" \
  "v_mul_f32_e32 v237, v237, v137\n" \
  "v_mfma_f32_32x32x16_bf16 v[208:223], a[28:31], a[60:63], v[208:223]\n" \
  "v_mul_f32_e32 v238, v238, v138\n" \
  "v_mul_f32_e32 v239, v239, v139\n" \
  "s_waitcnt vmcnt(14) lgkmcnt(0)\n" \
  "s_barrier\n" \
  "v_mfma_f32_32x32x16_bf16 v[160:175], a[0:3], a[64:67], v[160:175]\n" \
  "s_add_u32 m0, s67, 114688\n" \
  "ds_read_b64_tr_b16 a[16:17], v224 offset:36864\n" \
  "ds_read_b64_tr_b16 a[18:19], v224 offset:37376\n" \
  "ds_read_b64_tr_b16 a[20:21], v224 offset:37888\n" \
  "global_load_lds_dwordx4 v230, s[56:57] offset:0\n" \
  "v_mfma_f32_32x32x16_bf16 v[176:191], a[4:7], a[64:67], v[176:191]\n" \
  "ds_read_b64_tr_b16 a[22:23], v224 offset:38400\n" \
  "ds_read_b64_tr_b16 a[24:25], v225 offset:36864\n" \
  "ds_read_b64_tr_b16 a[26:27], v225 offset:37376\n" \
  "v_exp_f32_e32 v236, v236\n" \
  "v_mul_f32_e32 v136, v136, v152\n" \
  "v_mfma_f32_32x32x16_bf16 v[192:207], a[8:11], a[64:67], v[192:207]\n" \
  "ds_read_b64_tr_b16 a[28:29], v225 offset:37888\n" \
  "ds_read_b64_tr_b16 a[30:31], v225 offset:38400\n" \
  "v_exp_f32_e32 v237, v237\n" \
  "v_mul_f32_e32 v137, v137, v153\n" \
  "v_exp_f32_e32 v238, v238\n" \
  "v_mfma_f32_32x32x16_bf16 v[208:223], a[12:15], a[64:67], v[208:223]\n" \
  "v_mul_f32_e32 v138, v138, v154\n" \
  "v_exp_f32_e32 v239, v239\n" \
  "s_waitcnt lgkmcnt(0)\n" \
  "v_mfma_f32_32x32x16_bf16 v[160:175], a[16:19], a[68:71], v[160:175]\n" \
  "ds_read_b64_tr_b16 a[0:1], v224 offset:40960\n" \
  "ds_read_b64_tr_b16 a[2:3], v224 offset:41472\n" \
  "ds_read_b64_tr_b16 a[4:5], v224 offset:41984\n" \
  "global_load_lds_dwordx4 v230, s[58:59] offset:1024\n" \
  "v_mfma_f32_32x32x16_bf16 v[176:191], a[20:23], a[68:71], v[176:191]\n" \
  "ds_read_b64_tr_b16 a[6:7], v224 offset:42496\n" \
  "ds_read_b64_tr_b16 a[8:9], v225 offset:40960\n" \
  "ds_read_b64_tr_b16 a[10:11], v225 offset:41472\n" \
  "v_mul_f32_e32 v139, v139, v155\n" \
  "v_mfma_f32_32x32x16_bf16 v[192:207], a[24:27], a[68:71], v[192:207]\n" \
  "ds_read_b64_tr_b16 a[12:13], v225 offset:41984\n" \
  "ds_read_b64_tr_b16 a[14:15], v225 offset:42496\n" \
  "v_add_f32_e32 v236, 1.0, v236\n" \
  "v_add_f32_e32 v237, 1.0, v237\n" \
  "v_mfma_f32_32x32x16_bf16 v[208:223], a[28:31], a[68:71], v[208:223]\n" \
  "v_rcp_f32_e32 v236, v236\n" \
  "v_add_f32_e32 v238, 1.0, v238\n" \
  "v_rcp_f32_e32 v237, v237\n" \
  "v_add_f32_e32 v239, 1.0, v239\n" \
  "s_waitcnt lgkmcnt(0)\n" \
  "v_mfma_f32_32x32x16_bf16 v[160:175], a[0:3], a[72:75], v[160:175]\n" \
  "ds_read_b64_tr_b16 a[16:17], v224 offset:45056\n" \
  "ds_read_b64_tr_b16 a[18:19], v224 offset:45568\n" \
  "ds_read_b64_tr_b16 a[20:21], v224 offset:46080\n" \
  "global_load_lds_dwordx4 v230, s[60:61] offset:2048\n" \
  "v_mfma_f32_32x32x16_bf16 v[176:191], a[4:7], a[72:75], v[176:191]\n" \
  "ds_read_b64_tr_b16 a[22:23], v224 offset:46592\n" \
  "ds_read_b64_tr_b16 a[24:25], v225 offset:45056\n" \
  "ds_read_b64_tr_b16 a[26:27], v225 offset:45568\n" \
  "v_rcp_f32_e32 v238, v238\n" \
  "v_mfma_f32_32x32x16_bf16 v[192:207], a[8:11], a[72:75], v[192:207]\n" \
  "ds_read_b64_tr_b16 a[28:29], v225 offset:46080\n" \
  "ds_read_b64_tr_b16 a[30:31], v225 offset:46592\n" \
  "v_mul_f32_e32 v136, v136, v236\n" \
  "v_rcp_f32_e32 v239, v239\n" \
  "v_mfma_f32_32x32x16_bf16 v[208:223], a[12:15], a[72:75], v[208:223]\n" \
  "v_mul_f32_e32 v137, v137, v237\n" \
  "v_mul_f32_e32 v138, v138, v238\n" \
  "v_mul_f32_e32 v139, v139, v239\n" \
  "v_cvt_pk_bf16_f32 v148, v136, v137\n" \
  "s_waitcnt lgkmcnt(0)\n" \
  "v_mfma_f32_32x32x16_bf16 v[160:175], a[16:19], a[76:79], v[160:175]\n" \
  "ds_read_b64_tr_b16 a[0:1], v224 offset:49152\n" \
  "ds_read_b64_tr_b16 a[2:3], v224 offset:49664\n" \
  "ds_read_b64_tr_b16 a[4:5], v224 offset:50176\n" \
  "global_load_lds_dwordx4 v230, s[62:63] offset:3072\n" \
  "v_add_u32_e32 v230, s66, v230\n" \
  "v_mfma_f32_32x32x16_bf16 v[176:191], a[20:23], a[76:79], v[176:191]\n" \
  "ds_read_b64_tr_b16 a[6:7], v224 offset:50688\n" \
  "ds_read_b64_tr_b16 a[8:9], v225 offset:49152\n" \
  "ds_read_b64_tr_b16 a[10:11], v225 offset:49664\n" \
  "v_cvt_pk_bf16_f32 v149, v138, v139\n" \
  "v_mul_f32_e32 v240, v140, v140\n" \
  "v_mfma_f32_32x32x16_bf16 v[192:207], a[24:27], a[76:79], v[192:207]\n" \
  "ds_read_b64_tr_b16 a[12:13], v225 offset:50176\n" \
  "ds_read_b64_tr_b16 a[14:15], v225 offset:50688\n" \
  "v_mul_f32_e32 v241, v141, v141\n" \
  "v_mul_f32_e32 v242, v142, v142\n" \
  "v_mul_f32_e32 v243, v143, v143\n" \
  "v_mfma_f32_32x32x16_bf16 v[208:223], a[28:31], a[76:79], v[208:223]\n" \
  "v_fma_f32 v240, v240, v232, s77\n" \
  "v_fma_f32 v241, v241, v232, s77\n" \
  "s_waitcnt vmcnt(13) lgkmcnt(0)\n" \
  "s_barrier\n" \
  "v_mfma_f32_32x32x16_bf16 v[160:175], a[0:3], a[80:83], v[160:175]\n" \
  "s_add_u32 m0, s67, 0\n" \
  "ds_read_b64_tr_b16 a[16:17], v224 offset:53248\n" \
  "ds_read_b64_tr_b16 a[18:19], v224 offset:53760\n" \
  "ds_read_b64_tr_b16 a[20:21], v224 offset:54272\n" \
  "global_load_lds_dwordx4 v230, s[56:57] offset:0\n" \
  "v_mfma_f32_32x32x16_bf16 v[176:191], a[4:7], a[80:83], v[176:191]\n" \
  "ds_read_b64_tr_b16 a[22:23], v224 offset:54784\n" \
  "ds_read_b64_tr_b16 a[24:25], v225 offset:53248\n" \
  "ds_read_b64_tr_b16 a[26:27], v225 offset:53760\n" \
  "v_fma_f32 v242, v242, v232, s77\n" \
  "v_fma_f32 v243, v243, v232, s77\n" \
  "v_mfma_f32_32x32x16_bf16 v[192:207], a[8:11], a[80:83], v[192:207]\n" \
  "ds_read_b64_tr_b16 a[28:29], v225 offset:54272\n" \
  "ds_read_b64_tr_b16 a[30:31], v225 offset:54784\n" \
  "v_mul_f32_e32 v240, v240, v140\n" \
  "v_mul_f32_e32 v241, v241, v141\n" \
  "v_mul_f32_e32 v242, v242, v142\n" \
  "v_mfma_f32_32x32x16_bf16 v[208:223], a[12:15], a[80:83], v[208:223]\n" \
  "v_mul_f32_e32 v243, v243, v143\n" \
  "v_exp_f32_e32 v240, v240\n" \
  "s_waitcnt lgkmcnt(0)\n" \
  "v_mfma_f32_32x32x16_bf16 v[160:175], a[16:19], a[84:87], v[160:175]\n" \
  "ds_read_b64_tr_b16 a[0:1], v224 offset:57344\n" \
  "ds_read_b64_tr_b16 a[2:3], v224 offset:57856\n" \
  "ds_read_b64_tr_b16 a[4:5], v224 offset:58368\n" \
  "global_load_lds_dwordx4 v230, s[58:59] offset:1024\n" \
  "v_mfma_f32_32x32x16_bf16 v[176:191], a[20:23], a[84:87], v[176:191]\n" \
  "ds_read_b64_tr_b16 a[6:7], v224 offset:58880\n" \
  "ds_read_b64_tr_b16 a[8:9], v225 offset:57344\n" \
  "ds_read_b64_tr_b16 a[10:11], v225 offset:57856\n" \
  "v_mul_f32_e32 v140, v140, v156\n" \
  "v_mfma_f32_32x32x16_bf16 v[192:207], a[24:27], a[84:87], v[192:207]\n" \
  "ds_read_b64_tr_b16 a[12:13], v225 offset:58368\n" \
  "ds_read_b64_tr_b16 a[14:15], v225 offset:58880\n" \
  "v_exp_f32_e32 v241, v241\n" \
  "v_mul_f32_e32 v141, v141, v157\n" \
  "v_mfma_f32_32x32x16_bf16 v[208:223], a[28:31], a[84:87], v[208:223]\n" \
  "v_exp_f32_e32 v242, v242\n" \
  "v_mul_f32_e32 v142, v142, v158\n" \
  "v_exp_f32_e32 v243, v243\n" \
  "v_mul_f32_e32 v143, v143, v159\n" \
  "s_waitcnt lgkmcnt(0)\n" \
  "v_mfma_f32_32x32x16_bf16 v[160:175], a[0:3], a[88:91], v[160:175]\n" \
  "ds_read_b64_tr_b16 a[16:17], v224 offset:61440\n" \
  "ds_read_b64_tr_b16 a[18:19], v224 offset:61952\n" \
  "ds_read_b64_tr_b16 a[20:21], v224 offset:62464\n" \
  "global_load_lds_dwordx4 v230, s[60:61] offset:2048\n" \
  "v_mfma_f32_32x32x16_bf16 v[176:191], a[4:7], a[88:91], v[176:191]\n" \
  "ds_read_b64_tr_b16 a[22:23], v224 offset:62976\n" \
  "ds_read_b64_tr_b16 a[24:25], v225 offset:61440\n" \
  "ds_read_b64_tr_b16 a[26:27], v225 offset:61952\n" \
  "v_add_f32_e32 v240, 1.0, v240\n" \
  "v_mfma_f32_32x32x16_bf16 v[192:207], a[8:11], a[88:91], v[192:207]\n" \
  "ds_read_b64_tr_b16 a[28:29], v225 offset:62464\n" \
  "ds_read_b64_tr_b16 a[30:31], v225 offset:62976\n" \
  "v_add_f32_e32 v241, 1.0, v241\n" \
  "v_rcp_f32_e32 v240, v240\n" \
  "v_mfma_f32_32x32x16_bf16 v[208:223], a[12:15], a[88:91], v[208:223]\n" \
  "v_add_f32_e32 v242, 1.0, v242\n" \
  "v_rcp_f32_e32 v241, v241\n" \
  "v_add_f32_e32 v243, 1.0, v243\n" \
  "v_rcp_f32_e32 v242, v242\n" \
  "s_waitcnt lgkmcnt(0)\n" \
  "v_mfma_f32_32x32x16_bf16 v[160:175], a[16:19], a[92:95], v[160:175]\n" \
  "ds_read_b64_tr_b16 a[0:1], v226 offset:0\n" \
  "ds_read_b64_tr_b16 a[2:3], v226 offset:512\n" \
  "ds_read_b64_tr_b16 a[4:5], v226 offset:1024\n" \
  "global_load_lds_dwordx4 v230, s[62:63] offset:3072\n" \
  "v_add_u32_e32 v230, s66, v230\n" \
  "v_mfma_f32_32x32x16_bf16 v[176:191], a[20:23], a[92:95], v[176:191]\n" \
  "ds_read_b64_tr_b16 a[6:7], v226 offset:1536\n" \
  "ds_read_b64_tr_b16 a[8:9], v227 offset:0\n" \
  "ds_read_b64_tr_b16 a[10:11], v227 offset:512\n" \
  "v_mul_f32_e32 v140, v140, v240\n" \
  "v_rcp_f32_e32 v243, v243\n" \
  "v_mfma_f32_32x32x16_bf16 v[192:207], a[24:27], a[92:95], v[192:207]\n" \
  "ds_read_b64_tr_b16 a[12:13], v227 offset:1024\n" \
  "ds_read_b64_tr_b16 a[14:15], v227 offset:1536\n" \
  "v_mul_f32_e32 v141, v141, v241\n" \
  "v_mul_f32_e32 v142, v142, v242\n" \
  "v_mul_f32_e32 v143, v143, v243\n" \
  "v_mfma_f32_32x32x16_bf16 v[208:223], a[28:31], a[92:95], v[208:223]\n" \
  "v_cvt_pk_bf16_f32 v150, v140, v141\n" \
  "v_cvt_pk_bf16_f32 v151, v142, v143\n" \
  "global_store_dwordx4 v231, v[148:151], s[72:73] offset:3072 sc1\n" \
  "s_cmp_eq_u32 s70, 1\n" \
  "s_cbranch_scc1 1f\n" \
  "s_cmp_eq_u32 s70, 2\n" \
  "s_cbranch_scc1 2f\n" \
  "s_waitcnt vmcnt(14) lgkmcnt(0)\n" \
  "s_barrier\n" \
  "v_mfma_f32_32x32x16_bf16 v[96:111], a[0:3], v[0:3], 0\n" \
  "s_add_u32 m0, s67, 16384\n" \
  "ds_read_b64_tr_b16 a[16:17], v226 offset:4096\n" \
  "ds_read_b64_tr_b16 a[18:19], v226 offset:4608\n" \
  "ds_read_b64_tr_b16 a[20:21], v226 offset:5120\n" \
  "v_mfma_f32_32x32x16_bf16 v[112:127], a[4:7], v[0:3], 0\n" \
  "ds_read_b64_tr_b16 a[22:23], v226 offset:5632\n" \
  "ds_read_b64_tr_b16 a[24:25], v227 offset:4096\n" \
  "ds_read_b64_tr_b16 a[26:27], v227 offset:4608\n" \
  "v_mfma_f32_32x32x16_bf16 v[128:143], a[8:11], v[0:3], 0\n" \
  "ds_read_b64_tr_b16 a[28:29], v227 offset:5120\n" \
  "ds_read_b64_tr_b16 a[30:31], v227 offset:5632\n" \
  "global_load_lds_dwordx4 v230, s[56:57] offset:0\n" \
  "v_mfma_f32_32x32x16_bf16 v[144:159], a[12:15], v[0:3], 0\n" \
  "s_waitcnt lgkmcnt(0)\n" \
  "v_mfma_f32_32x32x16_bf16 v[96:111], a[16:19], v[4:7], v[96:111]\n" \
  "ds_read_b64_tr_b16 a[0:1], v226 offset:8192\n" \
  "ds_read_b64_tr_b16 a[2:3], v226 offset:8704\n" \
  "ds_read_b64_tr_b16 a[4:5], v226 offset:9216\n" \
  "global_load_lds_dwordx4 v230, s[58:59] offset:1024\n" \
  "v_mfma_f32_32x32x16_bf16 v[112:127], a[20:23], v[4:7], v[112:127]\n" \
  "ds_read_b64_tr_b16 a[6:7], v226 offset:9728\n" \
  "ds_read_b64_tr_b16 a[8:9], v227 offset:8192\n" \
  "ds_read_b64_tr_b16 a[10:11], v227 offset:8704\n" \
  "v_mul_f32_e32 v236, v160, v160\n" \
  "v_mfma_f32_32x32x16_bf16 v[128:143], a[24:27], v[4:7], v[128:143]\n" \
  "ds_read_b64_tr_b16 a[12:13], v227 offset:9216\n" \
  "ds_read_b64_tr_b16 a[14:15], v227 offset:9728\n" \
  "v_mul_f32_e32 v237, v161, v161\n" \
  "v_mul_f32_e32 v238, v162, v162\n" \
  "v_mfma_f32_32x32x16_bf16 v[144:159], a[28:31], v[4:7], v[144:159]\n" \
  "v_mul_f32_e32 v239, v163, v163\n" \
  "v_fma_f32 v236, v236, v232, s77\n" \
  "v_fma_f32 v237, v237, v232, s77\n" \
  "v_fma_f32 v238, v238, v232, s77\n" \
  "s_waitcnt lgkmcnt(0)\n" \
  "v_mfma_f32_32x32x16_bf16 v[96:111], a[0:3], v[8:11], v[96:111]\n" \
  "ds_read_b64_tr_b16 a[16:17], v226 offset:12288\n" \
  "ds_read_b64_tr_b16 a[18:19], v226 offset:12800\n" \
  "ds_read_b64_tr_b16 a[20:21], v226 offset:13312\n" \
  "global_load_lds_dwordx4 v230, s[60:61] offset:2048\n" \
  "v_mfma_f32_32x32x16_bf16 v[112:127], a[4:7], v[8:11], v[112:127]\n" \
  "ds_read_b64_tr_b16 a[22:23], v226 offset:13824\n" \
  "ds_read_b64_tr_b16 a[24:25], v227 offset:12288\n" \
  "ds_read_b64_tr_b16 a[26:27], v227 offset:12800\n" \
  "v_fma_f32 v239, v239, v232, s77\n" \
  "v_mfma_f32_32x32x16_bf16 v[128:143], a[8:11], v[8:11], v[128:143]\n" \
  "ds_read_b64_tr_b16 a[28:29], v227 offset:13312\n" \
  "ds_read_b64_tr_b16 a[30:31], v227 offset:13824\n" \
  "v_mul_f32_e32 v236, v236, v160\n" \
  "v_mul_f32_e32 v237, v237, v161\n" \
  "v_mfma_f32_32x32x16_bf16 v[144:159], a[12:15], v[8:11], v[144:159]\n" \
  "v_mul_f32_e32 v238, v238, v162\n" \
  "v_mul_f32_e32 v239, v239, v163\n" \
  "v_exp_f32_e32 v236, v236\n" \
  "v_mul_f32_e32 v160, v160, v176\n" \
  "s_waitcnt lgkmcnt(0)\n" \
  "v_mfma_f32_32x32x16_bf16 v[96:111], a[16:19], v[12:15], v[96:111]\n" \
  "ds_read_b64_tr_b16 a[0:1], v226 offset:16384\n" \
  "ds_read_b64_tr_b16 a[2:3], v226 offset:16896\n" \
  "ds_read_b64_tr_b16 a[4:5], v226 offset:17408\n" \
  "global_load_lds_dwordx4 v230, s[62:63] offset:3072\n" \
  "v_add_u32_e32 v230, s66, v230\n" \
  "v_mfma_f32_32x32x16_bf16 v[112:127], a[20:23], v[12:15], v[112:127]\n" \
  "ds_read_b64_tr_b16 a[6:7], v226 offset:17920\n" \
  "ds_read_b64_tr_b16 a[8:9], v227 offset:16384\n" \
  "ds_read_b64_tr_b16 a[10:11], v227 offset:16896\n" \
  "v_exp_f32_e32 v237, v237\n" \
  "v_mul_f32_e32 v161, v161, v177\n" \
  "v_mfma_f32_32x32x16_bf16 v[128:143], a[24:27], v[12:15], v[128:143]\n" \
  "ds_read_b64_tr_b16 a[12:13], v227 offset:17408\n" \
  "ds_read_b64_tr_b16 a[14:15], v227 offset:17920\n" \
  "v_exp_f32_e32 v238, v238\n" \
  "v_mul_f32_e32 v162, v162, v178\n" \
  "v_exp_f32_e32 v239, v239\n" \
  "v_mfma_f32_32x32x16_bf16 v[144:159], a[28:31], v[12:15], v[144:159]\n" \
  "v_mul_f32_e32 v163, v163, v179\n" \
  "v_add_f32_e32 v236, 1.0, v236\n" \
  "s_waitcnt vmcnt(13) lgkmcnt(0)\n" \
  "s_barrier\n" \
  "v_mfma_f32_32x32x16_bf16 v[96:111], a[0:3], v[16:19], v[96:111]\n" \
  "s_add_u32 m0, s67, 32768\n" \
  "ds_read_b64_tr_b16 a[16:17], v226 offset:20480\n" \
  "ds_read_b64_tr_b16 a[18:19], v226 offset:20992\n" \
  "ds_read_b64_tr_b16 a[20:21], v226 offset:21504\n" \
  "global_load_lds_dwordx4 v230, s[56:57] offset:0\n" \
  "v_mfma_f32_32x32x16_bf16 v[112:127], a[4:7], v[16:19], v[112:127]\n" \
  "ds_read_b64_tr_b16 a[22:23], v226 offset:22016\n" \
  "ds_read_b64_tr_b16 a[24:25], v227 offset:20480\n" \
  "ds_read_b64_tr_b16 a[26:27], v227 offset:20992\n" \
  "v_add_f32_e32 v237, 1.0, v237\n" \
  "v_rcp_f32_e32 v236, v236\n" \
  "v_mfma_f32_32x32x16_bf16 v[128:143], a[8:11], v[16:19], v[128:143]\n" \
  "ds_read_b64_tr_b16 a[28:29], v227 offset:21504\n" \
  "ds_read_b64_tr_b16 a[30:31], v227 offset:22016\n" \
  "v_add_f32_e32 v238, 1.0, v238\n" \
  "v_rcp_f32_e32 v237, v237\n" \
  "v_add_f32_e32 v239, 1.0, v239\n" \
  "v_mfma_f32_32x32x16_bf16 v[144:159], a[12:15], v[16:19], v[144:159]\n" \
  "v_rcp_f32_e32 v238, v238\n" \
  "v_mul_f32_e32 v160, v160, v236\n" \
  "s_waitcnt lgkmcnt(0)\n" \
  "v_mfma_f32_32x32x16_bf16 v[96:111], a[16:19], v[20:23], v[96:111]\n" \
  "ds_read_b64_tr_b16 a[0:1], v226 offset:24576\n" \
  "ds_read_b64_tr_b16 a[2:3], v226 offset:25088\n" \
  "ds_read_b64_tr_b16 a[4:5], v226 offset:25600\n" \
  "global_load_lds_dwordx4 v230, s[58:59] offset:1024\n" \
  "v_mfma_f32_32x32x16_bf16 v[112:127], a[20:23], v[20:23], v[112:127]\n" \
  "ds_read_b64_tr_b16 a[6:7], v226 offset:26112\n" \
  "ds_read_b64_tr_b16 a[8:9], v227 offset:24576\n" \
  "ds_read_b64_tr_b16 a[10:11], v227 offset:25088\n" \
  "v_rcp_f32_e32 v239, v239\n" \
  "v_mfma_f32_32x32x16_bf16 v[128:143], a[24:27], v[20:23], v[128:143]\n" \
  "ds_read_b64_tr_b16 a[12:13], v227 offset:25600\n" \
  "ds_read_b64_tr_b16 a[14:15], v227 offset:26112\n" \
  "v_mul_f32_e32 v161, v161, v237\n" \
  "v_mul_f32_e32 v162, v162, v238\n" \
  "v_mfma_f32_32x32x16_bf16 v[144:159], a[28:31], v[20:23], v[144:159]\n" \
  "v_mul_f32_e32 v163, v163, v239\n" \
  "v_cvt_pk_bf16_f32 v176, v160, v161\n" \
  "v_cvt_pk_bf16_f32 v177, v162, v163\n" \
  "v_mul_f32_e32 v240, v164, v164\n" \
  "s_waitcnt lgkmcnt(0)\n" \
  "v_mfma_f32_32x32x16_bf16 v[96:111], a[0:3], v[24:27], v[96:111]\n" \
  "ds_read_b64_tr_b16 a[16:17], v226 offset:28672\n" \
  "ds_read_b64_tr_b16 a[18:19], v226 offset:29184\n" \
  "ds_read_b64_tr_b16 a[20:21], v226 offset:29696\n" \
  "global_load_lds_dwordx4 v230, s[60:61] offset:2048\n" \
  "v_mfma_f32_32x32x16_bf16 v[112:127], a[4:7], v[24:27], v[112:127]\n" \
  "ds_read_b64_tr_b16 a[22:23], v226 offset:30208\n" \
  "ds_read_b64_tr_b16 a[24:25], v227 offset:28672\n" \
  "ds_read_b64_tr_b16 a[26:27], v227 offset:29184\n" \
  "v_mul_f32_e32 v241, v165, v165\n" \
  "v_mfma_f32_32x32x16_bf16 v[128:143], a[8:11], v[24:27], v[128:143]\n" \
  "ds_read_b64_tr_b16 a[28:29], v227 offset:29696\n" \
  "ds_read_b64_tr_b16 a[30:31], v227 offset:30208\n" \
  "v_mul_f32_e32 v242, v166, v166\n" \
  "v_mul_f32_e32 v243, v167, v167\n" \
  "v_mfma_f32_32x32x16_bf16 v[144:159], a[12:15], v[24:27], v[144:159]\n" \
  "v_fma_f32 v240, v240, v232, s77\n" \
  "v_fma_f32 v241, v241, v232, s77\n" \
  "v_fma_f32 v242, v242, v232, s77\n" \
  "v_fma_f32 v243, v243, v232, s77\n" \
  "s_waitcnt lgkmcnt(0)\n" \
  "v_mfma_f32_32x32x16_bf16 v[96:111], a[16:19], v[28:31], v[96:111]\n" \
  "ds_read_b64_tr_b16 a[0:1], v226 offset:32768\n" \
  "ds_read_b64_tr_b16 a[2:3], v226 offset:33280\n" \
  "ds_read_b64_tr_b16 a[4:5], v226 offset:33792\n" \
  "global_load_lds_dwordx4 v230, s[62:63] offset:3072\n" \
  "v_add_u32_e32 v230, s66, v230\n" \
  "v_mfma_f32_32x32x16_bf16 v[112:127], a[20:23], v[28:31], v[112:127]\n" \
  "ds_read_b64_tr_b16 a[6:7], v226 offset:34304\n" \
  "ds_read_b64_tr_b16 a[8:9], v227 offset:32768\n" \
  "ds_read_b64_tr_b16 a[10:11], v227 offset:33280\n" \
  "v_mul_f32_e32 v240, v240, v164\n" \
  "v_mul_f32_e32 v241, v241, v165\n" \
  "v_mfma_f32_32x32x16_bf16 v[128:143], a[24:27], v[28:31], v[128:143]\n" \
  "ds_read_b64_tr_b16 a[12:13], v227 offset:33792\n" \
  "ds_read_b64_tr_b16 a[14:15], v227 offset:34304\n" \
  "v_mul_f32_e32 v242, v242, v166\n" \
  "v_mul_f32_e32 v243, v243, v167\n" \
  "v_exp_f32_e32 v240, v240\n" \
  "v_mfma_f32_32x32x16_bf16 v[144:159], a[28:31], v[28:31], v[144:159]\n" \
  "v_mul_f32_e32 v164, v164, v180\n" \
  "v_exp_f32_e32 v241, v241\n" \
  "s_waitcnt vmcnt(13) lgkmcnt(0)\n" \
  "s_barrier\n" \
  "v_mfma_f32_32x32x16_bf16 v[96:111], a[0:3], v[32:35], v[96:111]\n" \
  "s_add_u32 m0, s67, 49152\n" \
  "ds_read_b64_tr_b16 a[16:17], v226 offset:36864\n" \
  "ds_read_b64_tr_b16 a[18:19], v226 offset:37376\n" \
  "ds_read_b64_tr_b16 a[20:21], v226 offset:37888\n" \
  "global_load_lds_dwordx4 v230, s[56:57] offset:0\n" \
  "v_mfma_f32_32x32x16_bf16 v[112:127], a[4:7], v[32:35], v[112:127]\n" \
  "ds_read_b64_tr_b16 a[22:23], v226 offset:38400\n" \
  "ds_read_b64_tr_b16 a[24:25], v227 offset:36864\n" \
  "ds_read_b64_tr_b16 a[26:27], v227 offset:37376\n" \
  "v_mul_f32_e32 v165, v165, v181\n" \
  "v_exp_f32_e32 v242, v242\n" \
  "v_mfma_f32_32x32x16_bf16 v[128:143], a[8:11], v[32:35], v[128:143]\n" \
  "ds_read_b64_tr_b16 a[28:29], v227 offset:37888\n" \
  "ds_read_b64_tr_b16 a[30:31], v227 offset:38400\n" \
  "v_mul_f32_e32 v166, v166, v182\n" \
  "v_exp_f32_e32 v243, v243\n" \
  "v_mul_f32_e32 v167, v167, v183\n" \
  "v_mfma_f32_32x32x16_bf16 v[144:159], a[12:15], v[32:35], v[144:159]\n" \
  "v_add_f32_e32 v240, 1.0, v240\n" \
  "v_add_f32_e32 v241, 1.0, v241\n" \
  "s_waitcnt lgkmcnt(0)\n" \
  "v_mfma_f32_32x32x16_bf16 v[96:111], a[16:19], v[36:39], v[96:111]\n" \
  "ds_read_b64_tr_b16 a[0:1], v226 offset:40960\n" \
  "ds_read_b64_tr_b16 a[2:3], v226 offset:41472\n" \
  "ds_read_b64_tr_b16 a[4:5], v226 offset:41984\n" \
  "global_load_lds_dwordx4 v230, s[58:59] offset:1024\n" \
  "v_mfma_f32_32x32x16_bf16 v[112:127], a[20:23], v[36:39], v[112:127]\n" \
  "ds_read_b64_tr_b16 a[6:7], v226 offset:42496\n" \
  "ds_read_b64_tr_b16 a[8:9], v227 offset:40960\n" \
  "ds_read_b64_tr_b16 a[10:11], v227 offset:41472\n" \
  "v_rcp_f32_e32 v240, v240\n" \
  "v_mfma_f32_32x32x16_bf16 v[128:143], a[24:27], v[36:39], v[128:143]\n" \
  "ds_read_b64_tr_b16 a[12:13], v227 offset:41984\n" \
  "ds_read_b64_tr_b16 a[14:15], v227 offset:42496\n" \
  "v_add_f32_e32 v242, 1.0, v242\n" \
  "v_rcp_f32_e32 v241, v241\n" \
  "v_mfma_f32_32x32x16_bf16 v[144:159], a[28:31], v[36:39], v[144:159]\n" \
  "v_add_f32_e32 v243, 1.0, v243\n" \
  "v_rcp_f32_e32 v242, v242\n" \
  "v_mul_f32_e32 v164, v164, v240\n" \
  "v_rcp_f32_e32 v243, v243\n" \
  "s_waitcnt lgkmcnt(0)\n" \
  "v_mfma_f32_32x32x16_bf16 v[96:111], a[0:3], v[40:43], v[96:111]\n" \
  "ds_read_b64_tr_b16 a[16:17], v226 offset:45056\n" \
  "ds_read_b64_tr_b16 a[18:19], v226 offset:45568\n" \
  "ds_read_b64_tr_b16 a[20:21], v226 offset:46080\n" \
  "global_load_lds_dwordx4 v230, s[60:61] offset:2048\n" \
  "v_mfma_f32_32x32x16_bf16 v[112:127], a[4:7], v[40:43], v[112:127]\n" \
  "ds_read_b64_tr_b16 a[22:23], v226 offset:46592\n" \
  "ds_read_b64_tr_b16 a[24:25], v227 offset:45056\n" \
  "ds_read_b64_tr_b16 a[26:27], v227 offset:45568\n" \
  "v_mul_f32_e32 v165, v165, v241\n" \
  "v_mfma_f32_32x32x16_bf16 v[128:143], a[8:11], v[40:43], v[128:143]\n" \
  "ds_read_b64_tr_b16 a[28:29], v227 offset:46080\n" \
  "ds_read_b64_tr_b16 a[30:31], v227 offset:46592\n" \
  "v_mul_f32_e32 v166, v166, v242\n" \
  "v_mul_f32_e32 v167, v167, v243\n" \
  "v_mfma_f32_32x32x16_bf16 v[144:159], a[12:15], v[40:43], v[144:159]\n" \
  "v_cvt_pk_bf16_f32 v178, v164, v165\n" \
  "v_cvt_pk_bf16_f32 v179, v166, v167\n" \
  "global_store_dwordx4 v231, v[176:179], s[74:75] offset:0 sc1\n" \
  "v_mul_f32_e32 v236, v168, v168\n" \
  "s_waitcnt lgkmcnt(0)\n" \
  "v_mfma_f32_32x32x16_bf16 v[96:111], a[16:19], v[44:47], v[96:111]\n" \
  "ds_read_b64_tr_b16 a[0:1], v226 offset:49152\n" \
  "ds_read_b64_tr_b16 a[2:3], v226 offset:49664\n" \
  "ds_read_b64_tr_b16 a[4:5], v226 offset:50176\n" \
  "global_load_lds_dwordx4 v230, s[62:63] offset:3072\n" \
  "v_add_u32_e32 v230, s66, v230\n" \
  "v_mfma_f32_32x32x16_bf16 v[112:127], a[20:23], v[44:47], v[112:127]\n" \
  "ds_read_b64_tr_b16 a[6:7], v226 offset:50688\n" \
  "ds_read_b64_tr_b16 a[8:9], v227 offset:49152\n" \
  "ds_read_b64_tr_b16 a[10:11], v227 offset:49664\n" \
  "v_mul_f32_e32 v237, v169, v169\n" \
  "v_mul_f32_e32 v238, v170, v170\n" \
  "v_mfma_f32_32x32x16_bf16 v[128:143], a[24:27], v[44:47], v[128:143]\n" \
  "ds_read_b64_tr_b16 a[12:13], v227 offset:50176\n" \
  "ds_read_b64_tr_b16 a[14:15], v227 offset:50688\n" \
  "v_mul_f32_e32 v239, v171, v171\n" \
  "v_fma_f32 v236, v236, v232, s77\n" \
  "v_fma_f32 v237, v237, v232, s77\n" \
  "v_mfma_f32_32x32x16_bf16 v[144:159], a[28:31], v[44:47], v[144:159]\n" \
  "v_fma_f32 v238, v238, v232, s77\n" \
  "v_fma_f32 v239, v239, v232, s77\n" \
  "s_waitcnt vmcnt(14) lgkmcnt(0)\n" \
  "s_barrier\n" \
  "v_mfma_f32_32x32x16_bf16 v[96:111], a[0:3], v[48:51], v[96:111]\n" \
  "s_add_u32 m0, s67, 65536\n" \
  "ds_read_b64_tr_b16 a[16:17], v226 offset:53248\n" \
  "ds_read_b64_tr_b16 a[18:19], v226 offset:53760\n" \
  "ds_read_b64_tr_b16 a[20:21], v226 offset:54272\n" \
  "global_load_lds_dwordx4 v230, s[56:57] offset:0\n" \
  "v_mfma_f32_32x32x16_bf16 v[112:127], a[4:7], v[48:51], v[112:127]\n" \
  "ds_read_b64_tr_b16 a[22:23], v226 offset:54784\n" \
  "ds_read_b64_tr_b16 a[24:25], v227 offset:53248\n" \
  "ds_read_b64_tr_b16 a[26:27], v227 offset:53760\n" \
  "v_mul_f32_e32 v236, v236, v168\n" \
  "v_mul_f32_e32 v237, v237, v169\n" \
  "v_mfma_f32_32x32x16_bf16 v[128:143], a[8:11], v[48:51], v[128:143]\n" \
  "ds_read_b64_tr_b16 a[28:29], v227 offset:54272\n" \
  "ds_read_b64_tr_b16 a[30:31], v227 offset:54784\n" \
  "v_mul_f32_e32 v238, v238, v170\n" \
  "v_mul_f32_e32 v239, v239, v171\n" \
  "v_exp_f32_e32 v236, v236\n" \
  "v_mfma_f32_32x32x16_bf16 v[144:159], a[12:15], v[48:51], v[144:159]\n" \
  "v_mul_f32_e32 v168, v168, v184\n" \
  "v_exp_f32_e32 v237, v237\n" \
  "s_waitcnt lgkmcnt(0)\n" \
  "v_mfma_f32_32x32x16_bf16 v[96:111], a[16:19], v[52:55], v[96:111]\n" \
  "ds_read_b64_tr_b16 a[0:1], v226 offset:57344\n" \
  "ds_read_b64_tr_b16 a[2:3], v226 offset:57856\n" \
  "ds_read_b64_tr_b16 a[4:5], v226 offset:58368\n" \
  "global_load_lds_dwordx4 v230, s[58:59] offset:1024\n" \
  "v_mul_f32_e32 v169, v169, v185\n" \
  "v_mfma_f32_32x32x16_bf16 v[112:127], a[20:23], v[52:55], v[112:127]\n" \
  "ds_read_b64_tr_b16 a[6:7], v226 offset:58880\n" \
  "ds_read_b64_tr_b16 a[8:9], v227 offset:57344\n" \
  "ds_read_b64_tr_b16 a[10:11], v227 offset:57856\n" \
  "v_exp_f32_e32 v238, v238\n" \
  "v_mul_f32_e32 v170, v170, v186\n" \
  "v_mfma_f32_32x32x16_bf16 v[128:143], a[24:27], v[52:55], v[128:143]\n" \
  "ds_read_b64_tr_b16 a[12:13], v227 offset:58368\n" \
  "ds_read_b64_tr_b16 a[14:15], v227 offset:58880\n" \
  "v_exp_f32_e32 v239, v239\n" \
  "v_mul_f32_e32 v171, v171, v187\n" \
  "v_add_f32_e32 v236, 1.0, v236\n" \
  "v_mfma_f32_32x32x16_bf16 v[144:159], a[28:31], v[52:55], v[144:159]\n" \
  "v_add_f32_e32 v237, 1.0, v237\n" \
  "v_rcp_f32_e32 v236, v236\n" \
  "s_waitcnt lgkmcnt(0)\n" \
  "v_mfma_f32_32x32x16_bf16 v[96:111], a[0:3], v[56:59], v[96:111]\n" \
  "ds_read_b64_tr_b16 a[16:17], v226 offset:61440\n" \
  "ds_read_b64_tr_b16 a[18:19], v226 offset:61952\n" \
  "ds_read_b64_tr_b16 a[20:21], v226 offset:62464\n" \
  "global_load_lds_dwordx4 v230, s[60:61] offset:2048\n" \
  "v_mfma_f32_32x32x16_bf16 v[112:127], a[4:7], v[56:59], v[112:127]\n" \
  "ds_read_b64_tr_b16 a[22:23], v226 offset:62976\n" \
  "ds_read_b64_tr_b16 a[24:25], v227 offset:61440\n" \
  "ds_read_b64_tr_b16 a[26:27], v227 offset:61952\n" \
  "v_add_f32_e32 v238, 1.0, v238\n" \
  "v_mfma_f32_32x32x16_bf16 v[128:143], a[8:11], v[56:59], v[128:143]\n" \
  "ds_read_b64_tr_b16 a[28:29], v227 offset:62464\n" \
  "ds_read_b64_tr_b16 a[30:31], v227 offset:62976\n" \
  "v_rcp_f32_e32 v237, v237\n" \
  "v_add_f32_e32 v239, 1.0, v239\n" \
  "v_mfma_f32_32x32x16_bf16 v[144:159], a[12:15], v[56:59], v[144:159]\n" \
  "v_rcp_f32_e32 v238, v238\n" \
  "v_mul_f32_e32 v168, v168, v236\n" \
  "v_rcp_f32_e32 v239, v239\n" \
  "v_mul_f32_e32 v169, v169, v237\n" \
  "s_waitcnt lgkmcnt(0)\n" \
  "v_mfma_f32_32x32x16_bf16 v[96:111], a[16:19], v[60:63], v[96:111]\n" \
  "ds_read_b64_tr_b16 a[0:1], v224 offset:0\n" \
  "ds_read_b64_tr_b16 a[2:3], v224 offset:512\n" \
  "ds_read_b64_tr_b16 a[4:5], v224 offset:1024\n" \
  "global_load_lds_dwordx4 v230, s[62:63] offset:3072\n" \
  "v_add_u32_e32 v230, s66, v230\n" \
  "v_mfma_f32_32x32x16_bf16 v[112:127], a[20:23], v[60:63], v[112:127]\n" \
  "ds_read_b64_tr_b16 a[6:7], v224 offset:1536\n" \
  "ds_read_b64_tr_b16 a[8:9], v225 offset:0\n" \
  "ds_read_b64_tr_b16 a[10:11], v225 offset:512\n" \
  "v_mul_f32_e32 v170, v170, v238\n" \
  "v_mul_f32_e32 v171, v171, v239\n" \
  "v_mfma_f32_32x32x16_bf16 v[128:143], a[24:27], v[60:63], v[128:143]\n" \
  "ds_read_b64_tr_b16 a[12:13], v225 offset:1024\n" \
  "ds_read_b64_tr_b16 a[14:15], v225 offset:1536\n" \
  "v_cvt_pk_bf16_f32 v180, v168, v169\n" \
  "v_cvt_pk_bf16_f32 v181, v170, v171\n" \
  "v_mul_f32_e32 v240, v172, v172\n" \
  "v_mfma_f32_32x32x16_bf16 v[144:159], a[28:31], v[60:63], v[144:159]\n" \
  "v_mul_f32_e32 v241, v173, v173\n" \
  "v_mul_f32_e32 v242, v174, v174\n" \
  "s_waitcnt vmcnt(13) lgkmcnt(0)\n" \
  "s_barrier\n" \
  "v_mfma_f32_32x32x16_bf16 v[96:111], a[0:3], v[64:67], v[96:111]\n" \
  "s_add_u32 m0, s67, 81920\n" \
  "ds_read_b64_tr_b16 a[16:17], v224 offset:4096\n" \
  "ds_read_b64_tr_b16 a[18:19], v224 offset:4608\n" \
  "ds_read_b64_tr_b16 a[20:21], v224 offset:5120\n" \
  "global_load_lds_dwordx4 v230, s[56:57] offset:0\n" \
  "v_mfma_f32_32x32x16_bf16 v[112:127], a[4:7], v[64:67], v[112:127]\n" \
  "ds_read_b64_tr_b16 a[22:23], v224 offset:5632\n" \
  "ds_read_b64_tr_b16 a[24:25], v225 offset:4096\n" \
  "ds_read_b64_tr_b16 a[26:27], v225 offset:4608\n" \
  "v_mul_f32_e32 v243, v175, v175\n" \
  "v_fma_f32 v240, v240, v232, s77\n" \
  "v_mfma_f32_32x32x16_bf16 v[128:143], a[8:11], v[64:67], v[128:143]\n" \
  "ds_read_b64_tr_b16 a[28:29], v225 offset:5120\n" \
  "ds_read_b64_tr_b16 a[30:31], v225 offset:5632\n" \
  "v_fma_f32 v241, v241, v232, s77\n" \
  "v_fma_f32 v242, v242, v232, s77\n" \
  "v_fma_f32 v243, v243, v232, s77\n" \
  "v_mfma_f32_32x32x16_bf16 v[144:159], a[12:15], v[64:67], v[144:159]\n" \
  "v_mul_f32_e32 v240, v240, v172\n" \
  "v_mul_f32_e32 v241, v241, v173\n" \
  "s_waitcnt lgkmcnt(0)\n" \
  "v_mfma_f32_32x32x16_bf16 v[96:111], a[16:19], v[68:71], v[96:111]\n" \
  "ds_read_b64_tr_b16 a[0:1], v224 offset:8192\n" \
  "ds_read_b64_tr_b16 a[2:3], v224 offset:8704\n" \
  "ds_read_b64_tr_b16 a[4:5], v224 offset:9216\n" \
  "global_load_lds_dwordx4 v230, s[58:59] offset:1024\n" \
  "v_mfma_f32_32x32x16_bf16 v[112:127], a[20:23], v[68:71], v[112:127]\n" \
  "ds_read_b64_tr_b16 a[6:7], v224 offset:9728\n" \
  "ds_read_b64_tr_b16 a[8:9], v225 offset:8192\n" \
  "ds_read_b64_tr_b16 a[10:11], v225 offset:8704\n" \
  "v_mul_f32_e32 v242, v242, v174\n" \
  "v_mfma_f32_32x32x16_bf16 v[128:143], a[24:27], v[68:71], v[128:143]\n" \
  "ds_read_b64_tr_b16 a[12:13], v225 offset:9216\n" \
  "ds_read_b64_tr_b16 a[14:15], v225 offset:9728\n" \
  "v_mul_f32_e32 v243, v243, v175\n" \
  "v_exp_f32_e32 v240, v240\n" \
  "v_mfma_f32_32x32x16_bf16 v[144:159], a[28:31], v[68:71], v[144:159]\n" \
  "v_mul_f32_e32 v172, v172, v188\n" \
  "v_exp_f32_e32 v241, v241\n" \
  "v_mul_f32_e32 v173, v173, v189\n" \
  "v_exp_f32_e32 v242, v242\n" \
  "s_waitcnt lgkmcnt(0)\n" \
  "v_mfma_f32_32x32x16_bf16 v[96:111], a[0:3], v[72:75], v[96:111]\n" \
  "ds_read_b64_tr_b16 a[16:17], v224 offset:12288\n" \
  "ds_read_b64_tr_b16 a[18:19], v224 offset:12800\n" \
  "ds_read_b64_tr_b16 a[20:21], v224 offset:13312\n" \
  "global_load_lds_dwordx4 v230, s[60:61] offset:2048\n" \
  "v_mfma_f32_32x32x16_bf16 v[112:127], a[4:7], v[72:75], v[112:127]\n" \
  "ds_read_b64_tr_b16 a[22:23], v224 offset:13824\n" \
  "ds_read_b64_tr_b16 a[24:25], v225 offset:12288\n" \
  "ds_read_b64_tr_b16 a[26:27], v225 offset:12800\n" \
  "v_mul_f32_e32 v174, v174, v190\n" \
  "v_mfma_f32_32x32x16_bf16 v[128:143], a[8:11], v[72:75], v[128:143]\n" \
  "ds_read_b64_tr_b16 a[28:29], v225 offset:13312\n" \
  "ds_read_b64_tr_b16 a[30:31], v225 offset:13824\n" \
  "v_exp_f32_e32 v243, v243\n" \
  "v_mul_f32_e32 v175, v175, v191\n" \
  "v_mfma_f32_32x32x16_bf16 v[144:159], a[12:15], v[72:75], v[144:159]\n" \
  "v_add_f32_e32 v240, 1.0, v240\n" \
  "v_add_f32_e32 v241, 1.0, v241\n" \
  "v_rcp_f32_e32 v240, v240\n" \
  "v_add_f32_e32 v242, 1.0, v242\n" \
  "s_waitcnt lgkmcnt(0)\n" \
  "v_mfma_f32_32x32x16_bf16 v[96:111], a[16:19], v[76:79], v[96:111]\n" \
  "ds_read_b64_tr_b16 a[0:1], v224 offset:16384\n" \
  "ds_read_b64_tr_b16 a[2:3], v224 offset:16896\n" \
  "ds_read_b64_tr_b16 a[4:5], v224 offset:17408\n" \
  "global_load_lds_dwordx4 v230, s[62:63] offset:3072\n" \
  "v_add_u32_e32 v230, s66, v230\n" \
  "v_mfma_f32_32x32x16_bf16 v[112:127], a[20:23], v[76:79], v[112:127]\n" \
  "ds_read_b64_tr_b16 a[6:7], v224 offset:17920\n" \
  "ds_read_b64_tr_b16 a[8:9], v225 offset:16384\n" \
  "ds_read_b64_tr_b16 a[10:11], v225 offset:16896\n" \
  "v_rcp_f32_e32 v241, v241\n" \
  "v_add_f32_e32 v243, 1.0, v243\n" \
  "v_mfma_f32_32x32x16_bf16 v[128:143], a[24:27], v[76:79], v[128:143]\n" \
  "ds_read_b64_tr_b16 a[12:13], v225 offset:17408\n" \
  "ds_read_b64_tr_b16 a[14:15], v225 offset:17920\n" \
  "v_rcp_f32_e32 v242, v242\n" \
  "v_mul_f32_e32 v172, v172, v240\n" \
  "v_rcp_f32_e32 v243, v243\n" \
  "v_mfma_f32_32x32x16_bf16 v[144:159], a[28:31], v[76:79], v[144:159]\n" \
  "v_mul_f32_e32 v173, v173, v241\n" \
  "v_mul_f32_e32 v174, v174, v242\n" \
  "s_waitcnt vmcnt(13) lgkmcnt(0)\n" \
  "s_barrier\n" \
  "v_mfma_f32_32x32x16_bf16 v[96:111], a[0:3], v[80:83], v[96:111]\n" \
  "ds_read_b64_tr_b16 a[16:17], v224 offset:20480\n" \
  "ds_read_b64_tr_b16 a[18:19], v224 offset:20992\n" \
  "ds_read_b64_tr_b16 a[20:21], v224 offset:21504\n" \
  "v_mul_f32_e32 v175, v175, v243\n" \
  "v_mfma_f32_32x32x16_bf16 v[112:127], a[4:7], v[80:83], v[112:127]\n" \
  "ds_read_b64_tr_b16 a[22:23], v224 offset:22016\n" \
  "ds_read_b64_tr_b16 a[24:25], v225 offset:20480\n" \
  "ds_read_b64_tr_b16 a[26:27], v225 offset:20992\n" \
  "v_cvt_pk_bf16_f32 v182, v172, v173\n" \
  "v_mfma_f32_32x32x16_bf16 v[128:143], a[8:11], v[80:83], v[128:143]\n" \
  "ds_read_b64_tr_b16 a[28:29], v225 offset:21504\n" \
  "ds_read_b64_tr_b16 a[30:31], v225 offset:22016\n" \
  "v_cvt_pk_bf16_f32 v183, v174, v175\n" \
  "global_store_dwordx4 v231, v[180:183], s[74:75] offset:1024 sc1\n" \
  "v_mfma_f32_32x32x16_bf16 v[144:159], a[12:15], v[80:83], v[144:159]\n" \
  "v_mul_f32_e32 v236, v192, v192\n" \
  "v_mul_f32_e32 v237, v193, v193\n" \
  "v_mul_f32_e32 v238, v194, v194\n" \
  "s_waitcnt lgkmcnt(0)\n" \
  "v_mfma_f32_32x32x16_bf16 v[96:111], a[16:19], v[84:87], v[96:111]\n" \
  "ds_read_b64_tr_b16 a[0:1], v224 offset:24576\n" \
  "ds_read_b64_tr_b16 a[2:3], v224 offset:25088\n" \
  "ds_read_b64_tr_b16 a[4:5], v224 offset:25600\n" \
  "v_mul_f32_e32 v239, v195, v195\n" \
  "v_mfma_f32_32x32x16_bf16 v[112:127], a[20:23], v[84:87], v[112:127]\n" \
  "ds_read_b64_tr_b16 a[6:7], v224 offset:26112\n" \
  "ds_read_b64_tr_b16 a[8:9], v225 offset:24576\n" \
  "ds_read_b64_tr_b16 a[10:11], v225 offset:25088\n" \
  "v_fma_f32 v236, v236, v232, s77\n" \
  "v_mfma_f32_32x32x16_bf16 v[128:143], a[24:27], v[84:87], v[128:143]\n" \
  "ds_read_b64_tr_b16 a[12:13], v225 offset:25600\n" \
  "ds_read_b64_tr_b16 a[14:15], v225 offset:26112\n" \
  "v_fma_f32 v237, v237, v232, s77\n" \
  "v_fma_f32 v238, v238, v232, s77\n" \
  "v_mfma_f32_32x32x16_bf16 v[144:159], a[28:31], v[84:87], v[144:159]\n" \
  "v_fma_f32 v239, v239, v232, s77\n" \
  "v_mul_f32_e32 v236, v236, v192\n" \
  "v_mul_f32_e32 v237, v237, v193\n" \
  "s_waitcnt lgkmcnt(0)\n" \
  "v_mfma_f32_32x32x16_bf16 v[96:111], a[0:3], v[88:91], v[96:111]\n" \
  "ds_read_b64_tr_b16 a[16:17], v224 offset:28672\n" \
  "ds_read_b64_tr_b16 a[18:19], v224 offset:29184\n" \
  "ds_read_b64_tr_b16 a[20:21], v224 offset:29696\n" \
  "v_mul_f32_e32 v238, v238, v194\n" \
  "v_mfma_f32_32x32x16_bf16 v[112:127], a[4:7], v[88:91], v[112:127]\n" \
  "ds_read_b64_tr_b16 a[22:23], v224 offset:30208\n" \
  "ds_read_b64_tr_b16 a[24:25], v225 offset:28672\n" \
  "ds_read_b64_tr_b16 a[26:27], v225 offset:29184\n" \
  "v_mul_f32_e32 v239, v239, v195\n" \
  "v_mfma_f32_32x32x16_bf16 v[128:143], a[8:11], v[88:91], v[128:143]\n" \
  "ds_read_b64_tr_b16 a[28:29], v225 offset:29696\n" \
  "ds_read_b64_tr_b16 a[30:31], v225 offset:30208\n" \
  "v_exp_f32_e32 v236, v236\n" \
  "v_mul_f32_e32 v192, v192, v208\n" \
  "v_mfma_f32_32x32x16_bf16 v[144:159], a[12:15], v[88:91], v[144:159]\n" \
  "v_exp_f32_e32 v237, v237\n" \
  "v_mul_f32_e32 v193, v193, v209\n" \
  "v_exp_f32_e32 v238, v238\n" \
  "s_waitcnt lgkmcnt(0)\n" \
  "v_mfma_f32_32x32x16_bf16 v[96:111], a[16:19], v[92:95], v[96:111]\n" \
  "ds_read_b64_tr_b16 a[0:1], v224 offset:32768\n" \
  "ds_read_b64_tr_b16 a[2:3], v224 offset:33280\n" \
  "ds_read_b64_tr_b16 a[4:5], v224 offset:33792\n" \
  "v_mul_f32_e32 v194, v194, v210\n" \
  "v_mfma_f32_32x32x16_bf16 v[112:127], a[20:23], v[92:95], v[112:127]\n" \
  "ds_read_b64_tr_b16 a[6:7], v224 offset:34304\n" \
  "ds_read_b64_tr_b16 a[8:9], v225 offset:32768\n" \
  "ds_read_b64_tr_b16 a[10:11], v225 offset:33280\n" \
  "v_exp_f32_e32 v239, v239\n" \
  "v_mfma_f32_32x32x16_bf16 v[128:143], a[24:27], v[92:95], v[128:143]\n" \
  "ds_read_b64_tr_b16 a[12:13], v225 offset:33792\n" \
  "ds_read_b64_tr_b16 a[14:15], v225 offset:34304\n" \
  "v_mul_f32_e32 v195, v195, v211\n" \
  "v_add_f32_e32 v236, 1.0, v236\n" \
  "v_mfma_f32_32x32x16_bf16 v[144:159], a[28:31], v[92:95], v[144:159]\n" \
  "v_add_f32_e32 v237, 1.0, v237\n" \
  "v_rcp_f32_e32 v236, v236\n" \
  "v_add_f32_e32 v238, 1.0, v238\n" \
  "s_waitcnt vmcnt(9) lgkmcnt(0)\n" \
  "s_barrier\n" \
  "v_mfma_f32_32x32x16_bf16 v[96:111], a[0:3], a[32:35], v[96:111]\n" \
  "ds_read_b64_tr_b16 a[16:17], v224 offset:36864\n" \
  "ds_read_b64_tr_b16 a[18:19], v224 offset:37376\n" \
  "ds_read_b64_tr_b16 a[20:21], v224 offset:37888\n" \
  "v_rcp_f32_e32 v237, v237\n" \
  "v_mfma_f32_32x32x16_bf16 v[112:127], a[4:7], a[32:35], v[112:127]\n" \
  "ds_read_b64_tr_b16 a[22:23], v224 offset:38400\n" \
  "ds_read_b64_tr_b16 a[24:25], v225 offset:36864\n" \
  "ds_read_b64_tr_b16 a[26:27], v225 offset:37376\n" \
  "v_add_f32_e32 v239, 1.0, v239\n" \
  "v_mfma_f32_32x32x16_bf16 v[128:143], a[8:11], a[32:35], v[128:143]\n" \
  "ds_read_b64_tr_b16 a[28:29], v225 offset:37888\n" \
  "ds_read_b64_tr_b16 a[30:31], v225 offset:38400\n" \
  "v_rcp_f32_e32 v238, v238\n" \
  "v_mul_f32_e32 v192, v192, v236\n" \
  "v_mfma_f32_32x32x16_bf16 v[144:159], a[12:15], a[32:35], v[144:159]\n" \
  "v_rcp_f32_e32 v239, v239\n" \
  "v_mul_f32_e32 v193, v193, v237\n" \
  "v_mul_f32_e32 v194, v194, v238\n" \
  "s_waitcnt lgkmcnt(0)\n" \
  "v_mfma_f32_32x32x16_bf16 v[96:111], a[16:19], a[36:39], v[96:111]\n" \
  "ds_read_b64_tr_b16 a[0:1], v224 offset:40960\n" \
  "ds_read_b64_tr_b16 a[2:3], v224 offset:41472\n" \
  "ds_read_b64_tr_b16 a[4:5], v224 offset:41984\n" \
  "v_mul_f32_e32 v195, v195, v239\n" \
  "v_mfma_f32_32x32x16_bf16 v[112:127], a[20:23], a[36:39], v[112:127]\n" \
  "ds_read_b64_tr_b16 a[6:7], v224 offset:42496\n" \
  "ds_read_b64_tr_b16 a[8:9], v225 offset:40960\n" \
  "ds_read_b64_tr_b16 a[10:11], v225 offset:41472\n" \
  "v_cvt_pk_bf16_f32 v208, v192, v193\n" \
  "v_mfma_f32_32x32x16_bf16 v[128:143], a[24:27], a[36:39], v[128:143]\n" \
  "ds_read_b64_tr_b16 a[12:13], v225 offset:41984\n" \
  "ds_read_b64_tr_b16 a[14:15], v225 offset:42496\n" \
  "v_cvt_pk_bf16_f32 v209, v194, v195\n" \
  "v_mul_f32_e32 v240, v196, v196\n" \
  "v_mfma_f32_32x32x16_bf16 v[144:159], a[28:31], a[36:39], v[144:159]\n" \
  "v_mul_f32_e32 v241, v197, v197\n" \
  "v_mul_f32_e32 v242, v198, v198\n" \
  "v_mul_f32_e32 v243, v199, v199\n" \
  "s_waitcnt lgkmcnt(0)\n" \
  "v_mfma_f32_32x32x16_bf16 v[96:111], a[0:3], a[40:43], v[96:111]\n" \
  "ds_read_b64_tr_b16 a[16:17], v224 offset:45056\n" \
  "ds_read_b64_tr_b16 a[18:19], v224 offset:45568\n" \
  "ds_read_b64_tr_b16 a[20:21], v224 offset:46080\n" \
  "v_fma_f32 v240, v240, v232, s77\n" \
  "v_mfma_f32_32x32x16_bf16 v[112:127], a[4:7], a[40:43], v[112:127]\n" \
  "ds_read_b64_tr_b16 a[22:23], v224 offset:46592\n" \
  "ds_read_b64_tr_b16 a[24:25], v225 offset:45056\n" \
  "ds_read_b64_tr_b16 a[26:27], v225 offset:45568\n" \
  "v_fma_f32 v241, v241, v232, s77\n" \
  "v_mfma_f32_32x32x16_bf16 v[128:143], a[8:11], a[40:43], v[128:143]\n" \
  "ds_read_b64_tr_b16 a[28:29], v225 offset:46080\n" \
  "ds_read_b64_tr_b16 a[30:31], v225 offset:46592\n" \
  "v_fma_f32 v242, v242, v232, s77\n" \
  "v_fma_f32 v243, v243, v232, s77\n" \
  "v_mfma_f32_32x32x16_bf16 v[144:159], a[12:15], a[40:43], v[144:159]\n" \
  "v_mul_f32_e32 v240, v240, v196\n" \
  "v_mul_f32_e32 v241, v241, v197\n" \
  "v_mul_f32_e32 v242, v242, v198\n" \
  "v_mul_f32_e32 v243, v243, v199\n" \
  "s_waitcnt lgkmcnt(0)\n" \
  "v_mfma_f32_32x32x16_bf16 v[96:111], a[16:19], a[44:47], v[96:111]\n" \
  "ds_read_b64_tr_b16 a[0:1], v224 offset:49152\n" \
  "ds_read_b64_tr_b16 a[2:3], v224 offset:49664\n" \
  "ds_read_b64_tr_b16 a[4:5], v224 offset:50176\n" \
  "v_exp_f32_e32 v240, v240\n" \
  "v_mfma_f32_32x32x16_bf16 v[112:127], a[20:23], a[44:47], v[112:127]\n" \
  "ds_read_b64_tr_b16 a[6:7], v224 offset:50688\n" \
  "ds_read_b64_tr_b16 a[8:9], v225 offset:49152\n" \
  "ds_read_b64_tr_b16 a[10:11], v225 offset:49664\n" \
  "v_mul_f32_e32 v196, v196, v212\n" \
  "v_mfma_f32_32x32x16_bf16 v[128:143], a[24:27], a[44:47], v[128:143]\n" \
  "ds_read_b64_tr_b16 a[12:13], v225 offset:50176\n" \
  "ds_read_b64_tr_b16 a[14:15], v225 offset:50688\n" \
  "v_exp_f32_e32 v241, v241\n" \
  "v_mul_f32_e32 v197, v197, v213\n" \
  "v_mfma_f32_32x32x16_bf16 v[144:159], a[28:31], a[44:47], v[144:159]\n" \
  "v_exp_f32_e32 v242, v242\n" \
  "v_mul_f32_e32 v198, v198, v214\n" \
  "v_exp_f32_e32 v243, v243\n" \
  "s_waitcnt vmcnt(5) lgkmcnt(0)\n" \
  "s_barrier\n" \
  "v_mfma_f32_32x32x16_bf16 v[96:111], a[0:3], a[48:51], v[96:111]\n" \
  "ds_read_b64_tr_b16 a[16:17], v224 offset:53248\n" \
  "ds_read_b64_tr_b16 a[18:19], v224 offset:53760\n" \
  "ds_read_b64_tr_b16 a[20:21], v224 offset:54272\n" \
  "v_mul_f32_e32 v199, v199, v215\n" \
  "v_mfma_f32_32x32x16_bf16 v[112:127], a[4:7], a[48:51], v[112:127]\n" \
  "ds_read_b64_tr_b16 a[22:23], v224 offset:54784\n" \
  "ds_read_b64_tr_b16 a[24:25], v225 offset:53248\n" \
  "ds_read_b64_tr_b16 a[26:27], v225 offset:53760\n" \
  "v_add_f32_e32 v240, 1.0, v240\n" \
  "v_mfma_f32_32x32x16_bf16 v[128:143], a[8:11], a[48:51], v[128:143]\n" \
  "ds_read_b64_tr_b16 a[28:29], v225 offset:54272\n" \
  "ds_read_b64_tr_b16 a[30:31], v225 offset:54784\n" \
  "v_add_f32_e32 v241, 1.0, v241\n" \
  "v_rcp_f32_e32 v240, v240\n" \
  "v_mfma_f32_32x32x16_bf16 v[144:159], a[12:15], a[48:51], v[144:159]\n" \
  "v_add_f32_e32 v242, 1.0, v242\n" \
  "v_rcp_f32_e32 v241, v241\n" \
  "v_add_f32_e32 v243, 1.0, v243\n" \
  "s_waitcnt lgkmcnt(0)\n" \
  "v_mfma_f32_32x32x16_bf16 v[96:111], a[16:19], a[52:55], v[96:111]\n" \
  "ds_read_b64_tr_b16 a[0:1], v224 offset:57344\n" \
  "ds_read_b64_tr_b16 a[2:3], v224 offset:57856\n" \
  "ds_read_b64_tr_b16 a[4:5], v224 offset:58368\n" \
  "v_rcp_f32_e32 v242, v242\n" \
  "v_mfma_f32_32x32x16_bf16 v[112:127], a[20:23], a[52:55], v[112:127]\n" \
  "ds_read_b64_tr_b16 a[6:7], v224 offset:58880\n" \
  "ds_read_b64_tr_b16 a[8:9], v225 offset:57344\n" \
  "ds_read_b64_tr_b16 a[10:11], v225 offset:57856\n" \
  "v_mul_f32_e32 v196, v196, v240\n" \
  "v_mfma_f32_32x32x16_bf16 v[128:143], a[24:27], a[52:55], v[128:143]\n" \
  "ds_read_b64_tr_b16 a[12:13], v225 offset:58368\n" \
  "ds_read_b64_tr_b16 a[14:15], v225 offset:58880\n" \
  "v_rcp_f32_e32 v243, v243\n" \
  "v_mul_f32_e32 v197, v197, v241\n" \
  "v_mfma_f32_32x32x16_bf16 v[144:159], a[28:31], a[52:55], v[144:159]\n" \
  "v_mul_f32_e32 v198, v198, v242\n" \
  "v_mul_f32_e32 v199, v199, v243\n" \
  "v_cvt_pk_bf16_f32 v210, v196, v197\n" \
  "s_waitcnt lgkmcnt(0)\n" \
  "v_mfma_f32_32x32x16_bf16 v[96:111], a[0:3], a[56:59], v[96:111]\n" \
  "ds_read_b64_tr_b16 a[16:17], v224 offset:61440\n" \
  "ds_read_b64_tr_b16 a[18:19], v224 offset:61952\n" \
  "ds_read_b64_tr_b16 a[20:21], v224 offset:62464\n" \
  "v_cvt_pk_bf16_f32 v211, v198, v199\n" \
  "v_mfma_f32_32x32x16_bf16 v[112:127], a[4:7], a[56:59], v[112:127]\n" \
  "ds_read_b64_tr_b16 a[22:23], v224 offset:62976\n" \
  "ds_read_b64_tr_b16 a[24:25], v225 offset:61440\n" \
  "ds_read_b64_tr_b16 a[26:27], v225 offset:61952\n" \
  "global_store_dwordx4 v231, v[208:211], s[74:75] offset:2048 sc1\n" \
  "v_mfma_f32_32x32x16_bf16 v[128:143], a[8:11], a[56:59], v[128:143]\n" \
  "ds_read_b64_tr_b16 a[28:29], v225 offset:62464\n" \
  "ds_read_b64_tr_b16 a[30:31], v225 offset:62976\n" \
  "v_mul_f32_e32 v236, v200, v200\n" \
  "v_mul_f32_e32 v237, v201, v201\n" \
  "v_mfma_f32_32x32x16_bf16 v[144:159], a[12:15], a[56:59], v[144:159]\n" \
  "v_mul_f32_e32 v238, v202, v202\n" \
  "v_mul_f32_e32 v239, v203, v203\n" \
  "v_fma_f32 v236, v236, v232, s77\n" \
  "s_waitcnt lgkmcnt(0)\n" \
  "v_mfma_f32_32x32x16_bf16 v[96:111], a[16:19], a[60:63], v[96:111]\n" \
  "ds_read_b64_tr_b16 a[0:1], v226 offset:0\n" \
  "ds_read_b64_tr_b16 a[2:3], v226 offset:512\n" \
  "ds_read_b64_tr_b16 a[4:5], v226 offset:1024\n" \
  "v_fma_f32 v237, v237, v232, s77\n" \
  "v_mfma_f32_32x32x16_bf16 v[112:127], a[20:23], a[60:63], v[112:127]\n" \
  "ds_read_b64_tr_b16 a[6:7], v226 offset:1536\n" \
  "ds_read_b64_tr_b16 a[8:9], v227 offset:0\n" \
  "ds_read_b64_tr_b16 a[10:11], v227 offset:512\n" \
  "v_fma_f32 v238, v238, v232, s77\n" \
  "v_mfma_f32_32x32x16_bf16 v[128:143], a[24:27], a[60:63], v[128:143]\n" \
  "ds_read_b64_tr_b16 a[12:13], v227 offset:1024\n" \
  "ds_read_b64_tr_b16 a[14:15], v227 offset:1536\n" \
  "v_fma_f32 v239, v239, v232, s77\n" \
  "v_mul_f32_e32 v236, v236, v200\n" \
  "v_mfma_f32_32x32x16_bf16 v[144:159], a[28:31], a[60:63], v[144:159]\n" \
  "v_mul_f32_e32 v237, v237, v201\n" \
  "v_mul_f32_e32 v238, v238, v202\n" \
  "v_mul_f32_e32 v239, v239, v203\n" \
  "s_waitcnt vmcnt(2) lgkmcnt(0)\n" \
  "s_barrier\n" \
  "v_mfma_f32_32x32x16_bf16 v[96:111], a[0:3], a[64:67], v[96:111]\n" \
  "ds_read_b64_tr_b16 a[16:17], v226 offset:4096\n" \
  "ds_read_b64_tr_b16 a[18:19], v226 offset:4608\n" \
  "ds_read_b64_tr_b16 a[20:21], v226 offset:5120\n" \
  "v_exp_f32_e32 v236, v236\n" \
  "v_mfma_f32_32x32x16_bf16 v[112:127], a[4:7], a[64:67], v[112:127]\n" \
  "ds_read_b64_tr_b16 a[22:23], v226 offset:5632\n" \
  "ds_read_b64_tr_b16 a[24:25], v227 offset:4096\n" \
  "ds_read_b64_tr_b16 a[26:27], v227 offset:4608\n" \
  "v_mul_f32_e32 v200, v200, v216\n" \
  "v_mfma_f32_32x32x16_bf16 v[128:143], a[8:11], a[64:67], v[128:143]\n" \
  "ds_read_b64_tr_b16 a[28:29], v227 offset:5120\n" \
  "ds_read_b64_tr_b16 a[30:31], v227 offset:5632\n" \
  "v_exp_f32_e32 v237, v237\n" \
  "v_mul_f32_e32 v201, v201, v217\n" \
  "v_mfma_f32_32x32x16_bf16 v[144:159], a[12:15], a[64:67], v[144:159]\n" \
  "v_exp_f32_e32 v238, v238\n" \
  "v_mul_f32_e32 v202, v202, v218\n" \
  "v_exp_f32_e32 v239, v239\n" \
  "s_waitcnt lgkmcnt(0)\n" \
  "v_mfma_f32_32x32x16_bf16 v[96:111], a[16:19], a[68:71], v[96:111]\n" \
  "ds_read_b64_tr_b16 a[0:1], v226 offset:8192\n" \
  "ds_read_b64_tr_b16 a[2:3], v226 offset:8704\n" \
  "ds_read_b64_tr_b16 a[4:5], v226 offset:9216\n" \
  "v_mul_f32_e32 v203, v203, v219\n" \
  "v_mfma_f32_32x32x16_bf16 v[112:127], a[20:23], a[68:71], v[112:127]\n" \
  "ds_read_b64_tr_b16 a[6:7], v226 offset:9728\n" \
  "ds_read_b64_tr_b16 a[8:9], v227 offset:8192\n" \
  "ds_read_b64_tr_b16 a[10:11], v227 offset:8704\n" \
  "v_add_f32_e32 v236, 1.0, v236\n" \
  "v_mfma_f32_32x32x16_bf16 v[128:143], a[24:27], a[68:71], v[128:143]\n" \
  "ds_read_b64_tr_b16 a[12:13], v227 offset:9216\n" \
  "ds_read_b64_tr_b16 a[14:15], v227 offset:9728\n" \
  "v_add_f32_e32 v237, 1.0, v237\n" \
  "v_rcp_f32_e32 v236, v236\n" \
  "v_mfma_f32_32x32x16_bf16 v[144:159], a[28:31], a[68:71], v[144:159]\n" \
  "v_add_f32_e32 v238, 1.0, v238\n" \
  "v_rcp_f32_e32 v237, v237\n" \
  "v_add_f32_e32 v239, 1.0, v239\n" \
  "s_waitcnt lgkmcnt(0)\n" \
  "v_mfma_f32_32x32x16_bf16 v[96:111], a[0:3], a[72:75], v[96:111]\n" \
  "ds_read_b64_tr_b16 a[16:17], v226 offset:12288\n" \
  "ds_read_b64_tr_b16 a[18:19], v226 offset:12800\n" \
  "ds_read_b64_tr_b16 a[20:21], v226 offset:13312\n" \
  "v_rcp_f32_e32 v238, v238\n" \
  "v_mfma_f32_32x32x16_bf16 v[112:127], a[4:7], a[72:75], v[112:127]\n" \
  "ds_read_b64_tr_b16 a[22:23], v226 offset:13824\n" \
  "ds_read_b64_tr_b16 a[24:25], v227 offset:12288\n" \
  "ds_read_b64_tr_b16 a[26:27], v227 offset:12800\n" \
  "v_mul_f32_e32 v200, v200, v236\n" \
  "v_mfma_f32_32x32x16_bf16 v[128:143], a[8:11], a[72:75], v[128:143]\n" \
  "ds_read_b64_tr_b16 a[28:29], v227 offset:13312\n" \
  "ds_read_b64_tr_b16 a[30:31], v227 offset:13824\n" \
  "v_rcp_f32_e32 v239, v239\n" \
  "v_mul_f32_e32 v201, v201, v237\n" \
  "v_mfma_f32_32x32x16_bf16 v[144:159], a[12:15], a[72:75], v[144:159]\n" \
  "v_mul_f32_e32 v202, v202, v238\n" \
  "v_mul_f32_e32 v203, v203, v239\n" \
  "v_cvt_pk_bf16_f32 v212, v200, v201\n" \
  "s_waitcnt lgkmcnt(0)\n" \
  "v_mfma_f32_32x32x16_bf16 v[96:111], a[16:19], a[76:79], v[96:111]\n" \
  "ds_read_b64_tr_b16 a[0:1], v226 offset:16384\n" \
  "ds_read_b64_tr_b16 a[2:3], v226 offset:16896\n" \
  "ds_read_b64_tr_b16 a[4:5], v226 offset:17408\n" \
  "v_cvt_pk_bf16_f32 v213, v202, v203\n" \
  "v_mfma_f32_32x32x16_bf16 v[112:127], a[20:23], a[76:79], v[112:127]\n" \
  "ds_read_b64_tr_b16 a[6:7], v226 offset:17920\n" \
  "ds_read_b64_tr_b16 a[8:9], v227 offset:16384\n" \
  "ds_read_b64_tr_b16 a[10:11], v227 offset:16896\n" \
  "v_mul_f32_e32 v240, v204, v204\n" \
  "v_mfma_f32_32x32x16_bf16 v[128:143], a[24:27], a[76:79], v[128:143]\n" \
  "ds_read_b64_tr_b16 a[12:13], v227 offset:17408\n" \
  "ds_read_b64_tr_b16 a[14:15], v227 offset:17920\n" \
  "v_mul_f32_e32 v241, v205, v205\n" \
  "v_mul_f32_e32 v242, v206, v206\n" \
  "v_mfma_f32_32x32x16_bf16 v[144:159], a[28:31], a[76:79], v[144:159]\n" \
  "v_mul_f32_e32 v243, v207, v207\n" \
  "v_fma_f32 v240, v240, v232, s77\n" \
  "v_fma_f32 v241, v241, v232, s77\n" \
  "s_waitcnt lgkmcnt(0)\n" \
  "v_mfma_f32_32x32x16_bf16 v[96:111], a[0:3], a[80:83], v[96:111]\n" \
  "ds_read_b64_tr_b16 a[16:17], v226 offset:20480\n" \
  "ds_read_b64_tr_b16 a[18:19], v226 offset:20992\n" \
  "ds_read_b64_tr_b16 a[20:21], v226 offset:21504\n" \
  "v_fma_f32 v242, v242, v232, s77\n" \
  "v_mfma_f32_32x32x16_bf16 v[112:127], a[4:7], a[80:83], v[112:127]\n" \
  "ds_read_b64_tr_b16 a[22:23], v226 offset:22016\n" \
  "ds_read_b64_tr_b16 a[24:25], v227 offset:20480\n" \
  "ds_read_b64_tr_b16 a[26:27], v227 offset:20992\n" \
  "v_fma_f32 v243, v243, v232, s77\n" \
  "v_mfma_f32_32x32x16_bf16 v[128:143], a[8:11], a[80:83], v[128:143]\n" \
  "ds_read_b64_tr_b16 a[28:29], v227 offset:21504\n" \
  "ds_read_b64_tr_b16 a[30:31], v227 offset:22016\n" \
  "v_mul_f32_e32 v240, v240, v204\n" \
  "v_mul_f32_e32 v241, v241, v205\n" \
  "v_mfma_f32_32x32x16_bf16 v[144:159], a[12:15], a[80:83], v[144:159]\n" \
  "v_mul_f32_e32 v242, v242, v206\n" \
  "v_mul_f32_e32 v243, v243, v207\n" \
  "v_exp_f32_e32 v240, v240\n" \
  "s_waitcnt lgkmcnt(0)\n" \
  "v_mfma_f32_32x32x16_bf16 v[96:111], a[16:19], a[84:87], v[96:111]\n" \
  "ds_read_b64_tr_b16 a[0:1], v226 offset:24576\n" \
  "ds_read_b64_tr_b16 a[2:3], v226 offset:25088\n" \
  "ds_read_b64_tr_b16 a[4:5], v226 offset:25600\n" \
  "v_mul_f32_e32 v204, v204, v220\n" \
  "v_mfma_f32_32x32x16_bf16 v[112:127], a[20:23], a[84:87], v[112:127]\n" \
  "ds_read_b64_tr_b16 a[6:7], v226 offset:26112\n" \
  "ds_read_b64_tr_b16 a[8:9], v227 offset:24576\n" \
  "ds_read_b64_tr_b16 a[10:11], v227 offset:25088\n" \
  "v_exp_f32_e32 v241, v241\n" \
  "v_mfma_f32_32x32x16_bf16 v[128:143], a[24:27], a[84:87], v[128:143]\n" \
  "ds_read_b64_tr_b16 a[12:13], v227 offset:25600\n" \
  "ds_read_b64_tr_b16 a[14:15], v227 offset:26112\n" \
  "v_mul_f32_e32 v205, v205, v221\n" \
  "v_exp_f32_e32 v242, v242\n" \
  "v_mfma_f32_32x32x16_bf16 v[144:159], a[28:31], a[84:87], v[144:159]\n" \
  "v_mul_f32_e32 v206, v206, v222\n" \
  "v_exp_f32_e32 v243, v243\n" \
  "v_mul_f32_e32 v207, v207, v223\n" \
  "s_waitcnt lgkmcnt(0)\n" \
  "v_mfma_f32_32x32x16_bf16 v[96:111], a[0:3], a[88:91], v[96:111]\n" \
  "ds_read_b64_tr_b16 a[16:17], v226 offset:28672\n" \
  "ds_read_b64_tr_b16 a[18:19], v226 offset:29184\n" \
  "ds_read_b64_tr_b16 a[20:21], v226 offset:29696\n" \
  "v_add_f32_e32 v240, 1.0, v240\n" \
  "v_mfma_f32_32x32x16_bf16 v[112:127], a[4:7], a[88:91], v[112:127]\n" \
  "ds_read_b64_tr_b16 a[22:23], v226 offset:30208\n" \
  "ds_read_b64_tr_b16 a[24:25], v227 offset:28672\n" \
  "ds_read_b64_tr_b16 a[26:27], v227 offset:29184\n" \
  "v_add_f32_e32 v241, 1.0, v241\n" \
  "v_mfma_f32_32x32x16_bf16 v[128:143], a[8:11], a[88:91], v[128:143]\n" \
  "ds_read_b64_tr_b16 a[28:29], v227 offset:29696\n" \
  "ds_read_b64_tr_b16 a[30:31], v227 offset:30208\n" \
  "v_rcp_f32_e32 v240, v240\n" \
  "v_add_f32_e32 v242, 1.0, v242\n" \
  "v_mfma_f32_32x32x16_bf16 v[144:159], a[12:15], a[88:91], v[144:159]\n" \
  "v_rcp_f32_e32 v241, v241\n" \
  "v_add_f32_e32 v243, 1.0, v243\n" \
  "v_rcp_f32_e32 v242, v242\n" \
  "s_waitcnt lgkmcnt(0)\n" \
  "v_mfma_f32_32x32x16_bf16 v[96:111], a[16:19], a[92:95], v[96:111]\n" \
  "v_mul_f32_e32 v204, v204, v240\n" \
  "v_rcp_f32_e32 v243, v243\n" \
  "v_mfma_f32_32x32x16_bf16 v[112:127], a[20:23], a[92:95], v[112:127]\n" \
  "v_mul_f32_e32 v205, v205, v241\n" \
  "v_mul_f32_e32 v206, v206, v242\n" \
  "v_mfma_f32_32x32x16_bf16 v[128:143], a[24:27], a[92:95], v[128:143]\n" \
  "v_mul_f32_e32 v207, v207, v243\n" \
  "v_cvt_pk_bf16_f32 v214, v204, v205\n" \
  "v_mfma_f32_32x32x16_bf16 v[144:159], a[28:31], a[92:95], v[144:159]\n" \
  "v_cvt_pk_bf16_f32 v215, v206, v207\n" \
  "global_store_dwordx4 v231, v[212:215], s[74:75] offset:3072 sc1\n" \
  "s_branch 3f\n" \
  "1:\n" \
  "s_waitcnt vmcnt(14) lgkmcnt(0)\n" \
  "s_barrier\n" \
  "v_mfma_f32_32x32x16_bf16 v[96:111], v[0:3], a[0:3], 0\n" \
  "s_add_u32 m0, s67, 16384\n" \
  "ds_read_b64_tr_b16 a[16:17], v226 offset:4096\n" \
  "ds_read_b64_tr_b16 a[18:19], v226 offset:4608\n" \
  "ds_read_b64_tr_b16 a[20:21], v226 offset:5120\n" \
  "v_mfma_f32_32x32x16_bf16 v[112:127], v[0:3], a[4:7], 0\n" \
  "ds_read_b64_tr_b16 a[22:23], v226 offset:5632\n" \
  "ds_read_b64_tr_b16 a[24:25], v227 offset:4096\n" \
  "ds_read_b64_tr_b16 a[26:27], v227 offset:4608\n" \
  "v_mfma_f32_32x32x16_bf16 v[128:143], v[0:3], a[8:11], 0\n" \
  "ds_read_b64_tr_b16 a[28:29], v227 offset:5120\n" \
  "ds_read_b64_tr_b16 a[30:31], v227 offset:5632\n" \
  "global_load_lds_dwordx4 v230, s[56:57] offset:0\n" \
  "v_mfma_f32_32x32x16_bf16 v[144:159], v[0:3], a[12:15], 0\n" \
  "s_waitcnt lgkmcnt(0)\n" \
  "v_mfma_f32_32x32x16_bf16 v[96:111], v[4:7], a[16:19], v[96:111]\n" \
  "ds_read_b64_tr_b16 a[0:1], v226 offset:8192\n" \
  "ds_read_b64_tr_b16 a[2:3], v226 offset:8704\n" \
  "ds_read_b64_tr_b16 a[4:5], v226 offset:9216\n" \
  "global_load_lds_dwordx4 v230, s[58:59] offset:1024\n" \
  "v_mfma_f32_32x32x16_bf16 v[112:127], v[4:7], a[20:23], v[112:127]\n" \
  "ds_read_b64_tr_b16 a[6:7], v226 offset:9728\n" \
  "ds_read_b64_tr_b16 a[8:9], v227 offset:8192\n" \
  "ds_read_b64_tr_b16 a[10:11], v227 offset:8704\n" \
  "v_mul_f32_e32 v236, v160, v160\n" \
  "v_mfma_f32_32x32x16_bf16 v[128:143], v[4:7], a[24:27], v[128:143]\n" \
  "ds_read_b64_tr_b16 a[12:13], v227 offset:9216\n" \
  "ds_read_b64_tr_b16 a[14:15], v227 offset:9728\n" \
  "v_mul_f32_e32 v237, v161, v161\n" \
  "v_mul_f32_e32 v238, v162, v162\n" \
  "v_mfma_f32_32x32x16_bf16 v[144:159], v[4:7], a[28:31], v[144:159]\n" \
  "v_mul_f32_e32 v239, v163, v163\n" \
  "v_fma_f32 v236, v236, v232, s77\n" \
  "v_fma_f32 v237, v237, v232, s77\n" \
  "v_fma_f32 v238, v238, v232, s77\n" \
  "s_waitcnt lgkmcnt(0)\n" \
  "v_mfma_f32_32x32x16_bf16 v[96:111], v[8:11], a[0:3], v[96:111]\n" \
  "ds_read_b64_tr_b16 a[16:17], v226 offset:12288\n" \
  "ds_read_b64_tr_b16 a[18:19], v226 offset:12800\n" \
  "ds_read_b64_tr_b16 a[20:21], v226 offset:13312\n" \
  "global_load_lds_dwordx4 v230, s[60:61] offset:2048\n" \
  "v_mfma_f32_32x32x16_bf16 v[112:127], v[8:11], a[4:7], v[112:127]\n" \
  "ds_read_b64_tr_b16 a[22:23], v226 offset:13824\n" \
  "ds_read_b64_tr_b16 a[24:25], v227 offset:12288\n" \
  "ds_read_b64_tr_b16 a[26:27], v227 offset:12800\n" \
  "v_fma_f32 v239, v239, v232, s77\n" \
  "v_mfma_f32_32x32x16_bf16 v[128:143], v[8:11], a[8:11], v[128:143]\n" \
  "ds_read_b64_tr_b16 a[28:29], v227 offset:13312\n" \
  "ds_read_b64_tr_b16 a[30:31], v227 offset:13824\n" \
  "v_mul_f32_e32 v236, v236, v160\n" \
  "v_mul_f32_e32 v237, v237, v161\n" \
  "v_mfma_f32_32x32x16_bf16 v[144:159], v[8:11], a[12:15], v[144:159]\n" \
  "v_mul_f32_e32 v238, v238, v162\n" \
  "v_mul_f32_e32 v239, v239, v163\n" \
  "v_exp_f32_e32 v236, v236\n" \
  "v_mul_f32_e32 v160, v160, v176\n" \
  "s_waitcnt lgkmcnt(0)\n" \
  "v_mfma_f32_32x32x16_bf16 v[96:111], v[12:15], a[16:19], v[96:111]\n" \
  "ds_read_b64_tr_b16 a[0:1], v226 offset:16384\n" \
  "ds_read_b64_tr_b16 a[2:3], v226 offset:16896\n" \
  "ds_read_b64_tr_b16 a[4:5], v226 offset:17408\n" \
  "global_load_lds_dwordx4 v230, s[62:63] offset:3072\n" \
  "v_add_u32_e32 v230, s66, v230\n" \
  "v_mfma_f32_32x32x16_bf16 v[112:127], v[12:15], a[20:23], v[112:127]\n" \
  "ds_read_b64_tr_b16 a[6:7], v226 offset:17920\n" \
  "ds_read_b64_tr_b16 a[8:9], v227 offset:16384\n" \
  "ds_read_b64_tr_b16 a[10:11], v227 offset:16896\n" \
  "v_exp_f32_e32 v237, v237\n" \
  "v_mul_f32_e32 v161, v161, v177\n" \
  "v_mfma_f32_32x32x16_bf16 v[128:143], v[12:15], a[24:27], v[128:143]\n" \
  "ds_read_b64_tr_b16 a[12:13], v227 offset:17408\n" \
  "ds_read_b64_tr_b16 a[14:15], v227 offset:17920\n" \
  "v_exp_f32_e32 v238, v238\n" \
  "v_mul_f32_e32 v162, v162, v178\n" \
  "v_exp_f32_e32 v239, v239\n" \
  "v_mfma_f32_32x32x16_bf16 v[144:159], v[12:15], a[28:31], v[144:159]\n" \
  "v_mul_f32_e32 v163, v163, v179\n" \
  "v_add_f32_e32 v236, 1.0, v236\n" \
  "s_waitcnt vmcnt(13) lgkmcnt(0)\n" \
  "s_barrier\n" \
  "v_mfma_f32_32x32x16_bf16 v[96:111], v[16:19], a[0:3], v[96:111]\n" \
  "s_add_u32 m0, s67, 32768\n" \
  "ds_read_b64_tr_b16 a[16:17], v226 offset:20480\n" \
  "ds_read_b64_tr_b16 a[18:19], v226 offset:20992\n" \
  "ds_read_b64_tr_b16 a[20:21], v226 offset:21504\n" \
  "global_load_lds_dwordx4 v230, s[56:57] offset:0\n" \
  "v_mfma_f32_32x32x16_bf16 v[112:127], v[16:19], a[4:7], v[112:127]\n" \
  "ds_read_b64_tr_b16 a[22:23], v226 offset:22016\n" \
  "ds_read_b64_tr_b16 a[24:25], v227 offset:20480\n" \
  "ds_read_b64_tr_b16 a[26:27], v227 offset:20992\n" \
  "v_add_f32_e32 v237, 1.0, v237\n" \
  "v_rcp_f32_e32 v236, v236\n" \
  "v_mfma_f32_32x32x16_bf16 v[128:143], v[16:19], a[8:11], v[128:143]\n" \
  "ds_read_b64_tr_b16 a[28:29], v227 offset:21504\n" \
  "ds_read_b64_tr_b16 a[30:31], v227 offset:22016\n" \
  "v_add_f32_e32 v238, 1.0, v238\n" \
  "v_rcp_f32_e32 v237, v237\n" \
  "v_add_f32_e32 v239, 1.0, v239\n" \
  "v_mfma_f32_32x32x16_bf16 v[144:159], v[16:19], a[12:15], v[144:159]\n" \
  "v_rcp_f32_e32 v238, v238\n" \
  "v_mul_f32_e32 v160, v160, v236\n" \
  "s_waitcnt lgkmcnt(0)\n" \
  "v_mfma_f32_32x32x16_bf16 v[96:111], v[20:23], a[16:19], v[96:111]\n" \
  "ds_read_b64_tr_b16 a[0:1], v226 offset:24576\n" \
  "ds_read_b64_tr_b16 a[2:3], v226 offset:25088\n" \
  "ds_read_b64_tr_b16 a[4:5], v226 offset:25600\n" \
  "global_load_lds_dwordx4 v230, s[58:59] offset:1024\n" \
  "v_mfma_f32_32x32x16_bf16 v[112:127], v[20:23], a[20:23], v[112:127]\n" \
  "ds_read_b64_tr_b16 a[6:7], v226 offset:26112\n" \
  "ds_read_b64_tr_b16 a[8:9], v227 offset:24576\n" \
  "ds_read_b64_tr_b16 a[10:11], v227 offset:25088\n" \
  "v_rcp_f32_e32 v239, v239\n" \
  "v_mfma_f32_32x32x16_bf16 v[128:143], v[20:23], a[24:27], v[128:143]\n" \
  "ds_read_b64_tr_b16 a[12:13], v227 offset:25600\n" \
  "ds_read_b64_tr_b16 a[14:15], v227 offset:26112\n" \
  "v_mul_f32_e32 v161, v161, v237\n" \
  "v_mul_f32_e32 v162, v162, v238\n" \
  "v_mfma_f32_32x32x16_bf16 v[144:159], v[20:23], a[28:31], v[144:159]\n" \
  "v_mul_f32_e32 v163, v163, v239\n" \
  "v_cvt_pk_bf16_f32 v176, v160, v161\n" \
  "v_cvt_pk_bf16_f32 v177, v162, v163\n" \
  "v_mul_f32_e32 v240, v164, v164\n" \
  "s_waitcnt lgkmcnt(0)\n" \
  "v_mfma_f32_32x32x16_bf16 v[96:111], v[24:27], a[0:3], v[96:111]\n" \
  "ds_read_b64_tr_b16 a[16:17], v226 offset:28672\n" \
  "ds_read_b64_tr_b16 a[18:19], v226 offset:29184\n" \
  "ds_read_b64_tr_b16 a[20:21], v226 offset:29696\n" \
  "global_load_lds_dwordx4 v230, s[60:61] offset:2048\n" \
  "v_mfma_f32_32x32x16_bf16 v[112:127], v[24:27], a[4:7], v[112:127]\n" \
  "ds_read_b64_tr_b16 a[22:23], v226 offset:30208\n" \
  "ds_read_b64_tr_b16 a[24:25], v227 offset:28672\n" \
  "ds_read_b64_tr_b16 a[26:27], v227 offset:29184\n" \
  "v_mul_f32_e32 v241, v165, v165\n" \
  "v_mfma_f32_32x32x16_bf16 v[128:143], v[24:27], a[8:11], v[128:143]\n" \
  "ds_read_b64_tr_b16 a[28:29], v227 offset:29696\n" \
  "ds_read_b64_tr_b16 a[30:31], v227 offset:30208\n" \
  "v_mul_f32_e32 v242, v166, v166\n" \
  "v_mul_f32_e32 v243, v167, v167\n" \
  "v_mfma_f32_32x32x16_bf16 v[144:159], v[24:27], a[12:15], v[144:159]\n" \
  "v_fma_f32 v240, v240, v232, s77\n" \
  "v_fma_f32 v241, v241, v232, s77\n" \
  "v_fma_f32 v242, v242, v232, s77\n" \
  "v_fma_f32 v243, v243, v232, s77\n" \
  "s_waitcnt lgkmcnt(0)\n" \
  "v_mfma_f32_32x32x16_bf16 v[96:111], v[28:31], a[16:19], v[96:111]\n" \
  "ds_read_b64_tr_b16 a[0:1], v226 offset:32768\n" \
  "ds_read_b64_tr_b16 a[2:3], v226 offset:33280\n" \
  "ds_read_b64_tr_b16 a[4:5], v226 offset:33792\n" \
  "global_load_lds_dwordx4 v230, s[62:63] offset:3072\n" \
  "v_add_u32_e32 v230, s66, v230\n" \
  "v_mfma_f32_32x32x16_bf16 v[112:127], v[28:31], a[20:23], v[112:127]\n" \
  "ds_read_b64_tr_b16 a[6:7], v226 offset:34304\n" \
  "ds_read_b64_tr_b16 a[8:9], v227 offset:32768\n" \
  "ds_read_b64_tr_b16 a[10:11], v227 offset:33280\n" \
  "v_mul_f32_e32 v240, v240, v164\n" \
  "v_mul_f32_e32 v241, v241, v165\n" \
  "v_mfma_f32_32x32x16_bf16 v[128:143], v[28:31], a[24:27], v[128:143]\n" \
  "ds_read_b64_tr_b16 a[12:13], v227 offset:33792\n" \
  "ds_read_b64_tr_b16 a[14:15], v227 offset:34304\n" \
  "v_mul_f32_e32 v242, v242, v166\n" \
  "v_mul_f32_e32 v243, v243, v167\n" \
  "v_exp_f32_e32 v240, v240\n" \
  "v_mfma_f32_32x32x16_bf16 v[144:159], v[28:31], a[28:31], v[144:159]\n" \
  "v_mul_f32_e32 v164, v164, v180\n" \
  "v_exp_f32_e32 v241, v241\n" \
  "s_waitcnt vmcnt(13) lgkmcnt(0)\n" \
  "s_barrier\n" \
  "v_mfma_f32_32x32x16_bf16 v[96:111], v[32:35], a[0:3], v[96:111]\n" \
  "s_add_u32 m0, s67, 49152\n" \
  "ds_read_b64_tr_b16 a[16:17], v226 offset:36864\n" \
  "ds_read_b64_tr_b16 a[18:19], v226 offset:37376\n" \
  "ds_read_b64_tr_b16 a[20:21], v226 offset:37888\n" \
  "global_load_lds_dwordx4 v230, s[56:57] offset:0\n" \
  "v_mfma_f32_32x32x16_bf16 v[112:127], v[32:35], a[4:7], v[112:127]\n" \
  "ds_read_b64_tr_b16 a[22:23], v226 offset:38400\n" \
  "ds_read_b64_tr_b16 a[24:25], v227 offset:36864\n" \
  "ds_read_b64_tr_b16 a[26:27], v227 offset:37376\n" \
  "v_mul_f32_e32 v165, v165, v181\n" \
  "v_exp_f32_e32 v242, v242\n" \
  "v_mfma_f32_32x32x16_bf16 v[128:143], v[32:35], a[8:11], v[128:143]\n" \
  "ds_read_b64_tr_b16 a[28:29], v227 offset:37888\n" \
  "ds_read_b64_tr_b16 a[30:31], v227 offset:38400\n" \
  "v_mul_f32_e32 v166, v166, v182\n" \
  "v_exp_f32_e32 v243, v243\n" \
  "v_mul_f32_e32 v167, v167, v183\n" \
  "v_mfma_f32_32x32x16_bf16 v[144:159], v[32:35], a[12:15], v[144:159]\n" \
  "v_add_f32_e32 v240, 1.0, v240\n" \
  "v_add_f32_e32 v241, 1.0, v241\n" \
  "s_waitcnt lgkmcnt(0)\n" \
  "v_mfma_f32_32x32x16_bf16 v[96:111], v[36:39], a[16:19], v[96:111]\n" \
  "ds_read_b64_tr_b16 a[0:1], v226 offset:40960\n" \
  "ds_read_b64_tr_b16 a[2:3], v226 offset:41472\n" \
  "ds_read_b64_tr_b16 a[4:5], v226 offset:41984\n" \
  "global_load_lds_dwordx4 v230, s[58:59] offset:1024\n" \
  "v_mfma_f32_32x32x16_bf16 v[112:127], v[36:39], a[20:23], v[112:127]\n" \
  "ds_read_b64_tr_b16 a[6:7], v226 offset:42496\n" \
  "ds_read_b64_tr_b16 a[8:9], v227 offset:40960\n" \
  "ds_read_b64_tr_b16 a[10:11], v227 offset:41472\n" \
  "v_rcp_f32_e32 v240, v240\n" \
  "v_mfma_f32_32x32x16_bf16 v[128:143], v[36:39], a[24:27], v[128:143]\n" \
  "ds_read_b64_tr_b16 a[12:13], v227 offset:41984\n" \
  "ds_read_b64_tr_b16 a[14:15], v227 offset:42496\n" \
  "v_add_f32_e32 v242, 1.0, v242\n" \
  "v_rcp_f32_e32 v241, v241\n" \
  "v_mfma_f32_32x32x16_bf16 v[144:159], v[36:39], a[28:31], v[144:159]\n" \
  "v_add_f32_e32 v243, 1.0, v243\n" \
  "v_rcp_f32_e32 v242, v242\n" \
  "v_mul_f32_e32 v164, v164, v240\n" \
  "v_rcp_f32_e32 v243, v243\n" \
  "s_waitcnt lgkmcnt(0)\n" \
  "v_mfma_f32_32x32x16_bf16 v[96:111], v[40:43], a[0:3], v[96:111]\n" \
  "ds_read_b64_tr_b16 a[16:17], v226 offset:45056\n" \
  "ds_read_b64_tr_b16 a[18:19], v226 offset:45568\n" \
  "ds_read_b64_tr_b16 a[20:21], v226 offset:46080\n" \
  "global_load_lds_dwordx4 v230, s[60:61] offset:2048\n" \
  "v_mfma_f32_32x32x16_bf16 v[112:127], v[40:43], a[4:7], v[112:127]\n" \
  "ds_read_b64_tr_b16 a[22:23], v226 offset:46592\n" \
  "ds_read_b64_tr_b16 a[24:25], v227 offset:45056\n" \
  "ds_read_b64_tr_b16 a[26:27], v227 offset:45568\n" \
  "v_mul_f32_e32 v165, v165, v241\n" \
  "v_mfma_f32_32x32x16_bf16 v[128:143], v[40:43], a[8:11], v[128:143]\n" \
  "ds_read_b64_tr_b16 a[28:29], v227 offset:46080\n" \
  "ds_read_b64_tr_b16 a[30:31], v227 offset:46592\n" \
  "v_mul_f32_e32 v166, v166, v242\n" \
  "v_mul_f32_e32 v167, v167, v243\n" \
  "v_mfma_f32_32x32x16_bf16 v[144:159], v[40:43], a[12:15], v[144:159]\n" \
  "v_cvt_pk_bf16_f32 v178, v164, v165\n" \
  "v_cvt_pk_bf16_f32 v179, v166, v167\n" \
  "global_store_dwordx4 v231, v[176:179], s[74:75] offset:0 sc1\n" \
  "v_mul_f32_e32 v236, v168, v168\n" \
  "s_waitcnt lgkmcnt(0)\n" \
  "v_mfma_f32_32x32x16_bf16 v[96:111], v[44:47], a[16:19], v[96:111]\n" \
  "ds_read_b64_tr_b16 a[0:1], v226 offset:49152\n" \
  "ds_read_b64_tr_b16 a[2:3], v226 offset:49664\n" \
  "ds_read_b64_tr_b16 a[4:5], v226 offset:50176\n" \
  "global_load_lds_dwordx4 v230, s[62:63] offset:3072\n" \
  "v_add_u32_e32 v230, s66, v230\n" \
  "v_mfma_f32_32x32x16_bf16 v[112:127], v[44:47], a[20:23], v[112:127]\n" \
  "ds_read_b64_tr_b16 a[6:7], v226 offset:50688\n" \
  "ds_read_b64_tr_b16 a[8:9], v227 offset:49152\n" \
  "ds_read_b64_tr_b16 a[10:11], v227 offset:49664\n" \
  "v_mul_f32_e32 v237, v169, v169\n" \
  "v_mul_f32_e32 v238, v170, v170\n" \
  "v_mfma_f32_32x32x16_bf16 v[128:143], v[44:47], a[24:27], v[128:143]\n" \
  "ds_read_b64_tr_b16 a[12:13], v227 offset:50176\n" \
  "ds_read_b64_tr_b16 a[14:15], v227 offset:50688\n" \
  "v_mul_f32_e32 v239, v171, v171\n" \
  "v_fma_f32 v236, v236, v232, s77\n" \
  "v_fma_f32 v237, v237, v232, s77\n" \
  "v_mfma_f32_32x32x16_bf16 v[144:159], v[44:47], a[28:31], v[144:159]\n" \
  "v_fma_f32 v238, v238, v232, s77\n" \
  "v_fma_f32 v239, v239, v232, s77\n" \
  "s_waitcnt vmcnt(14) lgkmcnt(0)\n" \
  "s_barrier\n" \
  "v_mfma_f32_32x32x16_bf16 v[96:111], v[48:51], a[0:3], v[96:111]\n" \
  "s_add_u32 m0, s67, 65536\n" \
  "ds_read_b64_tr_b16 a[16:17], v226 offset:53248\n" \
  "ds_read_b64_tr_b16 a[18:19], v226 offset:53760\n" \
  "ds_read_b64_tr_b16 a[20:21], v226 offset:54272\n" \
  "global_load_lds_dwordx4 v230, s[56:57] offset:0\n" \
  "v_mfma_f32_32x32x16_bf16 v[112:127], v[48:51], a[4:7], v[112:127]\n" \
  "ds_read_b64_tr_b16 a[22:23], v226 offset:54784\n" \
  "ds_read_b64_tr_b16 a[24:25], v227 offset:53248\n" \
  "ds_read_b64_tr_b16 a[26:27], v227 offset:53760\n" \
  "v_mul_f32_e32 v236, v236, v168\n" \
  "v_mul_f32_e32 v237, v237, v169\n" \
  "v_mfma_f32_32x32x16_bf16 v[128:143], v[48:51], a[8:11], v[128:143]\n" \
  "ds_read_b64_tr_b16 a[28:29], v227 offset:54272\n" \
  "ds_read_b64_tr_b16 a[30:31], v227 offset:54784\n" \
  "v_mul_f32_e32 v238, v238, v170\n" \
  "v_mul_f32_e32 v239, v239, v171\n" \
  "v_exp_f32_e32 v236, v236\n" \
  "v_mfma_f32_32x32x16_bf16 v[144:159], v[48:51], a[12:15], v[144:159]\n" \
  "v_mul_f32_e32 v168, v168, v184\n" \
  "v_exp_f32_e32 v237, v237\n" \
  "s_waitcnt lgkmcnt(0)\n" \
  "v_mfma_f32_32x32x16_bf16 v[96:111], v[52:55], a[16:19], v[96:111]\n" \
  "ds_read_b64_tr_b16 a[0:1], v226 offset:57344\n" \
  "ds_read_b64_tr_b16 a[2:3], v226 offset:57856\n" \
  "ds_read_b64_tr_b16 a[4:5], v226 offset:58368\n" \
  "global_load_lds_dwordx4 v230, s[58:59] offset:1024\n" \
  "v_mul_f32_e32 v169, v169, v185\n" \
  "v_mfma_f32_32x32x16_bf16 v[112:127], v[52:55], a[20:23], v[112:127]\n" \
  "ds_read_b64_tr_b16 a[6:7], v226 offset:58880\n" \
  "ds_read_b64_tr_b16 a[8:9], v227 offset:57344\n" \
  "ds_read_b64_tr_b16 a[10:11], v227 offset:57856\n" \
  "v_exp_f32_e32 v238, v238\n" \
  "v_mul_f32_e32 v170, v170, v186\n" \
  "v_mfma_f32_32x32x16_bf16 v[128:143], v[52:55], a[24:27], v[128:143]\n" \
  "ds_read_b64_tr_b16 a[12:13], v227 offset:58368\n" \
  "ds_read_b64_tr_b16 a[14:15], v227 offset:58880\n" \
  "v_exp_f32_e32 v239, v239\n" \
  "v_mul_f32_e32 v171, v171, v187\n" \
  "v_add_f32_e32 v236, 1.0, v236\n" \
  "v_mfma_f32_32x32x16_bf16 v[144:159], v[52:55], a[28:31], v[144:159]\n" \
  "v_add_f32_e32 v237, 1.0, v237\n" \
  "v_rcp_f32_e32 v236, v236\n" \
  "s_waitcnt lgkmcnt(0)\n" \
  "v_mfma_f32_32x32x16_bf16 v[96:111], v[56:59], a[0:3], v[96:111]\n" \
  "ds_read_b64_tr_b16 a[16:17], v226 offset:61440\n" \
  "ds_read_b64_tr_b16 a[18:19], v226 offset:61952\n" \
  "ds_read_b64_tr_b16 a[20:21], v226 offset:62464\n" \
  "global_load_lds_dwordx4 v230, s[60:61] offset:2048\n" \
  "v_mfma_f32_32x32x16_bf16 v[112:127], v[56:59], a[4:7], v[112:127]\n" \
  "ds_read_b64_tr_b16 a[22:23], v226 offset:62976\n" \
  "ds_read_b64_tr_b16 a[24:25], v227 offset:61440\n" \
  "ds_read_b64_tr_b16 a[26:27], v227 offset:61952\n" \
  "v_add_f32_e32 v238, 1.0, v238\n" \
  "v_mfma_f32_32x32x16_bf16 v[128:143], v[56:59], a[8:11], v[128:143]\n" \
  "ds_read_b64_tr_b16 a[28:29], v227 offset:62464\n" \
  "ds_read_b64_tr_b16 a[30:31], v227 offset:62976\n" \
  "v_rcp_f32_e32 v237, v237\n" \
  "v_add_f32_e32 v239, 1.0, v239\n" \
  "v_mfma_f32_32x32x16_bf16 v[144:159], v[56:59], a[12:15], v[144:159]\n" \
  "v_rcp_f32_e32 v238, v238\n" \
  "v_mul_f32_e32 v168, v168, v236\n" \
  "v_rcp_f32_e32 v239, v239\n" \
  "v_mul_f32_e32 v169, v169, v237\n" \
  "s_waitcnt lgkmcnt(0)\n" \
  "v_mfma_f32_32x32x16_bf16 v[96:111], v[60:63], a[16:19], v[96:111]\n" \
  "ds_read_b64_tr_b16 a[0:1], v224 offset:0\n" \
  "ds_read_b64_tr_b16 a[2:3], v224 offset:512\n" \
  "ds_read_b64_tr_b16 a[4:5], v224 offset:1024\n" \
  "global_load_lds_dwordx4 v230, s[62:63] offset:3072\n" \
  "v_add_u32_e32 v230, s66, v230\n" \
  "v_mfma_f32_32x32x16_bf16 v[112:127], v[60:63], a[20:23], v[112:127]\n" \
  "ds_read_b64_tr_b16 a[6:7], v224 offset:1536\n" \
  "ds_read_b64_tr_b16 a[8:9], v225 offset:0\n" \
  "ds_read_b64_tr_b16 a[10:11], v225 offset:512\n" \
  "v_mul_f32_e32 v170, v170, v238\n" \
  "v_mul_f32_e32 v171, v171, v239\n" \
  "v_mfma_f32_32x32x16_bf16 v[128:143], v[60:63], a[24:27], v[128:143]\n" \
  "ds_read_b64_tr_b16 a[12:13], v225 offset:1024\n" \
  "ds_read_b64_tr_b16 a[14:15], v225 offset:1536\n" \
  "v_cvt_pk_bf16_f32 v180, v168, v169\n" \
  "v_cvt_pk_bf16_f32 v181, v170, v171\n" \
  "v_mul_f32_e32 v240, v172, v172\n" \
  "v_mfma_f32_32x32x16_bf16 v[144:159], v[60:63], a[28:31], v[144:159]\n" \
  "v_mul_f32_e32 v241, v173, v173\n" \
  "v_mul_f32_e32 v242, v174, v174\n" \
  "s_waitcnt vmcnt(13) lgkmcnt(0)\n" \
  "s_barrier\n" \
  "v_mfma_f32_32x32x16_bf16 v[96:111], v[64:67], a[0:3], v[96:111]\n" \
  "s_add_u32 m0, s67, 81920\n" \
  "ds_read_b64_tr_b16 a[16:17], v224 offset:4096\n" \
  "ds_read_b64_tr_b16 a[18:19], v224 offset:4608\n" \
  "ds_read_b64_tr_b16 a[20:21], v224 offset:5120\n" \
  "global_load_lds_dwordx4 v230, s[56:57] offset:0\n" \
  "v_mfma_f32_32x32x16_bf16 v[112:127], v[64:67], a[4:7], v[112:127]\n" \
  "ds_read_b64_tr_b16 a[22:23], v224 offset:5632\n" \
  "ds_read_b64_tr_b16 a[24:25], v225 offset:4096\n" \
  "ds_read_b64_tr_b16 a[26:27], v225 offset:4608\n" \
  "v_mul_f32_e32 v243, v175, v175\n" \
  "v_fma_f32 v240, v240, v232, s77\n" \
  "v_mfma_f32_32x32x16_bf16 v[128:143], v[64:67], a[8:11], v[128:143]\n" \
  "ds_read_b64_tr_b16 a[28:29], v225 offset:5120\n" \
  "ds_read_b64_tr_b16 a[30:31], v225 offset:5632\n" \
  "v_fma_f32 v241, v241, v232, s77\n" \
  "v_fma_f32 v242, v242, v232, s77\n" \
  "v_fma_f32 v243, v243, v232, s77\n" \
  "v_mfma_f32_32x32x16_bf16 v[144:159], v[64:67], a[12:15], v[144:159]\n" \
  "v_mul_f32_e32 v240, v240, v172\n" \
  "v_mul_f32_e32 v241, v241, v173\n" \
  "s_waitcnt lgkmcnt(0)\n" \
  "v_mfma_f32_32x32x16_bf16 v[96:111], v[68:71], a[16:19], v[96:111]\n" \
  "ds_read_b64_tr_b16 a[0:1], v224 offset:8192\n" \
  "ds_read_b64_tr_b16 a[2:3], v224 offset:8704\n" \
  "ds_read_b64_tr_b16 a[4:5], v224 offset:9216\n" \
  "global_load_lds_dwordx4 v230, s[58:59] offset:1024\n" \
  "v_mfma_f32_32x32x16_bf16 v[112:127], v[68:71], a[20:23], v[112:127]\n" \
  "ds_read_b64_tr_b16 a[6:7], v224 offset:9728\n" \
  "ds_read_b64_tr_b16 a[8:9], v225 offset:8192\n" \
  "ds_read_b64_tr_b16 a[10:11], v225 offset:8704\n" \
  "v_mul_f32_e32 v242, v242, v174\n" \
  "v_mfma_f32_32x32x16_bf16 v[128:143], v[68:71], a[24:27], v[128:143]\n" \
  "ds_read_b64_tr_b16 a[12:13], v225 offset:9216\n" \
  "ds_read_b64_tr_b16 a[14:15], v225 offset:9728\n" \
  "v_mul_f32_e32 v243, v243, v175\n" \
  "v_exp_f32_e32 v240, v240\n" \
  "v_mfma_f32_32x32x16_bf16 v[144:159], v[68:71], a[28:31], v[144:159]\n" \
  "v_mul_f32_e32 v172, v172, v188\n" \
  "v_exp_f32_e32 v241, v241\n" \
  "v_mul_f32_e32 v173, v173, v189\n" \
  "v_exp_f32_e32 v242, v242\n" \
  "s_waitcnt lgkmcnt(0)\n" \
  "v_mfma_f32_32x32x16_bf16 v[96:111], v[72:75], a[0:3], v[96:111]\n" \
  "ds_read_b64_tr_b16 a[16:17], v224 offset:12288\n" \
  "ds_read_b64_tr_b16 a[18:19], v224 offset:12800\n" \
  "ds_read_b64_tr_b16 a[20:21], v224 offset:13312\n" \
  "global_load_lds_dwordx4 v230, s[60:61] offset:2048\n" \
  "v_mfma_f32_32x32x16_bf16 v[112:127], v[72:75], a[4:7], v[112:127]\n" \
  "ds_read_b64_tr_b16 a[22:23], v224 offset:13824\n" \
  "ds_read_b64_tr_b16 a[24:25], v225 offset:12288\n" \
  "ds_read_b64_tr_b16 a[26:27], v225 offset:12800\n" \
  "v_mul_f32_e32 v174, v174, v190\n" \
  "v_mfma_f32_32x32x16_bf16 v[128:143], v[72:75], a[8:11], v[128:143]\n" \
  "ds_read_b64_tr_b16 a[28:29], v225 offset:13312\n" \
  "ds_read_b64_tr_b16 a[30:31], v225 offset:13824\n" \
  "v_exp_f32_e32 v243, v243\n" \
  "v_mul_f32_e32 v175, v175, v191\n" \
  "v_mfma_f32_32x32x16_bf16 v[144:159], v[72:75], a[12:15], v[144:159]\n" \
  "v_add_f32_e32 v240, 1.0, v240\n" \
  "v_add_f32_e32 v241, 1.0, v241\n" \
  "v_rcp_f32_e32 v240, v240\n" \
  "v_add_f32_e32 v242, 1.0, v242\n" \
  "s_waitcnt lgkmcnt(0)\n" \
  "v_mfma_f32_32x32x16_bf16 v[96:111], v[76:79], a[16:19], v[96:111]\n" \
  "ds_read_b64_tr_b16 a[0:1], v224 offset:16384\n" \
  "ds_read_b64_tr_b16 a[2:3], v224 offset:16896\n" \
  "ds_read_b64_tr_b16 a[4:5], v224 offset:17408\n" \
  "global_load_lds_dwordx4 v230, s[62:63] offset:3072\n" \
  "v_add_u32_e32 v230, s66, v230\n" \
  "v_mfma_f32_32x32x16_bf16 v[112:127], v[76:79], a[20:23], v[112:127]\n" \
  "ds_read_b64_tr_b16 a[6:7], v224 offset:17920\n" \
  "ds_read_b64_tr_b16 a[8:9], v225 offset:16384\n" \
  "ds_read_b64_tr_b16 a[10:11], v225 offset:16896\n" \
  "v_rcp_f32_e32 v241, v241\n" \
  "v_add_f32_e32 v243, 1.0, v243\n" \
  "v_mfma_f32_32x32x16_bf16 v[128:143], v[76:79], a[24:27], v[128:143]\n" \
  "ds_read_b64_tr_b16 a[12:13], v225 offset:17408\n" \
  "ds_read_b64_tr_b16 a[14:15], v225 offset:17920\n" \
  "v_rcp_f32_e32 v242, v242\n" \
  "v_mul_f32_e32 v172, v172, v240\n" \
  "v_rcp_f32_e32 v243, v243\n" \
  "v_mfma_f32_32x32x16_bf16 v[144:159], v[76:79], a[28:31], v[144:159]\n" \
  "v_mul_f32_e32 v173, v173, v241\n" \
  "v_mul_f32_e32 v174, v174, v242\n" \
  "s_waitcnt vmcnt(13) lgkmcnt(0)\n" \
  "s_barrier\n" \
  "v_mfma_f32_32x32x16_bf16 v[96:111], v[80:83], a[0:3], v[96:111]\n" \
  "ds_read_b64_tr_b16 a[16:17], v224 offset:20480\n" \
  "ds_read_b64_tr_b16 a[18:19], v224 offset:20992\n" \
  "ds_read_b64_tr_b16 a[20:21], v224 offset:21504\n" \
  "v_mul_f32_e32 v175, v175, v243\n" \
  "v_mfma_f32_32x32x16_bf16 v[112:127], v[80:83], a[4:7], v[112:127]\n" \
  "ds_read_b64_tr_b16 a[22:23], v224 offset:22016\n" \
  "ds_read_b64_tr_b16 a[24:25], v225 offset:20480\n" \
  "ds_read_b64_tr_b16 a[26:27], v225 offset:20992\n" \
  "v_cvt_pk_bf16_f32 v182, v172, v173\n" \
  "v_mfma_f32_32x32x16_bf16 v[128:143], v[80:83], a[8:11], v[128:143]\n" \
  "ds_read_b64_tr_b16 a[28:29], v225 offset:21504\n" \
  "ds_read_b64_tr_b16 a[30:31], v225 offset:22016\n" \
  "v_cvt_pk_bf16_f32 v183, v174, v175\n" \
  "global_store_dwordx4 v231, v[180:183], s[74:75] offset:1024 sc1\n" \
  "v_mfma_f32_32x32x16_bf16 v[144:159], v[80:83], a[12:15], v[144:159]\n" \
  "v_mul_f32_e32 v236, v192, v192\n" \
  "v_mul_f32_e32 v237, v193, v193\n" \
  "v_mul_f32_e32 v238, v194, v194\n" \
  "s_waitcnt lgkmcnt(0)\n" \
  "v_mfma_f32_32x32x16_bf16 v[96:111], v[84:87], a[16:19], v[96:111]\n" \
  "ds_read_b64_tr_b16 a[0:1], v224 offset:24576\n" \
  "ds_read_b64_tr_b16 a[2:3], v224 offset:25088\n" \
  "ds_read_b64_tr_b16 a[4:5], v224 offset:25600\n" \
  "v_mul_f32_e32 v239, v195, v195\n" \
  "v_mfma_f32_32x32x16_bf16 v[112:127], v[84:87], a[20:23], v[112:127]\n" \
  "ds_read_b64_tr_b16 a[6:7], v224 offset:26112\n" \
  "ds_read_b64_tr_b16 a[8:9], v225 offset:24576\n" \
  "ds_read_b64_tr_b16 a[10:11], v225 offset:25088\n" \
  "v_fma_f32 v236, v236, v232, s77\n" \
  "v_mfma_f32_32x32x16_bf16 v[128:143], v[84:87], a[24:27], v[128:143]\n" \
  "ds_read_b64_tr_b16 a[12:13], v225 offset:25600\n" \
  "ds_read_b64_tr_b16 a[14:15], v225 offset:26112\n" \
  "v_fma_f32 v237, v237, v232, s77\n" \
  "v_fma_f32 v238, v238, v232, s77\n" \
  "v_mfma_f32_32x32x16_bf16 v[144:159], v[84:87], a[28:31], v[144:159]\n" \
  "v_fma_f32 v239, v239, v232, s77\n" \
  "v_mul_f32_e32 v236, v236, v192\n" \
  "v_mul_f32_e32 v237, v237, v193\n" \
  "s_waitcnt lgkmcnt(0)\n" \
  "v_mfma_f32_32x32x16_bf16 v[96:111], v[88:91], a[0:3], v[96:111]\n" \
  "ds_read_b64_tr_b16 a[16:17], v224 offset:28672\n" \
  "ds_read_b64_tr_b16 a[18:19], v224 offset:29184\n" \
  "ds_read_b64_tr_b16 a[20:21], v224 offset:29696\n" \
  "v_mul_f32_e32 v238, v238, v194\n" \
  "v_mfma_f32_32x32x16_bf16 v[112:127], v[88:91], a[4:7], v[112:127]\n" \
  "ds_read_b64_tr_b16 a[22:23], v224 offset:30208\n" \
  "ds_read_b64_tr_b16 a[24:25], v225 offset:28672\n" \
  "ds_read_b64_tr_b16 a[26:27], v225 offset:29184\n" \
  "v_mul_f32_e32 v239, v239, v195\n" \
  "v_mfma_f32_32x32x16_bf16 v[128:143], v[88:91], a[8:11], v[128:143]\n" \
  "ds_read_b64_tr_b16 a[28:29], v225 offset:29696\n" \
  "ds_read_b64_tr_b16 a[30:31], v225 offset:30208\n" \
  "v_exp_f32_e32 v236, v236\n" \
  "v_mul_f32_e32 v192, v192, v208\n" \
  "v_mfma_f32_32x32x16_bf16 v[144:159], v[88:91], a[12:15], v[144:159]\n" \
  "v_exp_f32_e32 v237, v237\n" \
  "v_mul_f32_e32 v193, v193, v209\n" \
  "v_exp_f32_e32 v238, v238\n" \
  "s_waitcnt lgkmcnt(0)\n" \
  "v_mfma_f32_32x32x16_bf16 v[96:111], v[92:95], a[16:19], v[96:111]\n" \
  "ds_read_b64_tr_b16 a[0:1], v224 offset:32768\n" \
  "ds_read_b64_tr_b16 a[2:3], v224 offset:33280\n" \
  "ds_read_b64_tr_b16 a[4:5], v224 offset:33792\n" \
  "v_mul_f32_e32 v194, v194, v210\n" \
  "v_mfma_f32_32x32x16_bf16 v[112:127], v[92:95], a[20:23], v[112:127]\n" \
  "ds_read_b64_tr_b16 a[6:7], v224 offset:34304\n" \
  "ds_read_b64_tr_b16 a[8:9], v225 offset:32768\n" \
  "ds_read_b64_tr_b16 a[10:11], v225 offset:33280\n" \
  "v_exp_f32_e32 v239, v239\n" \
  "v_mfma_f32_32x32x16_bf16 v[128:143], v[92:95], a[24:27], v[128:143]\n" \
  "ds_read_b64_tr_b16 a[12:13], v225 offset:33792\n" \
  "ds_read_b64_tr_b16 a[14:15], v225 offset:34304\n" \
  "v_mul_f32_e32 v195, v195, v211\n" \
  "v_add_f32_e32 v236, 1.0, v236\n" \
  "v_mfma_f32_32x32x16_bf16 v[144:159], v[92:95], a[28:31], v[144:159]\n" \
  "v_add_f32_e32 v237, 1.0, v237\n" \
  "v_rcp_f32_e32 v236, v236\n" \
  "v_add_f32_e32 v238, 1.0, v238\n" \
  "s_waitcnt vmcnt(9) lgkmcnt(0)\n" \
  "s_barrier\n" \
  "v_mfma_f32_32x32x16_bf16 v[96:111], a[32:35], a[0:3], v[96:111]\n" \
  "ds_read_b64_tr_b16 a[16:17], v224 offset:36864\n" \
  "ds_read_b64_tr_b16 a[18:19], v224 offset:37376\n" \
  "ds_read_b64_tr_b16 a[20:21], v224 offset:37888\n" \
  "v_rcp_f32_e32 v237, v237\n" \
  "v_mfma_f32_32x32x16_bf16 v[112:127], a[32:35], a[4:7], v[112:127]\n" \
  "ds_read_b64_tr_b16 a[22:23], v224 offset:38400\n" \
  "ds_read_b64_tr_b16 a[24:25], v225 offset:36864\n" \
  "ds_read_b64_tr_b16 a[26:27], v225 offset:37376\n" \
  "v_add_f32_e32 v239, 1.0, v239\n" \
  "v_mfma_f32_32x32x16_bf16 v[128:143], a[32:35], a[8:11], v[128:143]\n" \
  "ds_read_b64_tr_b16 a[28:29], v225 offset:37888\n" \
  "ds_read_b64_tr_b16 a[30:31], v225 offset:38400\n" \
  "v_rcp_f32_e32 v238, v238\n" \
  "v_mul_f32_e32 v192, v192, v236\n" \
  "v_mfma_f32_32x32x16_bf16 v[144:159], a[32:35], a[12:15], v[144:159]\n" \
  "v_rcp_f32_e32 v239, v239\n" \
  "v_mul_f32_e32 v193, v193, v237\n" \
  "v_mul_f32_e32 v194, v194, v238\n" \
  "s_waitcnt lgkmcnt(0)\n" \
  "v_mfma_f32_32x32x16_bf16 v[96:111], a[36:39], a[16:19], v[96:111]\n" \
  "ds_read_b64_tr_b16 a[0:1], v224 offset:40960\n" \
  "ds_read_b64_tr_b16 a[2:3], v224 offset:41472\n" \
  "ds_read_b64_tr_b16 a[4:5], v224 offset:41984\n" \
  "v_mul_f32_e32 v195, v195, v239\n" \
  "v_mfma_f32_32x32x16_bf16 v[112:127], a[36:39], a[20:23], v[112:127]\n" \
  "ds_read_b64_tr_b16 a[6:7], v224 offset:42496\n" \
  "ds_read_b64_tr_b16 a[8:9], v225 offset:40960\n" \
  "ds_read_b64_tr_b16 a[10:11], v225 offset:41472\n" \
  "v_cvt_pk_bf16_f32 v208, v192, v193\n" \
  "v_mfma_f32_32x32x16_bf16 v[128:143], a[36:39], a[24:27], v[128:143]\n" \
  "ds_read_b64_tr_b16 a[12:13], v225 offset:41984\n" \
  "ds_read_b64_tr_b16 a[14:15], v225 offset:42496\n" \
  "v_cvt_pk_bf16_f32 v209, v194, v195\n" \
  "v_mul_f32_e32 v240, v196, v196\n" \
  "v_mfma_f32_32x32x16_bf16 v[144:159], a[36:39], a[28:31], v[144:159]\n" \
  "v_mul_f32_e32 v241, v197, v197\n" \
  "v_mul_f32_e32 v242, v198, v198\n" \
  "v_mul_f32_e32 v243, v199, v199\n" \
  "s_waitcnt lgkmcnt(0)\n" \
  "v_mfma_f32_32x32x16_bf16 v[96:111], a[40:43], a[0:3], v[96:111]\n" \
  "ds_read_b64_tr_b16 a[16:17], v224 offset:45056\n" \
  "ds_read_b64_tr_b16 a[18:19], v224 offset:45568\n" \
  "ds_read_b64_tr_b16 a[20:21], v224 offset:46080\n" \
  "v_fma_f32 v240, v240, v232, s77\n" \
  "v_mfma_f32_32x32x16_bf16 v[112:127], a[40:43], a[4:7], v[112:127]\n" \
  "ds_read_b64_tr_b16 a[22:23], v224 offset:46592\n" \
  "ds_read_b64_tr_b16 a[24:25], v225 offset:45056\n" \
  "ds_read_b64_tr_b16 a[26:27], v225 offset:45568\n" \
  "v_fma_f32 v241, v241, v232, s77\n" \
  "v_mfma_f32_32x32x16_bf16 v[128:143], a[40:43], a[8:11], v[128:143]\n" \
  "ds_read_b64_tr_b16 a[28:29], v225 offset:46080\n" \
  "ds_read_b64_tr_b16 a[30:31], v225 offset:46592\n" \
  "v_fma_f32 v242, v242, v232, s77\n" \
  "v_fma_f32 v243, v243, v232, s77\n" \
  "v_mfma_f32_32x32x16_bf16 v[144:159], a[40:43], a[12:15], v[144:159]\n" \
  "v_mul_f32_e32 v240, v240, v196\n" \
  "v_mul_f32_e32 v241, v241, v197\n" \
  "v_mul_f32_e32 v242, v242, v198\n" \
  "v_mul_f32_e32 v243, v243, v199\n" \
  "s_waitcnt lgkmcnt(0)\n" \
  "v_mfma_f32_32x32x16_bf16 v[96:111], a[44:47], a[16:19], v[96:111]\n" \
  "ds_read_b64_tr_b16 a[0:1], v224 offset:49152\n" \
  "ds_read_b64_tr_b16 a[2:3], v224 offset:49664\n" \
  "ds_read_b64_tr_b16 a[4:5], v224 offset:50176\n" \
  "v_exp_f32_e32 v240, v240\n" \
  "v_mfma_f32_32x32x16_bf16 v[112:127], a[44:47], a[20:23], v[112:127]\n" \
  "ds_read_b64_tr_b16 a[6:7], v224 offset:50688\n" \
  "ds_read_b64_tr_b16 a[8:9], v225 offset:49152\n" \
  "ds_read_b64_tr_b16 a[10:11], v225 offset:49664\n" \
  "v_mul_f32_e32 v196, v196, v212\n" \
  "v_mfma_f32_32x32x16_bf16 v[128:143], a[44:47], a[24:27], v[128:143]\n" \
  "ds_read_b64_tr_b16 a[12:13], v225 offset:50176\n" \
  "ds_read_b64_tr_b16 a[14:15], v225 offset:50688\n" \
  "v_exp_f32_e32 v241, v241\n" \
  "v_mul_f32_e32 v197, v197, v213\n" \
  "v_mfma_f32_32x32x16_bf16 v[144:159], a[44:47], a[28:31], v[144:159]\n" \
  "v_exp_f32_e32 v242, v242\n" \
  "v_mul_f32_e32 v198, v198, v214\n" \
  "v_exp_f32_e32 v243, v243\n" \
  "s_waitcnt vmcnt(5) lgkmcnt(0)\n" \
  "s_barrier\n" \
  "v_mfma_f32_32x32x16_bf16 v[96:111], a[48:51], a[0:3], v[96:111]\n" \
  "ds_read_b64_tr_b16 a[16:17], v224 offset:53248\n" \
  "ds_read_b64_tr_b16 a[18:19], v224 offset:53760\n" \
  "ds_read_b64_tr_b16 a[20:21], v224 offset:54272\n" \
  "v_mul_f32_e32 v199, v199, v215\n" \
  "v_mfma_f32_32x32x16_bf16 v[112:127], a[48:51], a[4:7], v[112:127]\n" \
  "ds_read_b64_tr_b16 a[22:23], v224 offset:54784\n" \
  "ds_read_b64_tr_b16 a[24:25], v225 offset:53248\n" \
  "ds_read_b64_tr_b16 a[26:27], v225 offset:53760\n" \
  "v_add_f32_e32 v240, 1.0, v240\n" \
  "v_mfma_f32_32x32x16_bf16 v[128:143], a[48:51], a[8:11], v[128:143]\n" \
  "ds_read_b64_tr_b16 a[28:29], v225 offset:54272\n" \
  "ds_read_b64_tr_b16 a[30:31], v225 offset:54784\n" \
  "v_add_f32_e32 v241, 1.0, v241\n" \
  "v_rcp_f32_e32 v240, v240\n" \
  "v_mfma_f32_32x32x16_bf16 v[144:159], a[48:51], a[12:15], v[144:159]\n" \
  "v_add_f32_e32 v242, 1.0, v242\n" \
  "v_rcp_f32_e32 v241, v241\n" \
  "v_add_f32_e32 v243, 1.0, v243\n" \
  "s_waitcnt lgkmcnt(0)\n" \
  "v_mfma_f32_32x32x16_bf16 v[96:111], a[52:55], a[16:19], v[96:111]\n" \
  "ds_read_b64_tr_b16 a[0:1], v224 offset:57344\n" \
  "ds_read_b64_tr_b16 a[2:3], v224 offset:57856\n" \
  "ds_read_b64_tr_b16 a[4:5], v224 offset:58368\n" \
  "v_rcp_f32_e32 v242, v242\n" \
  "v_mfma_f32_32x32x16_bf16 v[112:127], a[52:55], a[20:23], v[112:127]\n" \
  "ds_read_b64_tr_b16 a[6:7], v224 offset:58880\n" \
  "ds_read_b64_tr_b16 a[8:9], v225 offset:57344\n" \
  "ds_read_b64_tr_b16 a[10:11], v225 offset:57856\n" \
  "v_mul_f32_e32 v196, v196, v240\n" \
  "v_mfma_f32_32x32x16_bf16 v[128:143], a[52:55], a[24:27], v[128:143]\n" \
  "ds_read_b64_tr_b16 a[12:13], v225 offset:58368\n" \
  "ds_read_b64_tr_b16 a[14:15], v225 offset:58880\n" \
  "v_rcp_f32_e32 v243, v243\n" \
  "v_mul_f32_e32 v197, v197, v241\n" \
  "v_mfma_f32_32x32x16_bf16 v[144:159], a[52:55], a[28:31], v[144:159]\n" \
  "v_mul_f32_e32 v198, v198, v242\n" \
  "v_mul_f32_e32 v199, v199, v243\n" \
  "v_cvt_pk_bf16_f32 v210, v196, v197\n" \
  "s_waitcnt lgkmcnt(0)\n" \
  "v_mfma_f32_32x32x16_bf16 v[96:111], a[56:59], a[0:3], v[96:111]\n" \
  "ds_read_b64_tr_b16 a[16:17], v224 offset:61440\n" \
  "ds_read_b64_tr_b16 a[18:19], v224 offset:61952\n" \
  "ds_read_b64_tr_b16 a[20:21], v224 offset:62464\n" \
  "v_cvt_pk_bf16_f32 v211, v198, v199\n" \
  "v_mfma_f32_32x32x16_bf16 v[112:127], a[56:59], a[4:7], v[112:127]\n" \
  "ds_read_b64_tr_b16 a[22:23], v224 offset:62976\n" \
  "ds_read_b64_tr_b16 a[24:25], v225 offset:61440\n" \
  "ds_read_b64_tr_b16 a[26:27], v225 offset:61952\n" \
  "global_store_dwordx4 v231, v[208:211], s[74:75] offset:2048 sc1\n" \
  "v_mfma_f32_32x32x16_bf16 v[128:143], a[56:59], a[8:11], v[128:143]\n" \
  "ds_read_b64_tr_b16 a[28:29], v225 offset:62464\n" \
  "ds_read_b64_tr_b16 a[30:31], v225 offset:62976\n" \
  "v_mul_f32_e32 v236, v200, v200\n" \
  "v_mul_f32_e32 v237, v201, v201\n" \
  "v_mfma_f32_32x32x16_bf16 v[144:159], a[56:59], a[12:15], v[144:159]\n" \
  "v_mul_f32_e32 v238, v202, v202\n" \
  "v_mul_f32_e32 v239, v203, v203\n" \
  "v_fma_f32 v236, v236, v232, s77\n" \
  "s_waitcnt lgkmcnt(0)\n" \
  "v_mfma_f32_32x32x16_bf16 v[96:111], a[60:63], a[16:19], v[96:111]\n" \
  "ds_read_b64_tr_b16 a[0:1], v226 offset:0\n" \
  "ds_read_b64_tr_b16 a[2:3], v226 offset:512\n" \
  "ds_read_b64_tr_b16 a[4:5], v226 offset:1024\n" \
  "v_fma_f32 v237, v237, v232, s77\n" \
  "v_mfma_f32_32x32x16_bf16 v[112:127], a[60:63], a[20:23], v[112:127]\n" \
  "ds_read_b64_tr_b16 a[6:7], v226 offset:1536\n" \
  "ds_read_b64_tr_b16 a[8:9], v227 offset:0\n" \
  "ds_read_b64_tr_b16 a[10:11], v227 offset:512\n" \
  "v_fma_f32 v238, v238, v232, s77\n" \
  "v_mfma_f32_32x32x16_bf16 v[128:143], a[60:63], a[24:27], v[128:143]\n" \
  "ds_read_b64_tr_b16 a[12:13], v227 offset:1024\n" \
  "ds_read_b64_tr_b16 a[14:15], v227 offset:1536\n" \
  "v_fma_f32 v239, v239, v232, s77\n" \
  "v_mul_f32_e32 v236, v236, v200\n" \
  "v_mfma_f32_32x32x16_bf16 v[144:159], a[60:63], a[28:31], v[144:159]\n" \
  "v_mul_f32_e32 v237, v237, v201\n" \
  "v_mul_f32_e32 v238, v238, v202\n" \
  "v_mul_f32_e32 v239, v239, v203\n" \
  "s_waitcnt vmcnt(2) lgkmcnt(0)\n" \
  "s_barrier\n" \
  "v_mfma_f32_32x32x16_bf16 v[96:111], a[64:67], a[0:3], v[96:111]\n" \
  "ds_read_b64_tr_b16 a[16:17], v226 offset:4096\n" \
  "ds_read_b64_tr_b16 a[18:19], v226 offset:4608\n" \
  "ds_read_b64_tr_b16 a[20:21], v226 offset:5120\n" \
  "v_exp_f32_e32 v236, v236\n" \
  "v_mfma_f32_32x32x16_bf16 v[112:127], a[64:67], a[4:7], v[112:127]\n" \
  "ds_read_b64_tr_b16 a[22:23], v226 offset:5632\n" \
  "ds_read_b64_tr_b16 a[24:25], v227 offset:4096\n" \
  "ds_read_b64_tr_b16 a[26:27], v227 offset:4608\n" \
  "v_mul_f32_e32 v200, v200, v216\n" \
  "v_mfma_f32_32x32x16_bf16 v[128:143], a[64:67], a[8:11], v[128:143]\n" \
  "ds_read_b64_tr_b16 a[28:29], v227 offset:5120\n" \
  "ds_read_b64_tr_b16 a[30:31], v227 offset:5632\n" \
  "v_exp_f32_e32 v237, v237\n" \
  "v_mul_f32_e32 v201, v201, v217\n" \
  "v_mfma_f32_32x32x16_bf16 v[144:159], a[64:67], a[12:15], v[144:159]\n" \
  "v_exp_f32_e32 v238, v238\n" \
  "v_mul_f32_e32 v202, v202, v218\n" \
  "v_exp_f32_e32 v239, v239\n" \
  "s_waitcnt lgkmcnt(0)\n" \
  "v_mfma_f32_32x32x16_bf16 v[96:111], a[68:71], a[16:19], v[96:111]\n" \
  "ds_read_b64_tr_b16 a[0:1], v226 offset:8192\n" \
  "ds_read_b64_tr_b16 a[2:3], v226 offset:8704\n" \
  "ds_read_b64_tr_b16 a[4:5], v226 offset:9216\n" \
  "v_mul_f32_e32 v203, v203, v219\n" \
  "v_mfma_f32_32x32x16_bf16 v[112:127], a[68:71], a[20:23], v[112:127]\n" \
  "ds_read_b64_tr_b16 a[6:7], v226 offset:9728\n" \
  "ds_read_b64_tr_b16 a[8:9], v227 offset:8192\n" \
  "ds_read_b64_tr_b16 a[10:11], v227 offset:8704\n" \
  "v_add_f32_e32 v236, 1.0, v236\n" \
  "v_mfma_f32_32x32x16_bf16 v[128:143], a[68:71], a[24:27], v[128:143]\n" \
  "ds_read_b64_tr_b16 a[12:13], v227 offset:9216\n" \
  "ds_read_b64_tr_b16 a[14:15], v227 offset:9728\n" \
  "v_add_f32_e32 v237, 1.0, v237\n" \
  "v_rcp_f32_e32 v236, v236\n" \
  "v_mfma_f32_32x32x16_bf16 v[144:159], a[68:71], a[28:31], v[144:159]\n" \
  "v_add_f32_e32 v238, 1.0, v238\n" \
  "v_rcp_f32_e32 v237, v237\n" \
  "v_add_f32_e32 v239, 1.0, v239\n" \
  "s_waitcnt lgkmcnt(0)\n" \
  "v_mfma_f32_32x32x16_bf16 v[96:111], a[72:75], a[0:3], v[96:111]\n" \
  "ds_read_b64_tr_b16 a[16:17], v226 offset:12288\n" \
  "ds_read_b64_tr_b16 a[18:19], v226 offset:12800\n" \
  "ds_read_b64_tr_b16 a[20:21], v226 offset:13312\n" \
  "v_rcp_f32_e32 v238, v238\n" \
  "v_mfma_f32_32x32x16_bf16 v[112:127], a[72:75], a[4:7], v[112:127]\n" \
  "ds_read_b64_tr_b16 a[22:23], v226 offset:13824\n" \
  "ds_read_b64_tr_b16 a[24:25], v227 offset:12288\n" \
  "ds_read_b64_tr_b16 a[26:27], v227 offset:12800\n" \
  "v_mul_f32_e32 v200, v200, v236\n" \
  "v_mfma_f32_32x32x16_bf16 v[128:143], a[72:75], a[8:11], v[128:143]\n" \
  "ds_read_b64_tr_b16 a[28:29], v227 offset:13312\n" \
  "ds_read_b64_tr_b16 a[30:31], v227 offset:13824\n" \
  "v_rcp_f32_e32 v239, v239\n" \
  "v_mul_f32_e32 v201, v201, v237\n" \
  "v_mfma_f32_32x32x16_bf16 v[144:159], a[72:75], a[12:15], v[144:159]\n" \
  "v_mul_f32_e32 v202, v202, v238\n" \
  "v_mul_f32_e32 v203, v203, v239\n" \
  "v_cvt_pk_bf16_f32 v212, v200, v201\n" \
  "s_waitcnt lgkmcnt(0)\n" \
  "v_mfma_f32_32x32x16_bf16 v[96:111], a[76:79], a[16:19], v[96:111]\n" \
  "ds_read_b64_tr_b16 a[0:1], v226 offset:16384\n" \
  "ds_read_b64_tr_b16 a[2:3], v226 offset:16896\n" \
  "ds_read_b64_tr_b16 a[4:5], v226 offset:17408\n" \
  "v_cvt_pk_bf16_f32 v213, v202, v203\n" \
  "v_mfma_f32_32x32x16_bf16 v[112:127], a[76:79], a[20:23], v[112:127]\n" \
  "ds_read_b64_tr_b16 a[6:7], v226 offset:17920\n" \
  "ds_read_b64_tr_b16 a[8:9], v227 offset:16384\n" \
  "ds_read_b64_tr_b16 a[10:11], v227 offset:16896\n" \
  "v_mul_f32_e32 v240, v204, v204\n" \
  "v_mfma_f32_32x32x16_bf16 v[128:143], a[76:79], a[24:27], v[128:143]\n" \
  "ds_read_b64_tr_b16 a[12:13], v227 offset:17408\n" \
  "ds_read_b64_tr_b16 a[14:15], v227 offset:17920\n" \
  "v_mul_f32_e32 v241, v205, v205\n" \
  "v_mul_f32_e32 v242, v206, v206\n" \
  "v_mfma_f32_32x32x16_bf16 v[144:159], a[76:79], a[28:31], v[144:159]\n" \
  "v_mul_f32_e32 v243, v207, v207\n" \
  "v_fma_f32 v240, v240, v232, s77\n" \
  "v_fma_f32 v241, v241, v232, s77\n" \
  "s_waitcnt lgkmcnt(0)\n" \
  "v_mfma_f32_32x32x16_bf16 v[96:111], a[80:83], a[0:3], v[96:111]\n" \
  "ds_read_b64_tr_b16 a[16:17], v226 offset:20480\n" \
  "ds_read_b64_tr_b16 a[18:19], v226 offset:20992\n" \
  "ds_read_b64_tr_b16 a[20:21], v226 offset:21504\n" \
  "v_fma_f32 v242, v242, v232, s77\n" \
  "v_mfma_f32_32x32x16_bf16 v[112:127], a[80:83], a[4:7], v[112:127]\n" \
  "ds_read_b64_tr_b16 a[22:23], v226 offset:22016\n" \
  "ds_read_b64_tr_b16 a[24:25], v227 offset:20480\n" \
  "ds_read_b64_tr_b16 a[26:27], v227 offset:20992\n" \
  "v_fma_f32 v243, v243, v232, s77\n" \
  "v_mfma_f32_32x32x16_bf16 v[128:143], a[80:83], a[8:11], v[128:143]\n" \
  "ds_read_b64_tr_b16 a[28:29], v227 offset:21504\n" \
  "ds_read_b64_tr_b16 a[30:31], v227 offset:22016\n" \
  "v_mul_f32_e32 v240, v240, v204\n" \
  "v_mul_f32_e32 v241, v241, v205\n" \
  "v_mfma_f32_32x32x16_bf16 v[144:159], a[80:83], a[12:15], v[144:159]\n" \
  "v_mul_f32_e32 v242, v242, v206\n" \
  "v_mul_f32_e32 v243, v243, v207\n" \
  "v_exp_f32_e32 v240, v240\n" \
  "s_waitcnt lgkmcnt(0)\n" \
  "v_mfma_f32_32x32x16_bf16 v[96:111], a[84:87], a[16:19], v[96:111]\n" \
  "ds_read_b64_tr_b16 a[0:1], v226 offset:24576\n" \
  "ds_read_b64_tr_b16 a[2:3], v226 offset:25088\n" \
  "ds_read_b64_tr_b16 a[4:5], v226 offset:25600\n" \
  "v_mul_f32_e32 v204, v204, v220\n" \
  "v_mfma_f32_32x32x16_bf16 v[112:127], a[84:87], a[20:23], v[112:127]\n" \
  "ds_read_b64_tr_b16 a[6:7], v226 offset:26112\n" \
  "ds_read_b64_tr_b16 a[8:9], v227 offset:24576\n" \
  "ds_read_b64_tr_b16 a[10:11], v227 offset:25088\n" \
  "v_exp_f32_e32 v241, v241\n" \
  "v_mfma_f32_32x32x16_bf16 v[128:143], a[84:87], a[24:27], v[128:143]\n" \
  "ds_read_b64_tr_b16 a[12:13], v227 offset:25600\n" \
  "ds_read_b64_tr_b16 a[14:15], v227 offset:26112\n" \
  "v_mul_f32_e32 v205, v205, v221\n" \
  "v_exp_f32_e32 v242, v242\n" \
  "v_mfma_f32_32x32x16_bf16 v[144:159], a[84:87], a[28:31], v[144:159]\n" \
  "v_mul_f32_e32 v206, v206, v222\n" \
  "v_exp_f32_e32 v243, v243\n" \
  "v_mul_f32_e32 v207, v207, v223\n" \
  "s_waitcnt lgkmcnt(0)\n" \
  "v_mfma_f32_32x32x16_bf16 v[96:111], a[88:91], a[0:3], v[96:111]\n" \
  "ds_read_b64_tr_b16 a[16:17], v226 offset:28672\n" \
  "ds_read_b64_tr_b16 a[18:19], v226 offset:29184\n" \
  "ds_read_b64_tr_b16 a[20:21], v226 offset:29696\n" \
  "v_add_f32_e32 v240, 1.0, v240\n" \
  "v_mfma_f32_32x32x16_bf16 v[112:127], a[88:91], a[4:7], v[112:127]\n" \
  "ds_read_b64_tr_b16 a[22:23], v226 offset:30208\n" \
  "ds_read_b64_tr_b16 a[24:25], v227 offset:28672\n" \
  "ds_read_b64_tr_b16 a[26:27], v227 offset:29184\n" \
  "v_add_f32_e32 v241, 1.0, v241\n" \
  "v_mfma_f32_32x32x16_bf16 v[128:143], a[88:91], a[8:11], v[128:143]\n" \
  "ds_read_b64_tr_b16 a[28:29], v227 offset:29696\n" \
  "ds_read_b64_tr_b16 a[30:31], v227 offset:30208\n" \
  "v_rcp_f32_e32 v240, v240\n" \
  "v_add_f32_e32 v242, 1.0, v242\n" \
  "v_mfma_f32_32x32x16_bf16 v[144:159], a[88:91], a[12:15], v[144:159]\n" \
  "v_rcp_f32_e32 v241, v241\n" \
  "v_add_f32_e32 v243, 1.0, v243\n" \
  "v_rcp_f32_e32 v242, v242\n" \
  "s_waitcnt lgkmcnt(0)\n" \
  "v_mfma_f32_32x32x16_bf16 v[96:111], a[92:95], a[16:19], v[96:111]\n" \
  "v_mul_f32_e32 v204, v204, v240\n" \
  "v_rcp_f32_e32 v243, v243\n" \
  "v_mfma_f32_32x32x16_bf16 v[112:127], a[92:95], a[20:23], v[112:127]\n" \
  "v_mul_f32_e32 v205, v205, v241\n" \
  "v_mul_f32_e32 v206, v206, v242\n" \
  "v_mfma_f32_32x32x16_bf16 v[128:143], a[92:95], a[24:27], v[128:143]\n" \
  "v_mul_f32_e32 v207, v207, v243\n" \
  "v_cvt_pk_bf16_f32 v214, v204, v205\n" \
  "v_mfma_f32_32x32x16_bf16 v[144:159], a[92:95], a[28:31], v[144:159]\n" \
  "v_cvt_pk_bf16_f32 v215, v206, v207\n" \
  "global_store_dwordx4 v231, v[212:215], s[74:75] offset:3072 sc1\n" \
  "s_branch 3f\n" \
  "2:\n" \
  "s_waitcnt vmcnt(14) lgkmcnt(0)\n" \
  "s_barrier\n" \
  "v_mfma_f32_32x32x16_bf16 v[96:111], v[0:3], a[0:3], 0\n" \
  "s_add_u32 m0, s67, 16384\n" \
  "ds_read_b64_tr_b16 a[16:17], v226 offset:4096\n" \
  "ds_read_b64_tr_b16 a[18:19], v226 offset:4608\n" \
  "ds_read_b64_tr_b16 a[20:21], v226 offset:5120\n" \
  "v_mfma_f32_32x32x16_bf16 v[112:127], v[0:3], a[4:7], 0\n" \
  "ds_read_b64_tr_b16 a[22:23], v226 offset:5632\n" \
  "ds_read_b64_tr_b16 a[24:25], v227 offset:4096\n" \
  "ds_read_b64_tr_b16 a[26:27], v227 offset:4608\n" \
  "v_mfma_f32_32x32x16_bf16 v[128:143], v[0:3], a[8:11], 0\n" \
  "ds_read_b64_tr_b16 a[28:29], v227 offset:5120\n" \
  "ds_read_b64_tr_b16 a[30:31], v227 offset:5632\n" \
  "global_load_lds_dwordx4 v230, s[56:57] offset:0\n" \
  "v_mfma_f32_32x32x16_bf16 v[144:159], v[0:3], a[12:15], 0\n" \
  "s_waitcnt lgkmcnt(0)\n" \
  "v_mfma_f32_32x32x16_bf16 v[96:111], v[4:7], a[16:19], v[96:111]\n" \
  "ds_read_b64_tr_b16 a[0:1], v226 offset:8192\n" \
  "ds_read_b64_tr_b16 a[2:3], v226 offset:8704\n" \
  "ds_read_b64_tr_b16 a[4:5], v226 offset:9216\n" \
  "v_mfma_f32_32x32x16_bf16 v[112:127], v[4:7], a[20:23], v[112:127]\n" \
  "ds_read_b64_tr_b16 a[6:7], v226 offset:9728\n" \
  "ds_read_b64_tr_b16 a[8:9], v227 offset:8192\n" \
  "ds_read_b64_tr_b16 a[10:11], v227 offset:8704\n" \
  "v_mfma_f32_32x32x16_bf16 v[128:143], v[4:7], a[24:27], v[128:143]\n" \
  "ds_read_b64_tr_b16 a[12:13], v227 offset:9216\n" \
  "ds_read_b64_tr_b16 a[14:15], v227 offset:9728\n" \
  "global_load_lds_dwordx4 v230, s[58:59] offset:1024\n" \
  "v_mfma_f32_32x32x16_bf16 v[144:159], v[4:7], a[28:31], v[144:159]\n" \
  "v_lshrrev_b32_e32 v240, 9, v231\n" \
  "v_and_b32_e32 v241, 0x1f0, v231\n" \
  "v_add_u32_e32 v242, s80, v241\n" \
  "s_waitcnt lgkmcnt(0)\n" \
  "v_mfma_f32_32x32x16_bf16 v[96:111], v[8:11], a[0:3], v[96:111]\n" \
  "ds_read_b64_tr_b16 a[16:17], v226 offset:12288\n" \
  "ds_read_b64_tr_b16 a[18:19], v226 offset:12800\n" \
  "ds_read_b64_tr_b16 a[20:21], v226 offset:13312\n" \
  "global_load_lds_dwordx4 v230, s[60:61] offset:2048\n" \
  "v_mfma_f32_32x32x16_bf16 v[112:127], v[8:11], a[4:7], v[112:127]\n" \
  "ds_read_b64_tr_b16 a[22:23], v226 offset:13824\n" \
  "ds_read_b64_tr_b16 a[24:25], v227 offset:12288\n" \
  "ds_read_b64_tr_b16 a[26:27], v227 offset:12800\n" \
  "v_add_u32_e32 v241, s71, v241\n" \
  "v_mfma_f32_32x32x16_bf16 v[128:143], v[8:11], a[8:11], v[128:143]\n" \
  "ds_read_b64_tr_b16 a[28:29], v227 offset:13312\n" \
  "ds_read_b64_tr_b16 a[30:31], v227 offset:13824\n" \
  "v_lshl_add_u32 v236, v240, 3, v241\n" \
  "v_mul_u32_u24_e32 v237, 0x1f8, v240\n" \
  "v_mfma_f32_32x32x16_bf16 v[144:159], v[8:11], a[12:15], v[144:159]\n" \
  "v_add_u32_e32 v237, v237, v241\n" \
  "s_waitcnt lgkmcnt(0)\n" \
  "v_mfma_f32_32x32x16_bf16 v[96:111], v[12:15], a[16:19], v[96:111]\n" \
  "ds_read_b64_tr_b16 a[0:1], v226 offset:16384\n" \
  "ds_read_b64_tr_b16 a[2:3], v226 offset:16896\n" \
  "ds_read_b64_tr_b16 a[4:5], v226 offset:17408\n" \
  "global_load_lds_dwordx4 v230, s[62:63] offset:3072\n" \
  "v_mfma_f32_32x32x16_bf16 v[112:127], v[12:15], a[20:23], v[112:127]\n" \
  "ds_read_b64_tr_b16 a[6:7], v226 offset:17920\n" \
  "ds_read_b64_tr_b16 a[8:9], v227 offset:16384\n" \
  "ds_read_b64_tr_b16 a[10:11], v227 offset:16896\n" \
  "v_add_u32_e32 v230, s66, v230\n" \
  "v_mfma_f32_32x32x16_bf16 v[128:143], v[12:15], a[24:27], v[128:143]\n" \
  "ds_read_b64_tr_b16 a[12:13], v227 offset:17408\n" \
  "ds_read_b64_tr_b16 a[14:15], v227 offset:17920\n" \
  "v_sub_u32_e32 v243, v242, v241\n" \
  "v_subrev_u32_e32 v238, 0x208, v243\n" \
  "v_mfma_f32_32x32x16_bf16 v[144:159], v[12:15], a[28:31], v[144:159]\n" \
  "v_sub_u32_e32 v239, 0x14000, v243\n" \
  "s_waitcnt vmcnt(13) lgkmcnt(0)\n" \
  "s_barrier\n" \
  "v_mfma_f32_32x32x16_bf16 v[96:111], v[16:19], a[0:3], v[96:111]\n" \
  "s_add_u32 m0, s67, 32768\n" \
  "ds_read_b64_tr_b16 a[16:17], v226 offset:20480\n" \
  "ds_read_b64_tr_b16 a[18:19], v226 offset:20992\n" \
  "ds_read_b64_tr_b16 a[20:21], v226 offset:21504\n" \
  "v_mfma_f32_32x32x16_bf16 v[112:127], v[16:19], a[4:7], v[112:127]\n" \
  "ds_read_b64_tr_b16 a[22:23], v226 offset:22016\n" \
  "ds_read_b64_tr_b16 a[24:25], v227 offset:20480\n" \
  "ds_read_b64_tr_b16 a[26:27], v227 offset:20992\n" \
  "global_load_lds_dwordx4 v230, s[56:57] offset:0\n" \
  "v_mfma_f32_32x32x16_bf16 v[128:143], v[16:19], a[8:11], v[128:143]\n" \
  "ds_read_b64_tr_b16 a[28:29], v227 offset:21504\n" \
  "ds_read_b64_tr_b16 a[30:31], v227 offset:22016\n" \
  "v_mad_i32_i24 v238, v238, v240, v241\n" \
  "v_mad_i32_i24 v239, v239, v240, v242\n" \
  "v_mfma_f32_32x32x16_bf16 v[144:159], v[16:19], a[12:15], v[144:159]\n" \
  "v_mul_f32_e32 v160, s79, v160\n" \
  "v_mul_f32_e32 v161, s79, v161\n" \
  "s_waitcnt lgkmcnt(0)\n" \
  "v_mfma_f32_32x32x16_bf16 v[96:111], v[20:23], a[16:19], v[96:111]\n" \
  "ds_read_b64_tr_b16 a[0:1], v226 offset:24576\n" \
  "ds_read_b64_tr_b16 a[2:3], v226 offset:25088\n" \
  "ds_read_b64_tr_b16 a[4:5], v226 offset:25600\n" \
  "v_mfma_f32_32x32x16_bf16 v[112:127], v[20:23], a[20:23], v[112:127]\n" \
  "ds_read_b64_tr_b16 a[6:7], v226 offset:26112\n" \
  "ds_read_b64_tr_b16 a[8:9], v227 offset:24576\n" \
  "ds_read_b64_tr_b16 a[10:11], v227 offset:25088\n" \
  "v_mfma_f32_32x32x16_bf16 v[128:143], v[20:23], a[24:27], v[128:143]\n" \
  "ds_read_b64_tr_b16 a[12:13], v227 offset:25600\n" \
  "ds_read_b64_tr_b16 a[14:15], v227 offset:26112\n" \
  "global_load_lds_dwordx4 v230, s[58:59] offset:1024\n" \
  "v_mfma_f32_32x32x16_bf16 v[144:159], v[20:23], a[28:31], v[144:159]\n" \
  "v_mul_f32_e32 v162, s79, v162\n" \
  "v_mul_f32_e32 v163, s79, v163\n" \
  "v_cvt_pk_bf16_f32 v160, v160, v161\n" \
  "s_waitcnt lgkmcnt(0)\n" \
  "v_mfma_f32_32x32x16_bf16 v[96:111], v[24:27], a[0:3], v[96:111]\n" \
  "ds_read_b64_tr_b16 a[16:17], v226 offset:28672\n" \
  "ds_read_b64_tr_b16 a[18:19], v226 offset:29184\n" \
  "ds_read_b64_tr_b16 a[20:21], v226 offset:29696\n" \
  "global_load_lds_dwordx4 v230, s[60:61] offset:2048\n" \
  "v_mfma_f32_32x32x16_bf16 v[112:127], v[24:27], a[4:7], v[112:127]\n" \
  "ds_read_b64_tr_b16 a[22:23], v226 offset:30208\n" \
  "ds_read_b64_tr_b16 a[24:25], v227 offset:28672\n" \
  "ds_read_b64_tr_b16 a[26:27], v227 offset:29184\n" \
  "v_cvt_pk_bf16_f32 v161, v162, v163\n" \
  "v_mfma_f32_32x32x16_bf16 v[128:143], v[24:27], a[8:11], v[128:143]\n" \
  "ds_read_b64_tr_b16 a[28:29], v227 offset:29696\n" \
  "ds_read_b64_tr_b16 a[30:31], v227 offset:30208\n" \
  "v_add_u32_e32 v162, 0x0, v236\n" \
  "global_store_dwordx2 v162, v[160:161], s[74:75] offset:0 sc1\n" \
  "v_mfma_f32_32x32x16_bf16 v[144:159], v[24:27], a[12:15], v[144:159]\n" \
  "v_mul_f32_e32 v164, s79, v164\n" \
  "s_waitcnt lgkmcnt(0)\n" \
  "v_mfma_f32_32x32x16_bf16 v[96:111], v[28:31], a[16:19], v[96:111]\n" \
  "ds_read_b64_tr_b16 a[0:1], v226 offset:32768\n" \
  "ds_read_b64_tr_b16 a[2:3], v226 offset:33280\n" \
  "ds_read_b64_tr_b16 a[4:5], v226 offset:33792\n" \
  "global_load_lds_dwordx4 v230, s[62:63] offset:3072\n" \
  "v_mfma_f32_32x32x16_bf16 v[112:127], v[28:31], a[20:23], v[112:127]\n" \
  "ds_read_b64_tr_b16 a[6:7], v226 offset:34304\n" \
  "ds_read_b64_tr_b16 a[8:9], v227 offset:32768\n" \
  "ds_read_b64_tr_b16 a[10:11], v227 offset:33280\n" \
  "v_add_u32_e32 v230, s66, v230\n" \
  "v_mfma_f32_32x32x16_bf16 v[128:143], v[28:31], a[24:27], v[128:143]\n" \
  "ds_read_b64_tr_b16 a[12:13], v227 offset:33792\n" \
  "ds_read_b64_tr_b16 a[14:15], v227 offset:34304\n" \
  "v_mul_f32_e32 v165, s79, v165\n" \
  "v_mul_f32_e32 v166, s79, v166\n" \
  "v_mfma_f32_32x32x16_bf16 v[144:159], v[28:31], a[28:31], v[144:159]\n" \
  "v_mul_f32_e32 v167, s79, v167\n" \
  "v_cvt_pk_bf16_f32 v164, v164, v165\n" \
  "s_waitcnt vmcnt(14) lgkmcnt(0)\n" \
  "s_barrier\n" \
  "v_mfma_f32_32x32x16_bf16 v[96:111], v[32:35], a[0:3], v[96:111]\n" \
  "s_add_u32 m0, s67, 49152\n" \
  "ds_read_b64_tr_b16 a[16:17], v226 offset:36864\n" \
  "ds_read_b64_tr_b16 a[18:19], v226 offset:37376\n" \
  "ds_read_b64_tr_b16 a[20:21], v226 offset:37888\n" \
  "v_mfma_f32_32x32x16_bf16 v[112:127], v[32:35], a[4:7], v[112:127]\n" \
  "ds_read_b64_tr_b16 a[22:23], v226 offset:38400\n" \
  "ds_read_b64_tr_b16 a[24:25], v227 offset:36864\n" \
  "ds_read_b64_tr_b16 a[26:27], v227 offset:37376\n" \
  "global_load_lds_dwordx4 v230, s[56:57] offset:0\n" \
  "v_mfma_f32_32x32x16_bf16 v[128:143], v[32:35], a[8:11], v[128:143]\n" \
  "ds_read_b64_tr_b16 a[28:29], v227 offset:37888\n" \
  "ds_read_b64_tr_b16 a[30:31], v227 offset:38400\n" \
  "v_cvt_pk_bf16_f32 v165, v166, v167\n" \
  "v_add_u32_e32 v166, 0x200, v236\n" \
  "v_mfma_f32_32x32x16_bf16 v[144:159], v[32:35], a[12:15], v[144:159]\n" \
  "global_store_dwordx2 v166, v[164:165], s[74:75] offset:0 sc1\n" \
  "s_waitcnt lgkmcnt(0)\n" \
  "v_mfma_f32_32x32x16_bf16 v[96:111], v[36:39], a[16:19], v[96:111]\n" \
  "ds_read_b64_tr_b16 a[0:1], v226 offset:40960\n" \
  "ds_read_b64_tr_b16 a[2:3], v226 offset:41472\n" \
  "ds_read_b64_tr_b16 a[4:5], v226 offset:41984\n" \
  "global_load_lds_dwordx4 v230, s[58:59] offset:1024\n" \
  "v_mfma_f32_32x32x16_bf16 v[112:127], v[36:39], a[20:23], v[112:127]\n" \
  "ds_read_b64_tr_b16 a[6:7], v226 offset:42496\n" \
  "ds_read_b64_tr_b16 a[8:9], v227 offset:40960\n" \
  "ds_read_b64_tr_b16 a[10:11], v227 offset:41472\n" \
  "v_mul_f32_e32 v168, s79, v168\n" \
  "v_mfma_f32_32x32x16_bf16 v[128:143], v[36:39], a[24:27], v[128:143]\n" \
  "ds_read_b64_tr_b16 a[12:13], v227 offset:41984\n" \
  "ds_read_b64_tr_b16 a[14:15], v227 offset:42496\n" \
  "v_mul_f32_e32 v169, s79, v169\n" \
  "v_mul_f32_e32 v170, s79, v170\n" \
  "v_mfma_f32_32x32x16_bf16 v[144:159], v[36:39], a[28:31], v[144:159]\n" \
  "v_mul_f32_e32 v171, s79, v171\n" \
  "s_waitcnt lgkmcnt(0)\n" \
  "v_mfma_f32_32x32x16_bf16 v[96:111], v[40:43], a[0:3], v[96:111]\n" \
  "ds_read_b64_tr_b16 a[16:17], v226 offset:45056\n" \
  "ds_read_b64_tr_b16 a[18:19], v226 offset:45568\n" \
  "ds_read_b64_tr_b16 a[20:21], v226 offset:46080\n" \
  "v_mfma_f32_32x32x16_bf16 v[112:127], v[40:43], a[4:7], v[112:127]\n" \
  "ds_read_b64_tr_b16 a[22:23], v226 offset:46592\n" \
  "ds_read_b64_tr_b16 a[24:25], v227 offset:45056\n" \
  "ds_read_b64_tr_b16 a[26:27], v227 offset:45568\n" \
  "v_mfma_f32_32x32x16_bf16 v[128:143], v[40:43], a[8:11], v[128:143]\n" \
  "ds_read_b64_tr_b16 a[28:29], v227 offset:46080\n" \
  "ds_read_b64_tr_b16 a[30:31], v227 offset:46592\n" \
  "global_load_lds_dwordx4 v230, s[60:61] offset:2048\n" \
  "v_mfma_f32_32x32x16_bf16 v[144:159], v[40:43], a[12:15], v[144:159]\n" \
  "v_cvt_pk_bf16_f32 v168, v168, v169\n" \
  "v_cvt_pk_bf16_f32 v169, v170, v171\n" \
  "v_add_u32_e32 v170, 0x0, v239\n" \
  "s_waitcnt lgkmcnt(0)\n" \
  "v_mfma_f32_32x32x16_bf16 v[96:111], v[44:47], a[16:19], v[96:111]\n" \
  "ds_read_b64_tr_b16 a[0:1], v226 offset:49152\n" \
  "ds_read_b64_tr_b16 a[2:3], v226 offset:49664\n" \
  "ds_read_b64_tr_b16 a[4:5], v226 offset:50176\n" \
  "global_load_lds_dwordx4 v230, s[62:63] offset:3072\n" \
  "v_mfma_f32_32x32x16_bf16 v[112:127], v[44:47], a[20:23], v[112:127]\n" \
  "ds_read_b64_tr_b16 a[6:7], v226 offset:50688\n" \
  "ds_read_b64_tr_b16 a[8:9], v227 offset:49152\n" \
  "ds_read_b64_tr_b16 a[10:11], v227 offset:49664\n" \
  "v_add_u32_e32 v230, s66, v230\n" \
  "v_mfma_f32_32x32x16_bf16 v[128:143], v[44:47], a[24:27], v[128:143]\n" \
  "ds_read_b64_tr_b16 a[12:13], v227 offset:50176\n" \
  "ds_read_b64_tr_b16 a[14:15], v227 offset:50688\n" \
  "global_store_dwordx2 v170, v[168:169], s[74:75] offset:0 sc1\n" \
  "v_mul_f32_e32 v172, s79, v172\n" \
  "v_mfma_f32_32x32x16_bf16 v[144:159], v[44:47], a[28:31], v[144:159]\n" \
  "v_mul_f32_e32 v173, s79, v173\n" \
  "v_mul_f32_e32 v174, s79, v174\n" \
  "s_waitcnt vmcnt(16) lgkmcnt(0)\n" \
  "s_barrier\n" \
  "v_mfma_f32_32x32x16_bf16 v[96:111], v[48:51], a[0:3], v[96:111]\n" \
  "s_add_u32 m0, s67, 65536\n" \
  "ds_read_b64_tr_b16 a[16:17], v226 offset:53248\n" \
  "ds_read_b64_tr_b16 a[18:19], v226 offset:53760\n" \
  "ds_read_b64_tr_b16 a[20:21], v226 offset:54272\n" \
  "v_mfma_f32_32x32x16_bf16 v[112:127], v[48:51], a[4:7], v[112:127]\n" \
  "ds_read_b64_tr_b16 a[22:23], v226 offset:54784\n" \
  "ds_read_b64_tr_b16 a[24:25], v227 offset:53248\n" \
  "ds_read_b64_tr_b16 a[26:27], v227 offset:53760\n" \
  "global_load_lds_dwordx4 v230, s[56:57] offset:0\n" \
  "v_mfma_f32_32x32x16_bf16 v[128:143], v[48:51], a[8:11], v[128:143]\n" \
  "ds_read_b64_tr_b16 a[28:29], v227 offset:54272\n" \
  "ds_read_b64_tr_b16 a[30:31], v227 offset:54784\n" \
  "v_mul_f32_e32 v175, s79, v175\n" \
  "v_cvt_pk_bf16_f32 v172, v172, v173\n" \
  "v_mfma_f32_32x32x16_bf16 v[144:159], v[48:51], a[12:15], v[144:159]\n" \
  "v_cvt_pk_bf16_f32 v173, v174, v175\n" \
  "v_add_u32_e32 v174, 0x14008, v237\n" \
  "s_waitcnt lgkmcnt(0)\n" \
  "v_mfma_f32_32x32x16_bf16 v[96:111], v[52:55], a[16:19], v[96:111]\n" \
  "ds_read_b64_tr_b16 a[0:1], v226 offset:57344\n" \
  "ds_read_b64_tr_b16 a[2:3], v226 offset:57856\n" \
  "ds_read_b64_tr_b16 a[4:5], v226 offset:58368\n" \
  "v_mfma_f32_32x32x16_bf16 v[112:127], v[52:55], a[20:23], v[112:127]\n" \
  "ds_read_b64_tr_b16 a[6:7], v226 offset:58880\n" \
  "ds_read_b64_tr_b16 a[8:9], v227 offset:57344\n" \
  "ds_read_b64_tr_b16 a[10:11], v227 offset:57856\n" \
  "v_mfma_f32_32x32x16_bf16 v[128:143], v[52:55], a[24:27], v[128:143]\n" \
  "ds_read_b64_tr_b16 a[12:13], v227 offset:58368\n" \
  "ds_read_b64_tr_b16 a[14:15], v227 offset:58880\n" \
  "global_load_lds_dwordx4 v230, s[58:59] offset:1024\n" \
  "v_mfma_f32_32x32x16_bf16 v[144:159], v[52:55], a[28:31], v[144:159]\n" \
  "global_store_dwordx2 v174, v[172:173], s[74:75] offset:0 sc1\n" \
  "v_mul_f32_e32 v176, s79, v176\n" \
  "v_mul_f32_e32 v177, s79, v177\n" \
  "s_waitcnt lgkmcnt(0)\n" \
  "v_mfma_f32_32x32x16_bf16 v[96:111], v[56:59], a[0:3], v[96:111]\n" \
  "ds_read_b64_tr_b16 a[16:17], v226 offset:61440\n" \
  "ds_read_b64_tr_b16 a[18:19], v226 offset:61952\n" \
  "ds_read_b64_tr_b16 a[20:21], v226 offset:62464\n" \
  "global_load_lds_dwordx4 v230, s[60:61] offset:2048\n" \
  "v_mfma_f32_32x32x16_bf16 v[112:127], v[56:59], a[4:7], v[112:127]\n" \
  "ds_read_b64_tr_b16 a[22:23], v226 offset:62976\n" \
  "ds_read_b64_tr_b16 a[24:25], v227 offset:61440\n" \
  "ds_read_b64_tr_b16 a[26:27], v227 offset:61952\n" \
  "v_mul_f32_e32 v178, s79, v178\n" \
  "v_mfma_f32_32x32x16_bf16 v[128:143], v[56:59], a[8:11], v[128:143]\n" \
  "ds_read_b64_tr_b16 a[28:29], v227 offset:62464\n" \
  "ds_read_b64_tr_b16 a[30:31], v227 offset:62976\n" \
  "v_mul_f32_e32 v179, s79, v179\n" \
  "v_cvt_pk_bf16_f32 v176, v176, v177\n" \
  "v_mfma_f32_32x32x16_bf16 v[144:159], v[56:59], a[12:15], v[144:159]\n" \
  "v_cvt_pk_bf16_f32 v177, v178, v179\n" \
  "s_waitcnt lgkmcnt(0)\n" \
  "v_mfma_f32_32x32x16_bf16 v[96:111], v[60:63], a[16:19], v[96:111]\n" \
  "ds_read_b64_tr_b16 a[0:1], v224 offset:0\n" \
  "ds_read_b64_tr_b16 a[2:3], v224 offset:512\n" \
  "ds_read_b64_tr_b16 a[4:5], v224 offset:1024\n" \
  "global_load_lds_dwordx4 v230, s[62:63] offset:3072\n" \
  "v_mfma_f32_32x32x16_bf16 v[112:127], v[60:63], a[20:23], v[112:127]\n" \
  "ds_read_b64_tr_b16 a[6:7], v224 offset:1536\n" \
  "ds_read_b64_tr_b16 a[8:9], v225 offset:0\n" \
  "ds_read_b64_tr_b16 a[10:11], v225 offset:512\n" \
  "v_add_u32_e32 v230, s66, v230\n" \
  "v_mfma_f32_32x32x16_bf16 v[128:143], v[60:63], a[24:27], v[128:143]\n" \
  "ds_read_b64_tr_b16 a[12:13], v225 offset:1024\n" \
  "ds_read_b64_tr_b16 a[14:15], v225 offset:1536\n" \
  "v_add_u32_e32 v178, 0x3c008, v237\n" \
  "global_store_dwordx2 v178, v[176:177], s[74:75] offset:0 sc1\n" \
  "v_mfma_f32_32x32x16_bf16 v[144:159], v[60:63], a[28:31], v[144:159]\n" \
  "v_mul_f32_e32 v180, s79, v180\n" \
  "s_waitcnt vmcnt(17) lgkmcnt(0)\n" \
  "s_barrier\n" \
  "v_mfma_f32_32x32x16_bf16 v[96:111], v[64:67], a[0:3], v[96:111]\n" \
  "s_add_u32 m0, s67, 81920\n" \
  "ds_read_b64_tr_b16 a[16:17], v224 offset:4096\n" \
  "ds_read_b64_tr_b16 a[18:19], v224 offset:4608\n" \
  "ds_read_b64_tr_b16 a[20:21], v224 offset:5120\n" \
  "v_mfma_f32_32x32x16_bf16 v[112:127], v[64:67], a[4:7], v[112:127]\n" \
  "ds_read_b64_tr_b16 a[22:23], v224 offset:5632\n" \
  "ds_read_b64_tr_b16 a[24:25], v225 offset:4096\n" \
  "ds_read_b64_tr_b16 a[26:27], v225 offset:4608\n" \
  "global_load_lds_dwordx4 v230, s[56:57] offset:0\n" \
  "v_mfma_f32_32x32x16_bf16 v[128:143], v[64:67], a[8:11], v[128:143]\n" \
  "ds_read_b64_tr_b16 a[28:29], v225 offset:5120\n" \
  "ds_read_b64_tr_b16 a[30:31], v225 offset:5632\n" \
  "v_mul_f32_e32 v181, s79, v181\n" \
  "v_mul_f32_e32 v182, s79, v182\n" \
  "v_mfma_f32_32x32x16_bf16 v[144:159], v[64:67], a[12:15], v[144:159]\n" \
  "v_mul_f32_e32 v183, s79, v183\n" \
  "v_cvt_pk_bf16_f32 v180, v180, v181\n" \
  "s_waitcnt lgkmcnt(0)\n" \
  "v_mfma_f32_32x32x16_bf16 v[96:111], v[68:71], a[16:19], v[96:111]\n" \
  "ds_read_b64_tr_b16 a[0:1], v224 offset:8192\n" \
  "ds_read_b64_tr_b16 a[2:3], v224 offset:8704\n" \
  "ds_read_b64_tr_b16 a[4:5], v224 offset:9216\n" \
  "global_load_lds_dwordx4 v230, s[58:59] offset:1024\n" \
  "v_mfma_f32_32x32x16_bf16 v[112:127], v[68:71], a[20:23], v[112:127]\n" \
  "ds_read_b64_tr_b16 a[6:7], v224 offset:9728\n" \
  "ds_read_b64_tr_b16 a[8:9], v225 offset:8192\n" \
  "ds_read_b64_tr_b16 a[10:11], v225 offset:8704\n" \
  "v_cvt_pk_bf16_f32 v181, v182, v183\n" \
  "v_mfma_f32_32x32x16_bf16 v[128:143], v[68:71], a[24:27], v[128:143]\n" \
  "ds_read_b64_tr_b16 a[12:13], v225 offset:9216\n" \
  "ds_read_b64_tr_b16 a[14:15], v225 offset:9728\n" \
  "v_add_u32_e32 v182, 0x3c208, v238\n" \
  "global_store_dwordx2 v182, v[180:181], s[74:75] offset:0 sc1\n" \
  "v_mfma_f32_32x32x16_bf16 v[144:159], v[68:71], a[28:31], v[144:159]\n" \
  "v_mul_f32_e32 v184, s79, v184\n" \
  "s_waitcnt lgkmcnt(0)\n" \
  "v_mfma_f32_32x32x16_bf16 v[96:111], v[72:75], a[0:3], v[96:111]\n" \
  "ds_read_b64_tr_b16 a[16:17], v224 offset:12288\n" \
  "ds_read_b64_tr_b16 a[18:19], v224 offset:12800\n" \
  "ds_read_b64_tr_b16 a[20:21], v224 offset:13312\n" \
  "v_mfma_f32_32x32x16_bf16 v[112:127], v[72:75], a[4:7], v[112:127]\n" \
  "ds_read_b64_tr_b16 a[22:23], v224 offset:13824\n" \
  "ds_read_b64_tr_b16 a[24:25], v225 offset:12288\n" \
  "ds_read_b64_tr_b16 a[26:27], v225 offset:12800\n" \
  "v_mfma_f32_32x32x16_bf16 v[128:143], v[72:75], a[8:11], v[128:143]\n" \
  "ds_read_b64_tr_b16 a[28:29], v225 offset:13312\n" \
  "ds_read_b64_tr_b16 a[30:31], v225 offset:13824\n" \
  "global_load_lds_dwordx4 v230, s[60:61] offset:2048\n" \
  "v_mfma_f32_32x32x16_bf16 v[144:159], v[72:75], a[12:15], v[144:159]\n" \
  "v_mul_f32_e32 v185, s79, v185\n" \
  "v_mul_f32_e32 v186, s79, v186\n" \
  "v_mul_f32_e32 v187, s79, v187\n" \
  "s_waitcnt lgkmcnt(0)\n" \
  "v_mfma_f32_32x32x16_bf16 v[96:111], v[76:79], a[16:19], v[96:111]\n" \
  "ds_read_b64_tr_b16 a[0:1], v224 offset:16384\n" \
  "ds_read_b64_tr_b16 a[2:3], v224 offset:16896\n" \
  "ds_read_b64_tr_b16 a[4:5], v224 offset:17408\n" \
  "global_load_lds_dwordx4 v230, s[62:63] offset:3072\n" \
  "v_mfma_f32_32x32x16_bf16 v[112:127], v[76:79], a[20:23], v[112:127]\n" \
  "ds_read_b64_tr_b16 a[6:7], v224 offset:17920\n" \
  "ds_read_b64_tr_b16 a[8:9], v225 offset:16384\n" \
  "ds_read_b64_tr_b16 a[10:11], v225 offset:16896\n" \
  "v_add_u32_e32 v230, s66, v230\n" \
  "v_mfma_f32_32x32x16_bf16 v[128:143], v[76:79], a[24:27], v[128:143]\n" \
  "ds_read_b64_tr_b16 a[12:13], v225 offset:17408\n" \
  "ds_read_b64_tr_b16 a[14:15], v225 offset:17920\n" \
  "v_cvt_pk_bf16_f32 v184, v184, v185\n" \
  "v_cvt_pk_bf16_f32 v185, v186, v187\n" \
  "v_mfma_f32_32x32x16_bf16 v[144:159], v[76:79], a[28:31], v[144:159]\n" \
  "v_add_u32_e32 v186, 0x50000, v236\n" \
  "global_store_dwordx2 v186, v[184:185], s[74:75] offset:0 sc1\n" \
  "s_waitcnt vmcnt(18) lgkmcnt(0)\n" \
  "s_barrier\n" \
  "v_mfma_f32_32x32x16_bf16 v[96:111], v[80:83], a[0:3], v[96:111]\n" \
  "ds_read_b64_tr_b16 a[16:17], v224 offset:20480\n" \
  "ds_read_b64_tr_b16 a[18:19], v224 offset:20992\n" \
  "ds_read_b64_tr_b16 a[20:21], v224 offset:21504\n" \
  "v_mfma_f32_32x32x16_bf16 v[112:127], v[80:83], a[4:7], v[112:127]\n" \
  "ds_read_b64_tr_b16 a[22:23], v224 offset:22016\n" \
  "ds_read_b64_tr_b16 a[24:25], v225 offset:20480\n" \
  "ds_read_b64_tr_b16 a[26:27], v225 offset:20992\n" \
  "v_mfma_f32_32x32x16_bf16 v[128:143], v[80:83], a[8:11], v[128:143]\n" \
  "ds_read_b64_tr_b16 a[28:29], v225 offset:21504\n" \
  "ds_read_b64_tr_b16 a[30:31], v225 offset:22016\n" \
  "v_mul_f32_e32 v188, s79, v188\n" \
  "v_mfma_f32_32x32x16_bf16 v[144:159], v[80:83], a[12:15], v[144:159]\n" \
  "v_mul_f32_e32 v189, s79, v189\n" \
  "v_mul_f32_e32 v190, s79, v190\n" \
  "s_waitcnt lgkmcnt(0)\n" \
  "v_mfma_f32_32x32x16_bf16 v[96:111], v[84:87], a[16:19], v[96:111]\n" \
  "ds_read_b64_tr_b16 a[0:1], v224 offset:24576\n" \
  "ds_read_b64_tr_b16 a[2:3], v224 offset:25088\n" \
  "ds_read_b64_tr_b16 a[4:5], v224 offset:25600\n" \
  "v_mfma_f32_32x32x16_bf16 v[112:127], v[84:87], a[20:23], v[112:127]\n" \
  "ds_read_b64_tr_b16 a[6:7], v224 offset:26112\n" \
  "ds_read_b64_tr_b16 a[8:9], v225 offset:24576\n" \
  "ds_read_b64_tr_b16 a[10:11], v225 offset:25088\n" \
  "v_mfma_f32_32x32x16_bf16 v[128:143], v[84:87], a[24:27], v[128:143]\n" \
  "ds_read_b64_tr_b16 a[12:13], v225 offset:25600\n" \
  "ds_read_b64_tr_b16 a[14:15], v225 offset:26112\n" \
  "v_mul_f32_e32 v191, s79, v191\n" \
  "v_mfma_f32_32x32x16_bf16 v[144:159], v[84:87], a[28:31], v[144:159]\n" \
  "v_cvt_pk_bf16_f32 v188, v188, v189\n" \
  "v_cvt_pk_bf16_f32 v189, v190, v191\n" \
  "v_add_u32_e32 v190, 0x50200, v236\n" \
  "s_waitcnt lgkmcnt(0)\n" \
  "v_mfma_f32_32x32x16_bf16 v[96:111], v[88:91], a[0:3], v[96:111]\n" \
  "ds_read_b64_tr_b16 a[16:17], v224 offset:28672\n" \
  "ds_read_b64_tr_b16 a[18:19], v224 offset:29184\n" \
  "ds_read_b64_tr_b16 a[20:21], v224 offset:29696\n" \
  "v_mfma_f32_32x32x16_bf16 v[112:127], v[88:91], a[4:7], v[112:127]\n" \
  "ds_read_b64_tr_b16 a[22:23], v224 offset:30208\n" \
  "ds_read_b64_tr_b16 a[24:25], v225 offset:28672\n" \
  "ds_read_b64_tr_b16 a[26:27], v225 offset:29184\n" \
  "v_mfma_f32_32x32x16_bf16 v[128:143], v[88:91], a[8:11], v[128:143]\n" \
  "ds_read_b64_tr_b16 a[28:29], v225 offset:29696\n" \
  "ds_read_b64_tr_b16 a[30:31], v225 offset:30208\n" \
  "global_store_dwordx2 v190, v[188:189], s[74:75] offset:0 sc1\n" \
  "v_mfma_f32_32x32x16_bf16 v[144:159], v[88:91], a[12:15], v[144:159]\n" \
  "v_mul_f32_e32 v192, s79, v192\n" \
  "v_mul_f32_e32 v193, s79, v193\n" \
  "s_waitcnt lgkmcnt(0)\n" \
  "v_mfma_f32_32x32x16_bf16 v[96:111], v[92:95], a[16:19], v[96:111]\n" \
  "ds_read_b64_tr_b16 a[0:1], v224 offset:32768\n" \
  "ds_read_b64_tr_b16 a[2:3], v224 offset:33280\n" \
  "ds_read_b64_tr_b16 a[4:5], v224 offset:33792\n" \
  "v_mfma_f32_32x32x16_bf16 v[112:127], v[92:95], a[20:23], v[112:127]\n" \
  "ds_read_b64_tr_b16 a[6:7], v224 offset:34304\n" \
  "ds_read_b64_tr_b16 a[8:9], v225 offset:32768\n" \
  "ds_read_b64_tr_b16 a[10:11], v225 offset:33280\n" \
  "v_mfma_f32_32x32x16_bf16 v[128:143], v[92:95], a[24:27], v[128:143]\n" \
  "ds_read_b64_tr_b16 a[12:13], v225 offset:33792\n" \
  "ds_read_b64_tr_b16 a[14:15], v225 offset:34304\n" \
  "v_mul_f32_e32 v194, s79, v194\n" \
  "v_mfma_f32_32x32x16_bf16 v[144:159], v[92:95], a[28:31], v[144:159]\n" \
  "v_mul_f32_e32 v195, s79, v195\n" \
  "v_cvt_pk_bf16_f32 v192, v192, v193\n" \
  "v_cvt_pk_bf16_f32 v193, v194, v195\n" \
  "s_waitcnt vmcnt(14) lgkmcnt(0)\n" \
  "s_barrier\n" \
  "v_mfma_f32_32x32x16_bf16 v[96:111], a[32:35], a[0:3], v[96:111]\n" \
  "ds_read_b64_tr_b16 a[16:17], v224 offset:36864\n" \
  "ds_read_b64_tr_b16 a[18:19], v224 offset:37376\n" \
  "ds_read_b64_tr_b16 a[20:21], v224 offset:37888\n" \
  "v_mfma_f32_32x32x16_bf16 v[112:127], a[32:35], a[4:7], v[112:127]\n" \
  "ds_read_b64_tr_b16 a[22:23], v224 offset:38400\n" \
  "ds_read_b64_tr_b16 a[24:25], v225 offset:36864\n" \
  "ds_read_b64_tr_b16 a[26:27], v225 offset:37376\n" \
  "v_mfma_f32_32x32x16_bf16 v[128:143], a[32:35], a[8:11], v[128:143]\n" \
  "ds_read_b64_tr_b16 a[28:29], v225 offset:37888\n" \
  "ds_read_b64_tr_b16 a[30:31], v225 offset:38400\n" \
  "v_add_u32_e32 v194, 0x14208, v238\n" \
  "v_mfma_f32_32x32x16_bf16 v[144:159], a[32:35], a[12:15], v[144:159]\n" \
  "global_store_dwordx2 v194, v[192:193], s[74:75] offset:0 sc1\n" \
  "v_mul_f32_e32 v196, s79, v196\n" \
  "v_mul_f32_e32 v197, s79, v197\n" \
  "s_waitcnt lgkmcnt(0)\n" \
  "v_mfma_f32_32x32x16_bf16 v[96:111], a[36:39], a[16:19], v[96:111]\n" \
  "ds_read_b64_tr_b16 a[0:1], v224 offset:40960\n" \
  "ds_read_b64_tr_b16 a[2:3], v224 offset:41472\n" \
  "ds_read_b64_tr_b16 a[4:5], v224 offset:41984\n" \
  "v_mfma_f32_32x32x16_bf16 v[112:127], a[36:39], a[20:23], v[112:127]\n" \
  "ds_read_b64_tr_b16 a[6:7], v224 offset:42496\n" \
  "ds_read_b64_tr_b16 a[8:9], v225 offset:40960\n" \
  "ds_read_b64_tr_b16 a[10:11], v225 offset:41472\n" \
  "v_mfma_f32_32x32x16_bf16 v[128:143], a[36:39], a[24:27], v[128:143]\n" \
  "ds_read_b64_tr_b16 a[12:13], v225 offset:41984\n" \
  "ds_read_b64_tr_b16 a[14:15], v225 offset:42496\n" \
  "v_mul_f32_e32 v198, s79, v198\n" \
  "v_mfma_f32_32x32x16_bf16 v[144:159], a[36:39], a[28:31], v[144:159]\n" \
  "v_mul_f32_e32 v199, s79, v199\n" \
  "v_cvt_pk_bf16_f32 v196, v196, v197\n" \
  "s_waitcnt lgkmcnt(0)\n" \
  "v_mfma_f32_32x32x16_bf16 v[96:111], a[40:43], a[0:3], v[96:111]\n" \
  "ds_read_b64_tr_b16 a[16:17], v224 offset:45056\n" \
  "ds_read_b64_tr_b16 a[18:19], v224 offset:45568\n" \
  "ds_read_b64_tr_b16 a[20:21], v224 offset:46080\n" \
  "v_mfma_f32_32x32x16_bf16 v[112:127], a[40:43], a[4:7], v[112:127]\n" \
  "ds_read_b64_tr_b16 a[22:23], v224 offset:46592\n" \
  "ds_read_b64_tr_b16 a[24:25], v225 offset:45056\n" \
  "ds_read_b64_tr_b16 a[26:27], v225 offset:45568\n" \
  "v_mfma_f32_32x32x16_bf16 v[128:143], a[40:43], a[8:11], v[128:143]\n" \
  "ds_read_b64_tr_b16 a[28:29], v225 offset:46080\n" \
  "ds_read_b64_tr_b16 a[30:31], v225 offset:46592\n" \
  "v_cvt_pk_bf16_f32 v197, v198, v199\n" \
  "v_mfma_f32_32x32x16_bf16 v[144:159], a[40:43], a[12:15], v[144:159]\n" \
  "v_add_u32_e32 v198, 0x28000, v236\n" \
  "global_store_dwordx2 v198, v[196:197], s[74:75] offset:0 sc1\n" \
  "v_mul_f32_e32 v200, s79, v200\n" \
  "s_waitcnt lgkmcnt(0)\n" \
  "v_mfma_f32_32x32x16_bf16 v[96:111], a[44:47], a[16:19], v[96:111]\n" \
  "ds_read_b64_tr_b16 a[0:1], v224 offset:49152\n" \
  "ds_read_b64_tr_b16 a[2:3], v224 offset:49664\n" \
  "ds_read_b64_tr_b16 a[4:5], v224 offset:50176\n" \
  "v_mfma_f32_32x32x16_bf16 v[112:127], a[44:47], a[20:23], v[112:127]\n" \
  "ds_read_b64_tr_b16 a[6:7], v224 offset:50688\n" \
  "ds_read_b64_tr_b16 a[8:9], v225 offset:49152\n" \
  "ds_read_b64_tr_b16 a[10:11], v225 offset:49664\n" \
  "v_mfma_f32_32x32x16_bf16 v[128:143], a[44:47], a[24:27], v[128:143]\n" \
  "ds_read_b64_tr_b16 a[12:13], v225 offset:50176\n" \
  "ds_read_b64_tr_b16 a[14:15], v225 offset:50688\n" \
  "v_mul_f32_e32 v201, s79, v201\n" \
  "v_mfma_f32_32x32x16_bf16 v[144:159], a[44:47], a[28:31], v[144:159]\n" \
  "v_mul_f32_e32 v202, s79, v202\n" \
  "v_mul_f32_e32 v203, s79, v203\n" \
  "s_waitcnt vmcnt(10) lgkmcnt(0)\n" \
  "s_barrier\n" \
  "v_mfma_f32_32x32x16_bf16 v[96:111], a[48:51], a[0:3], v[96:111]\n" \
  "ds_read_b64_tr_b16 a[16:17], v224 offset:53248\n" \
  "ds_read_b64_tr_b16 a[18:19], v224 offset:53760\n" \
  "ds_read_b64_tr_b16 a[20:21], v224 offset:54272\n" \
  "v_mfma_f32_32x32x16_bf16 v[112:127], a[48:51], a[4:7], v[112:127]\n" \
  "ds_read_b64_tr_b16 a[22:23], v224 offset:54784\n" \
  "ds_read_b64_tr_b16 a[24:25], v225 offset:53248\n" \
  "ds_read_b64_tr_b16 a[26:27], v225 offset:53760\n" \
  "v_mfma_f32_32x32x16_bf16 v[128:143], a[48:51], a[8:11], v[128:143]\n" \
  "ds_read_b64_tr_b16 a[28:29], v225 offset:54272\n" \
  "ds_read_b64_tr_b16 a[30:31], v225 offset:54784\n" \
  "v_cvt_pk_bf16_f32 v200, v200, v201\n" \
  "v_mfma_f32_32x32x16_bf16 v[144:159], a[48:51], a[12:15], v[144:159]\n" \
  "v_cvt_pk_bf16_f32 v201, v202, v203\n" \
  "v_add_u32_e32 v202, 0x28200, v236\n" \
  "global_store_dwordx2 v202, v[200:201], s[74:75] offset:0 sc1\n" \
  "s_waitcnt lgkmcnt(0)\n" \
  "v_mfma_f32_32x32x16_bf16 v[96:111], a[52:55], a[16:19], v[96:111]\n" \
  "ds_read_b64_tr_b16 a[0:1], v224 offset:57344\n" \
  "ds_read_b64_tr_b16 a[2:3], v224 offset:57856\n" \
  "ds_read_b64_tr_b16 a[4:5], v224 offset:58368\n" \
  "v_mfma_f32_32x32x16_bf16 v[112:127], a[52:55], a[20:23], v[112:127]\n" \
  "ds_read_b64_tr_b16 a[6:7], v224 offset:58880\n" \
  "ds_read_b64_tr_b16 a[8:9], v225 offset:57344\n" \
  "ds_read_b64_tr_b16 a[10:11], v225 offset:57856\n" \
  "v_mfma_f32_32x32x16_bf16 v[128:143], a[52:55], a[24:27], v[128:143]\n" \
  "ds_read_b64_tr_b16 a[12:13], v225 offset:58368\n" \
  "ds_read_b64_tr_b16 a[14:15], v225 offset:58880\n" \
  "v_mul_f32_e32 v204, s79, v204\n" \
  "v_mfma_f32_32x32x16_bf16 v[144:159], a[52:55], a[28:31], v[144:159]\n" \
  "v_mul_f32_e32 v205, s79, v205\n" \
  "v_mul_f32_e32 v206, s79, v206\n" \
  "v_mul_f32_e32 v207, s79, v207\n" \
  "s_waitcnt lgkmcnt(0)\n" \
  "v_mfma_f32_32x32x16_bf16 v[96:111], a[56:59], a[0:3], v[96:111]\n" \
  "ds_read_b64_tr_b16 a[16:17], v224 offset:61440\n" \
  "ds_read_b64_tr_b16 a[18:19], v224 offset:61952\n" \
  "ds_read_b64_tr_b16 a[20:21], v224 offset:62464\n" \
  "v_mfma_f32_32x32x16_bf16 v[112:127], a[56:59], a[4:7], v[112:127]\n" \
  "ds_read_b64_tr_b16 a[22:23], v224 offset:62976\n" \
  "ds_read_b64_tr_b16 a[24:25], v225 offset:61440\n" \
  "ds_read_b64_tr_b16 a[26:27], v225 offset:61952\n" \
  "v_mfma_f32_32x32x16_bf16 v[128:143], a[56:59], a[8:11], v[128:143]\n" \
  "ds_read_b64_tr_b16 a[28:29], v225 offset:62464\n" \
  "ds_read_b64_tr_b16 a[30:31], v225 offset:62976\n" \
  "v_cvt_pk_bf16_f32 v204, v204, v205\n" \
  "v_mfma_f32_32x32x16_bf16 v[144:159], a[56:59], a[12:15], v[144:159]\n" \
  "v_cvt_pk_bf16_f32 v205, v206, v207\n" \
  "v_add_u32_e32 v206, 0x28000, v239\n" \
  "s_waitcnt lgkmcnt(0)\n" \
  "v_mfma_f32_32x32x16_bf16 v[96:111], a[60:63], a[16:19], v[96:111]\n" \
  "ds_read_b64_tr_b16 a[0:1], v226 offset:0\n" \
  "ds_read_b64_tr_b16 a[2:3], v226 offset:512\n" \
  "ds_read_b64_tr_b16 a[4:5], v226 offset:1024\n" \
  "v_mfma_f32_32x32x16_bf16 v[112:127], a[60:63], a[20:23], v[112:127]\n" \
  "ds_read_b64_tr_b16 a[6:7], v226 offset:1536\n" \
  "ds_read_b64_tr_b16 a[8:9], v227 offset:0\n" \
  "ds_read_b64_tr_b16 a[10:11], v227 offset:512\n" \
  "v_mfma_f32_32x32x16_bf16 v[128:143], a[60:63], a[24:27], v[128:143]\n" \
  "ds_read_b64_tr_b16 a[12:13], v227 offset:1024\n" \
  "ds_read_b64_tr_b16 a[14:15], v227 offset:1536\n" \
  "global_store_dwordx2 v206, v[204:205], s[74:75] offset:0 sc1\n" \
  "v_mfma_f32_32x32x16_bf16 v[144:159], a[60:63], a[28:31], v[144:159]\n" \
  "v_mul_f32_e32 v208, s79, v208\n" \
  "v_mul_f32_e32 v209, s79, v209\n" \
  "v_mul_f32_e32 v210, s79, v210\n" \
  "s_waitcnt vmcnt(6) lgkmcnt(0)\n" \
  "s_barrier\n" \
  "v_mfma_f32_32x32x16_bf16 v[96:111], a[64:67], a[0:3], v[96:111]\n" \
  "ds_read_b64_tr_b16 a[16:17], v226 offset:4096\n" \
  "ds_read_b64_tr_b16 a[18:19], v226 offset:4608\n" \
  "ds_read_b64_tr_b16 a[20:21], v226 offset:5120\n" \
  "v_mfma_f32_32x32x16_bf16 v[112:127], a[64:67], a[4:7], v[112:127]\n" \
  "ds_read_b64_tr_b16 a[22:23], v226 offset:5632\n" \
  "ds_read_b64_tr_b16 a[24:25], v227 offset:4096\n" \
  "ds_read_b64_tr_b16 a[26:27], v227 offset:4608\n" \
  "v_mfma_f32_32x32x16_bf16 v[128:143], a[64:67], a[8:11], v[128:143]\n" \
  "ds_read_b64_tr_b16 a[28:29], v227 offset:5120\n" \
  "ds_read_b64_tr_b16 a[30:31], v227 offset:5632\n" \
  "v_mul_f32_e32 v211, s79, v211\n" \
  "v_mfma_f32_32x32x16_bf16 v[144:159], a[64:67], a[12:15], v[144:159]\n" \
  "v_cvt_pk_bf16_f32 v208, v208, v209\n" \
  "v_cvt_pk_bf16_f32 v209, v210, v211\n" \
  "s_waitcnt lgkmcnt(0)\n" \
  "v_mfma_f32_32x32x16_bf16 v[96:111], a[68:71], a[16:19], v[96:111]\n" \
  "ds_read_b64_tr_b16 a[0:1], v226 offset:8192\n" \
  "ds_read_b64_tr_b16 a[2:3], v226 offset:8704\n" \
  "ds_read_b64_tr_b16 a[4:5], v226 offset:9216\n" \
  "v_mfma_f32_32x32x16_bf16 v[112:127], a[68:71], a[20:23], v[112:127]\n" \
  "ds_read_b64_tr_b16 a[6:7], v226 offset:9728\n" \
  "ds_read_b64_tr_b16 a[8:9], v227 offset:8192\n" \
  "ds_read_b64_tr_b16 a[10:11], v227 offset:8704\n" \
  "v_mfma_f32_32x32x16_bf16 v[128:143], a[68:71], a[24:27], v[128:143]\n" \
  "ds_read_b64_tr_b16 a[12:13], v227 offset:9216\n" \
  "ds_read_b64_tr_b16 a[14:15], v227 offset:9728\n" \
  "v_add_u32_e32 v210, 0x50000, v239\n" \
  "v_mfma_f32_32x32x16_bf16 v[144:159], a[68:71], a[28:31], v[144:159]\n" \
  "global_store_dwordx2 v210, v[208:209], s[74:75] offset:0 sc1\n" \
  "v_mul_f32_e32 v212, s79, v212\n" \
  "v_mul_f32_e32 v213, s79, v213\n" \
  "s_waitcnt lgkmcnt(0)\n" \
  "v_mfma_f32_32x32x16_bf16 v[96:111], a[72:75], a[0:3], v[96:111]\n" \
  "ds_read_b64_tr_b16 a[16:17], v226 offset:12288\n" \
  "ds_read_b64_tr_b16 a[18:19], v226 offset:12800\n" \
  "ds_read_b64_tr_b16 a[20:21], v226 offset:13312\n" \
  "v_mfma_f32_32x32x16_bf16 v[112:127], a[72:75], a[4:7], v[112:127]\n" \
  "ds_read_b64_tr_b16 a[22:23], v226 offset:13824\n" \
  "ds_read_b64_tr_b16 a[24:25], v227 offset:12288\n" \
  "ds_read_b64_tr_b16 a[26:27], v227 offset:12800\n" \
  "v_mfma_f32_32x32x16_bf16 v[128:143], a[72:75], a[8:11], v[128:143]\n" \
  "ds_read_b64_tr_b16 a[28:29], v227 offset:13312\n" \
  "ds_read_b64_tr_b16 a[30:31], v227 offset:13824\n" \
  "v_mul_f32_e32 v214, s79, v214\n" \
  "v_mfma_f32_32x32x16_bf16 v[144:159], a[72:75], a[12:15], v[144:159]\n" \
  "v_mul_f32_e32 v215, s79, v215\n" \
  "v_cvt_pk_bf16_f32 v212, v212, v213\n" \
  "v_cvt_pk_bf16_f32 v213, v214, v215\n" \
  "s_waitcnt lgkmcnt(0)\n" \
  "v_mfma_f32_32x32x16_bf16 v[96:111], a[76:79], a[16:19], v[96:111]\n" \
  "ds_read_b64_tr_b16 a[0:1], v226 offset:16384\n" \
  "ds_read_b64_tr_b16 a[2:3], v226 offset:16896\n" \
  "ds_read_b64_tr_b16 a[4:5], v226 offset:17408\n" \
  "v_mfma_f32_32x32x16_bf16 v[112:127], a[76:79], a[20:23], v[112:127]\n" \
  "ds_read_b64_tr_b16 a[6:7], v226 offset:17920\n" \
  "ds_read_b64_tr_b16 a[8:9], v227 offset:16384\n" \
  "ds_read_b64_tr_b16 a[10:11], v227 offset:16896\n" \
  "v_mfma_f32_32x32x16_bf16 v[128:143], a[76:79], a[24:27], v[128:143]\n" \
  "ds_read_b64_tr_b16 a[12:13], v227 offset:17408\n" \
  "ds_read_b64_tr_b16 a[14:15], v227 offset:17920\n" \
  "v_add_u32_e32 v214, 0x64008, v237\n" \
  "v_mfma_f32_32x32x16_bf16 v[144:159], a[76:79], a[28:31], v[144:159]\n" \
  "global_store_dwordx2 v214, v[212:213], s[74:75] offset:0 sc1\n" \
  "v_mul_f32_e32 v216, s79, v216\n" \
  "s_waitcnt lgkmcnt(0)\n" \
  "v_mfma_f32_32x32x16_bf16 v[96:111], a[80:83], a[0:3], v[96:111]\n" \
  "ds_read_b64_tr_b16 a[16:17], v226 offset:20480\n" \
  "ds_read_b64_tr_b16 a[18:19], v226 offset:20992\n" \
  "ds_read_b64_tr_b16 a[20:21], v226 offset:21504\n" \
  "v_mfma_f32_32x32x16_bf16 v[112:127], a[80:83], a[4:7], v[112:127]\n" \
  "ds_read_b64_tr_b16 a[22:23], v226 offset:22016\n" \
  "ds_read_b64_tr_b16 a[24:25], v227 offset:20480\n" \
  "ds_read_b64_tr_b16 a[26:27], v227 offset:20992\n" \
  "v_mfma_f32_32x32x16_bf16 v[128:143], a[80:83], a[8:11], v[128:143]\n" \
  "ds_read_b64_tr_b16 a[28:29], v227 offset:21504\n" \
  "ds_read_b64_tr_b16 a[30:31], v227 offset:22016\n" \
  "v_mul_f32_e32 v217, s79, v217\n" \
  "v_mfma_f32_32x32x16_bf16 v[144:159], a[80:83], a[12:15], v[144:159]\n" \
  "v_mul_f32_e32 v218, s79, v218\n" \
  "v_mul_f32_e32 v219, s79, v219\n" \
  "v_cvt_pk_bf16_f32 v216, v216, v217\n" \
  "s_waitcnt lgkmcnt(0)\n" \
  "v_mfma_f32_32x32x16_bf16 v[96:111], a[84:87], a[16:19], v[96:111]\n" \
  "ds_read_b64_tr_b16 a[0:1], v226 offset:24576\n" \
  "ds_read_b64_tr_b16 a[2:3], v226 offset:25088\n" \
  "ds_read_b64_tr_b16 a[4:5], v226 offset:25600\n" \
  "v_mfma_f32_32x32x16_bf16 v[112:127], a[84:87], a[20:23], v[112:127]\n" \
  "ds_read_b64_tr_b16 a[6:7], v226 offset:26112\n" \
  "ds_read_b64_tr_b16 a[8:9], v227 offset:24576\n" \
  "ds_read_b64_tr_b16 a[10:11], v227 offset:25088\n" \
  "v_mfma_f32_32x32x16_bf16 v[128:143], a[84:87], a[24:27], v[128:143]\n" \
  "ds_read_b64_tr_b16 a[12:13], v227 offset:25600\n" \
  "ds_read_b64_tr_b16 a[14:15], v227 offset:26112\n" \
  "v_cvt_pk_bf16_f32 v217, v218, v219\n" \
  "v_mfma_f32_32x32x16_bf16 v[144:159], a[84:87], a[28:31], v[144:159]\n" \
  "v_add_u32_e32 v218, 0x64208, v238\n" \
  "global_store_dwordx2 v218, v[216:217], s[74:75] offset:0 sc1\n" \
  "s_waitcnt lgkmcnt(0)\n" \
  "v_mfma_f32_32x32x16_bf16 v[96:111], a[88:91], a[0:3], v[96:111]\n" \
  "ds_read_b64_tr_b16 a[16:17], v226 offset:28672\n" \
  "ds_read_b64_tr_b16 a[18:19], v226 offset:29184\n" \
  "ds_read_b64_tr_b16 a[20:21], v226 offset:29696\n" \
  "v_mfma_f32_32x32x16_bf16 v[112:127], a[88:91], a[4:7], v[112:127]\n" \
  "ds_read_b64_tr_b16 a[22:23], v226 offset:30208\n" \
  "ds_read_b64_tr_b16 a[24:25], v227 offset:28672\n" \
  "ds_read_b64_tr_b16 a[26:27], v227 offset:29184\n" \
  "v_mfma_f32_32x32x16_bf16 v[128:143], a[88:91], a[8:11], v[128:143]\n" \
  "ds_read_b64_tr_b16 a[28:29], v227 offset:29696\n" \
  "ds_read_b64_tr_b16 a[30:31], v227 offset:30208\n" \
  "v_mul_f32_e32 v220, s79, v220\n" \
  "v_mfma_f32_32x32x16_bf16 v[144:159], a[88:91], a[12:15], v[144:159]\n" \
  "v_mul_f32_e32 v221, s79, v221\n" \
  "v_mul_f32_e32 v222, s79, v222\n" \
  "v_mul_f32_e32 v223, s79, v223\n" \
  "s_waitcnt lgkmcnt(0)\n" \
  "v_mfma_f32_32x32x16_bf16 v[96:111], a[92:95], a[16:19], v[96:111]\n" \
  "v_cvt_pk_bf16_f32 v220, v220, v221\n" \
  "v_mfma_f32_32x32x16_bf16 v[112:127], a[92:95], a[20:23], v[112:127]\n" \
  "v_cvt_pk_bf16_f32 v221, v222, v223\n" \
  "v_mfma_f32_32x32x16_bf16 v[128:143], a[92:95], a[24:27], v[128:143]\n" \
  "v_add_u32_e32 v222, 0x78000, v236\n" \
  "v_mfma_f32_32x32x16_bf16 v[144:159], a[92:95], a[28:31], v[144:159]\n" \
  "global_store_dwordx2 v222, v[220:221], s[74:75] offset:0 sc1\n" \
  "3:\n" \
  "s_nop 12\n"
#define K1_CLOB "v0","v1","v2","v3","v4","v5","v6","v7","v8","v9","v10","v11","v12","v13","v14","v15","v16","v17","v18","v19","v20","v21","v22","v23","v24","v25","v26","v27","v28","v29","v30","v31","v32","v33","v34","v35","v36","v37","v38","v39","v40","v41","v42","v43","v44","v45","v46","v47","v48","v49","v50","v51","v52","v53","v54","v55","v56","v57","v58","v59","v60","v61","v62","v63","v64","v65","v66","v67","v68","v69","v70","v71","v72","v73","v74","v75","v76","v77","v78","v79","v80","v81","v82","v83","v84","v85","v86","v87","v88","v89","v90","v91","v92","v93","v94","v95","v160","v161","v162","v163","v164","v165","v166","v167","v168","v169","v170","v171","v172","v173","v174","v175","v176","v177","v178","v179","v180","v181","v182","v183","v184","v185","v186","v187","v188","v189","v190","v191","v233","v234","v235","v236","v237","v238","v239","v240","v241","v242","v243","a0","a1","a2","a3","a4","a5","a6","a7","a8","a9","a10","a11","a12","a13","a14","a15","a16","a17","a18","a19","a20","a21","a22","a23","a24","a25","a26","a27","a28","a29","a30","a31","a32","a33","a34","a35","a36","a37","a38","a39","a40","a41","a42","a43","a44","a45","a46","a47","a48","a49","a50","a51","a52","a53","a54","a55","a56","a57","a58","a59","a60","a61","a62","a63","a64","a65","a66","a67","a68","a69","a70","a71","a72","a73","a74","a75","a76","a77","a78","a79","a80","a81","a82","a83","a84","a85","a86","a87","a88","a89","a90","a91","a92","a93","a94","a95"
#define K2_PRE 7
#define K2_PRE_END "49152"
#define K2_PRE_W "5"
#define K2_ASM \
  ".p2align 3\n" \
  "s_nop 1\n" \
  "v_mfma_f32_32x32x16_bf16 v[0:15], v[124:127], v[112:115], 0\n" \
  "v_mfma_f32_32x32x8_bf16 v[0:15], v[148:149], v[120:121], v[0:15]\n" \
  "s_setprio 3\n" \
  "ds_read_b128 v[140:143], v214 offset:0\n" \
  "ds_read_b128 v[144:147], v214 offset:640\n" \
  "ds_read_b128 v[128:131], v212 offset:1024\n" \
  "s_add_u32 m0, s46, 57344\n" \
  "s_add_u32 s44, s44, 8192\n" \
  "s_addc_u32 s45, s45, 0\n" \
  "global_load_lds_dwordx4 v222, s[44:45] offset:0\n" \
  "s_nop 3\n" \
  "v_exp_f32_e32 v0, v0\n" \
  "v_exp_f32_e32 v1, v1\n" \
  "v_exp_f32_e32 v2, v2\n" \
  "v_exp_f32_e32 v3, v3\n" \
  "v_exp_f32_e64 v4, v4\n" \
  "v_mfma_f32_32x32x16_bf16 v[16:31], v[124:127], v[116:119], 0\n" \
  "v_exp_f32_e32 v5, v5\n" \
  "v_exp_f32_e32 v6, v6\n" \
  "v_mfma_f32_32x32x8_bf16 v[16:31], v[148:149], v[122:123], v[16:31]\n" \
  "v_exp_f32_e32 v7, v7\n" \
  "v_exp_f32_e32 v8, v8\n" \
  "v_exp_f32_e32 v9, v9\n" \
  "v_exp_f32_e32 v10, v10\n" \
  "global_load_dwordx4 v[164:167], v218, s[40:41] offset:-4096\n" \
  "v_exp_f32_e32 v11, v11\n" \
  "v_exp_f32_e32 v12, v12\n" \
  "v_exp_f32_e32 v13, v13\n" \
  "v_exp_f32_e32 v14, v14\n" \
  "v_exp_f32_e32 v15, v15\n" \
  "s_waitcnt lgkmcnt(0)\n" \
  "v_smfmac_f32_32x32x32_bf16 v[32:47], v[140:143], v[0:7], v217\n" \
  "s_add_u32 m0, s46, 65536\n" \
  "s_add_u32 s44, s44, 8192\n" \
  "s_addc_u32 s45, s45, 0\n" \
  "global_load_lds_dwordx4 v222, s[44:45] offset:0\n" \
  "v_exp_f32_e32 v16, v16\n" \
  "v_exp_f32_e32 v17, v17\n" \
  "v_exp_f32_e32 v18, v18\n" \
  "v_smfmac_f32_32x32x32_bf16 v[32:47], v[144:147], v[8:15], v217\n" \
  "v_exp_f32_e32 v19, v19\n" \
  "v_exp_f32_e32 v20, v20\n" \
  "v_mfma_f32_32x32x16_bf16 v[0:15], v[128:131], v[112:115], 0\n" \
  "v_exp_f32_e32 v21, v21\n" \
  "v_exp_f32_e32 v22, v22\n" \
  "v_mfma_f32_32x32x8_bf16 v[0:15], v[150:151], v[120:121], v[0:15]\n" \
  "v_exp_f32_e32 v23, v23\n" \
  "v_exp_f32_e32 v24, v24\n" \
  "v_exp_f32_e32 v25, v25\n" \
  "v_exp_f32_e32 v26, v26\n" \
  "global_load_dwordx4 v[168:171], v218, s[40:41] offset:-3072\n" \
  "v_exp_f32_e32 v27, v27\n" \
  "v_exp_f32_e32 v28, v28\n" \
  "v_exp_f32_e32 v29, v29\n" \
  "v_exp_f32_e32 v30, v30\n" \
  "v_exp_f32_e64 v31, v31\n" \
  "v_smfmac_f32_32x32x32_bf16 v[48:63], v[140:143], v[16:23], v217\n" \
  "ds_read_b128 v[132:135], v214 offset:1280\n" \
  "ds_read_b128 v[136:139], v214 offset:1920\n" \
  "ds_read_b128 v[124:127], v212 offset:2048\n" \
  "ds_read_b128 v[152:155], v213 offset:512\n" \
  "s_add_u32 m0, s46, 73728\n" \
  "s_add_u32 s44, s44, 8192\n" \
  "s_addc_u32 s45, s45, 0\n" \
  "global_load_lds_dwordx4 v222, s[44:45] offset:0\n" \
  "v_exp_f32_e32 v0, v0\n" \
  "v_exp_f32_e32 v1, v1\n" \
  "v_exp_f32_e32 v2, v2\n" \
  "v_smfmac_f32_32x32x32_bf16 v[48:63], v[144:147], v[24:31], v217\n" \
  "v_exp_f32_e32 v3, v3\n" \
  "v_exp_f32_e32 v4, v4\n" \
  "v_mfma_f32_32x32x16_bf16 v[16:31], v[128:131], v[116:119], 0\n" \
  "v_exp_f32_e32 v5, v5\n" \
  "v_exp_f32_e32 v6, v6\n" \
  "v_mfma_f32_32x32x8_bf16 v[16:31], v[150:151], v[122:123], v[16:31]\n" \
  "v_exp_f32_e32 v7, v7\n" \
  "v_exp_f32_e32 v8, v8\n" \
  "v_exp_f32_e32 v9, v9\n" \
  "v_exp_f32_e32 v10, v10\n" \
  "global_load_dwordx4 v[172:175], v218, s[40:41] offset:-2048\n" \
  "global_load_dwordx4 v[156:159], v220, s[42:43] offset:-4096\n" \
  "v_exp_f32_e32 v11, v11\n" \
  "v_exp_f32_e32 v12, v12\n" \
  "global_load_dwordx4 v[176:179], v218, s[40:41] offset:-1024\n" \
  "v_exp_f32_e32 v13, v13\n" \
  "v_exp_f32_e32 v14, v14\n" \
  "v_exp_f32_e32 v15, v15\n" \
  "s_waitcnt lgkmcnt(0)\n" \
  "v_smfmac_f32_32x32x32_bf16 v[32:47], v[132:135], v[0:7], v217\n" \
  "v_mad_i32_i24 v213, v215, s47, v213\n" \
  "s_add_u32 m0, s46, 81920\n" \
  "s_add_u32 s44, s44, 8192\n" \
  "s_addc_u32 s45, s45, 0\n" \
  "global_load_lds_dwordx4 v222, s[44:45] offset:0\n" \
  "v_exp_f32_e32 v16, v16\n" \
  "v_exp_f32_e32 v17, v17\n" \
  "v_exp_f32_e32 v18, v18\n" \
  "v_smfmac_f32_32x32x32_bf16 v[32:47], v[136:139], v[8:15], v217\n" \
  "v_exp_f32_e32 v19, v19\n" \
  "v_exp_f32_e32 v20, v20\n" \
  "v_mfma_f32_32x32x16_bf16 v[0:15], v[124:127], v[112:115], 0\n" \
  "v_exp_f32_e32 v21, v21\n" \
  "v_exp_f32_e32 v22, v22\n" \
  "v_mfma_f32_32x32x8_bf16 v[0:15], v[152:153], v[120:121], v[0:15]\n" \
  "v_exp_f32_e32 v23, v23\n" \
  "v_exp_f32_e32 v24, v24\n" \
  "v_exp_f32_e32 v25, v25\n" \
  "v_exp_f32_e32 v26, v26\n" \
  "global_load_dwordx4 v[180:183], v218, s[40:41] offset:0\n" \
  "global_load_dwordx4 v[160:163], v221, s[42:43] offset:-4096\n" \
  "v_exp_f32_e32 v27, v27\n" \
  "v_exp_f32_e32 v28, v28\n" \
  "v_exp_f32_e32 v29, v29\n" \
  "v_exp_f32_e32 v30, v30\n" \
  "v_exp_f32_e64 v31, v31\n" \
  "v_smfmac_f32_32x32x32_bf16 v[48:63], v[132:135], v[16:23], v217\n" \
  "ds_read_b128 v[140:143], v214 offset:2560\n" \
  "ds_read_b128 v[144:147], v214 offset:3200\n" \
  "ds_read_b128 v[128:131], v212 offset:3072\n" \
  "s_add_u32 m0, s46, 90112\n" \
  "s_add_u32 s44, s44, 8192\n" \
  "s_addc_u32 s45, s45, 0\n" \
  "global_load_lds_dwordx4 v222, s[44:45] offset:0\n" \
  "v_exp_f32_e32 v0, v0\n" \
  "v_exp_f32_e32 v1, v1\n" \
  "v_exp_f32_e32 v2, v2\n" \
  "v_smfmac_f32_32x32x32_bf16 v[48:63], v[136:139], v[24:31], v217\n" \
  "v_exp_f32_e32 v3, v3\n" \
  "v_exp_f32_e32 v4, v4\n" \
  "v_mfma_f32_32x32x16_bf16 v[16:31], v[124:127], v[116:119], 0\n" \
  "v_exp_f32_e32 v5, v5\n" \
  "v_exp_f32_e32 v6, v6\n" \
  "v_mfma_f32_32x32x8_bf16 v[16:31], v[152:153], v[122:123], v[16:31]\n" \
  "v_exp_f32_e32 v7, v7\n" \
  "v_exp_f32_e32 v8, v8\n" \
  "v_exp_f32_e32 v9, v9\n" \
  "v_exp_f32_e32 v10, v10\n" \
  "global_load_dwordx4 v[192:195], v218, s[40:41] offset:1024\n" \
  "v_exp_f32_e32 v11, v11\n" \
  "v_exp_f32_e32 v12, v12\n" \
  "v_exp_f32_e32 v13, v13\n" \
  "v_exp_f32_e32 v14, v14\n" \
  "v_exp_f32_e32 v15, v15\n" \
  "s_waitcnt lgkmcnt(0)\n" \
  "v_smfmac_f32_32x32x32_bf16 v[32:47], v[140:143], v[0:7], v217\n" \
  "v_add_u32_e64 v212, s47, v212\n" \
  "s_add_u32 m0, s46, 98304\n" \
  "s_add_u32 s44, s44, 8192\n" \
  "s_addc_u32 s45, s45, 0\n" \
  "global_load_lds_dwordx4 v222, s[44:45] offset:0\n" \
  "v_exp_f32_e32 v16, v16\n" \
  "v_exp_f32_e32 v17, v17\n" \
  "v_exp_f32_e32 v18, v18\n" \
  "v_smfmac_f32_32x32x32_bf16 v[32:47], v[144:147], v[8:15], v217\n" \
  "v_exp_f32_e32 v19, v19\n" \
  "v_exp_f32_e32 v20, v20\n" \
  "v_mfma_f32_32x32x16_bf16 v[0:15], v[128:131], v[112:115], 0\n" \
  "v_exp_f32_e32 v21, v21\n" \
  "v_exp_f32_e32 v22, v22\n" \
  "v_mfma_f32_32x32x8_bf16 v[0:15], v[154:155], v[120:121], v[0:15]\n" \
  "v_exp_f32_e32 v23, v23\n" \
  "v_exp_f32_e32 v24, v24\n" \
  "v_exp_f32_e32 v25, v25\n" \
  "v_exp_f32_e32 v26, v26\n" \
  "global_load_dwordx4 v[196:199], v218, s[40:41] offset:2048\n" \
  "v_exp_f32_e32 v27, v27\n" \
  "v_exp_f32_e32 v28, v28\n" \
  "v_exp_f32_e32 v29, v29\n" \
  "v_exp_f32_e32 v30, v30\n" \
  "v_exp_f32_e64 v31, v31\n" \
  "v_smfmac_f32_32x32x32_bf16 v[48:63], v[140:143], v[16:23], v217\n" \
  "ds_read_b128 v[132:135], v214 offset:3840\n" \
  "ds_read_b128 v[136:139], v214 offset:4480\n" \
  "ds_read_b128 v[124:127], v212 offset:0\n" \
  "ds_read_b128 v[148:151], v213 offset:0\n" \
  "s_add_u32 m0, s46, 106496\n" \
  "s_add_u32 s44, s44, 8192\n" \
  "s_addc_u32 s45, s45, 0\n" \
  "global_load_lds_dwordx4 v222, s[44:45] offset:0\n" \
  "v_exp_f32_e32 v0, v0\n" \
  "v_exp_f32_e32 v1, v1\n" \
  "v_exp_f32_e32 v2, v2\n" \
  "v_smfmac_f32_32x32x32_bf16 v[48:63], v[144:147], v[24:31], v217\n" \
  "v_exp_f32_e32 v3, v3\n" \
  "v_exp_f32_e32 v4, v4\n" \
  "v_mfma_f32_32x32x16_bf16 v[16:31], v[128:131], v[116:119], 0\n" \
  "v_exp_f32_e32 v5, v5\n" \
  "v_exp_f32_e32 v6, v6\n" \
  "v_mfma_f32_32x32x8_bf16 v[16:31], v[154:155], v[122:123], v[16:31]\n" \
  "v_exp_f32_e32 v7, v7\n" \
  "v_exp_f32_e32 v8, v8\n" \
  "v_exp_f32_e32 v9, v9\n" \
  "v_exp_f32_e32 v10, v10\n" \
  "global_load_dwordx4 v[200:203], v218, s[40:41] offset:3072\n" \
  "global_load_dwordx4 v[184:187], v220, s[42:43] offset:-2048\n" \
  "v_exp_f32_e32 v11, v11\n" \
  "v_exp_f32_e32 v12, v12\n" \
  "global_load_dwordx4 v[204:207], v219, s[40:41] offset:0\n" \
  "v_exp_f32_e32 v13, v13\n" \
  "v_exp_f32_e32 v14, v14\n" \
  "v_exp_f32_e32 v15, v15\n" \
  "s_waitcnt vmcnt(8) lgkmcnt(0)\n" \
  "v_smfmac_f32_32x32x32_bf16 v[32:47], v[132:135], v[0:7], v217\n" \
  "v_mad_i32_i24 v214, v216, s47, v214\n" \
  "s_add_u32 m0, s46, 114688\n" \
  "s_add_u32 s44, s44, 8192\n" \
  "s_addc_u32 s45, s45, 0\n" \
  "global_load_lds_dwordx4 v222, s[44:45] offset:0\n" \
  "v_exp_f32_e32 v16, v16\n" \
  "v_exp_f32_e32 v17, v17\n" \
  "v_exp_f32_e32 v18, v18\n" \
  "v_smfmac_f32_32x32x32_bf16 v[32:47], v[136:139], v[8:15], v217\n" \
  "v_exp_f32_e32 v19, v19\n" \
  "v_exp_f32_e32 v20, v20\n" \
  "v_mfma_f32_32x32x16_bf16 v[0:15], v[124:127], v[112:115], 0\n" \
  "v_exp_f32_e32 v21, v21\n" \
  "v_exp_f32_e32 v22, v22\n" \
  "v_mfma_f32_32x32x8_bf16 v[0:15], v[148:149], v[120:121], v[0:15]\n" \
  "v_exp_f32_e32 v23, v23\n" \
  "v_exp_f32_e32 v24, v24\n" \
  "v_exp_f32_e32 v25, v25\n" \
  "v_exp_f32_e32 v26, v26\n" \
  "global_load_dwordx4 v[208:211], v219, s[40:41] offset:1024\n" \
  "global_load_dwordx4 v[188:191], v221, s[42:43] offset:-2048\n" \
  "s_add_u32 s40, s40, 10240\n" \
  "s_addc_u32 s41, s41, 0\n" \
  "v_exp_f32_e32 v27, v27\n" \
  "v_exp_f32_e32 v28, v28\n" \
  "v_exp_f32_e32 v29, v29\n" \
  "v_exp_f32_e32 v30, v30\n" \
  "v_exp_f32_e32 v31, v31\n" \
  "v_smfmac_f32_32x32x32_bf16 v[48:63], v[132:135], v[16:23], v217\n" \
  "ds_read_b128 v[140:143], v214 offset:0\n" \
  "ds_read_b128 v[144:147], v214 offset:640\n" \
  "ds_read_b128 v[128:131], v212 offset:1024\n" \
  "v_exp_f32_e32 v0, v0\n" \
  "v_exp_f32_e32 v1, v1\n" \
  "v_exp_f32_e64 v2, v2\n" \
  "v_smfmac_f32_32x32x32_bf16 v[48:63], v[136:139], v[24:31], v217\n" \
  "v_exp_f32_e32 v3, v3\n" \
  "v_exp_f32_e32 v4, v4\n" \
  "v_mfma_f32_32x32x16_bf16 v[16:31], v[124:127], v[116:119], 0\n" \
  "v_exp_f32_e32 v5, v5\n" \
  "v_exp_f32_e32 v6, v6\n" \
  "v_mfma_f32_32x32x8_bf16 v[16:31], v[148:149], v[122:123], v[16:31]\n" \
  "v_exp_f32_e32 v7, v7\n" \
  "v_exp_f32_e32 v8, v8\n" \
  "v_exp_f32_e32 v9, v9\n" \
  "v_exp_f32_e32 v10, v10\n" \
  "v_mfma_f32_32x32x16_bf16 v[64:79], v[164:167], v[156:159], 0\n" \
  "global_load_dwordx4 v[164:167], v218, s[40:41] offset:-4096\n" \
  "v_exp_f32_e32 v11, v11\n" \
  "v_exp_f32_e32 v12, v12\n" \
  "v_exp_f32_e32 v13, v13\n" \
  "v_exp_f32_e32 v14, v14\n" \
  "v_exp_f32_e32 v15, v15\n" \
  "s_waitcnt lgkmcnt(0)\n" \
  "v_smfmac_f32_32x32x32_bf16 v[32:47], v[140:143], v[0:7], v217\n" \
  "v_exp_f32_e32 v16, v16\n" \
  "v_exp_f32_e32 v17, v17\n" \
  "v_exp_f32_e64 v18, v18\n" \
  "v_smfmac_f32_32x32x32_bf16 v[32:47], v[144:147], v[8:15], v217\n" \
  "v_exp_f32_e32 v19, v19\n" \
  "v_exp_f32_e32 v20, v20\n" \
  "v_mfma_f32_32x32x16_bf16 v[0:15], v[128:131], v[112:115], 0\n" \
  "v_exp_f32_e32 v21, v21\n" \
  "v_exp_f32_e32 v22, v22\n" \
  "v_mfma_f32_32x32x8_bf16 v[0:15], v[150:151], v[120:121], v[0:15]\n" \
  "v_exp_f32_e32 v23, v23\n" \
  "v_exp_f32_e32 v24, v24\n" \
  "v_exp_f32_e32 v25, v25\n" \
  "v_exp_f32_e32 v26, v26\n" \
  "v_mfma_f32_32x32x16_bf16 v[80:95], v[168:171], v[156:159], 0\n" \
  "global_load_dwordx4 v[168:171], v218, s[40:41] offset:-3072\n" \
  "v_exp_f32_e32 v27, v27\n" \
  "v_exp_f32_e32 v28, v28\n" \
  "v_exp_f32_e32 v29, v29\n" \
  "v_exp_f32_e32 v30, v30\n" \
  "v_exp_f32_e64 v31, v31\n" \
  "v_smfmac_f32_32x32x32_bf16 v[48:63], v[140:143], v[16:23], v217\n" \
  "ds_read_b128 v[132:135], v214 offset:1280\n" \
  "ds_read_b128 v[136:139], v214 offset:1920\n" \
  "ds_read_b128 v[124:127], v212 offset:2048\n" \
  "ds_read_b128 v[152:155], v213 offset:512\n" \
  "v_exp_f32_e32 v0, v0\n" \
  "v_exp_f32_e32 v1, v1\n" \
  "v_exp_f32_e64 v2, v2\n" \
  "v_smfmac_f32_32x32x32_bf16 v[48:63], v[144:147], v[24:31], v217\n" \
  "v_exp_f32_e32 v3, v3\n" \
  "v_exp_f32_e32 v4, v4\n" \
  "v_mfma_f32_32x32x16_bf16 v[16:31], v[128:131], v[116:119], 0\n" \
  "v_exp_f32_e32 v5, v5\n" \
  "v_exp_f32_e32 v6, v6\n" \
  "v_mfma_f32_32x32x8_bf16 v[16:31], v[150:151], v[122:123], v[16:31]\n" \
  "v_exp_f32_e32 v7, v7\n" \
  "v_exp_f32_e32 v8, v8\n" \
  "v_exp_f32_e32 v9, v9\n" \
  "v_exp_f32_e32 v10, v10\n" \
  "v_mfma_f32_32x32x16_bf16 v[96:111], v[172:175], v[156:159], 0\n" \
  "global_load_dwordx4 v[172:175], v218, s[40:41] offset:-2048\n" \
  "global_load_dwordx4 v[156:159], v220, s[42:43] offset:0\n" \
  "v_exp_f32_e32 v11, v11\n" \
  "v_exp_f32_e32 v12, v12\n" \
  "v_mfma_f32_32x32x16_bf16 v[64:79], v[176:179], v[160:163], v[64:79]\n" \
  "global_load_dwordx4 v[176:179], v218, s[40:41] offset:-1024\n" \
  "v_exp_f32_e32 v13, v13\n" \
  "v_exp_f32_e32 v14, v14\n" \
  "v_exp_f32_e32 v15, v15\n" \
  "s_waitcnt vmcnt(5) lgkmcnt(0)\n" \
  "v_smfmac_f32_32x32x32_bf16 v[32:47], v[132:135], v[0:7], v217\n" \
  "v_mad_i32_i24 v213, v215, s47, v213\n" \
  "v_exp_f32_e32 v16, v16\n" \
  "v_exp_f32_e32 v17, v17\n" \
  "v_exp_f32_e64 v18, v18\n" \
  "v_smfmac_f32_32x32x32_bf16 v[32:47], v[136:139], v[8:15], v217\n" \
  "v_exp_f32_e32 v19, v19\n" \
  "v_exp_f32_e32 v20, v20\n" \
  "v_mfma_f32_32x32x16_bf16 v[0:15], v[124:127], v[112:115], 0\n" \
  "v_exp_f32_e32 v21, v21\n" \
  "v_exp_f32_e32 v22, v22\n" \
  "v_mfma_f32_32x32x8_bf16 v[0:15], v[152:153], v[120:121], v[0:15]\n" \
  "v_exp_f32_e32 v23, v23\n" \
  "v_exp_f32_e32 v24, v24\n" \
  "v_exp_f32_e32 v25, v25\n" \
  "v_exp_f32_e32 v26, v26\n" \
  "v_mfma_f32_32x32x16_bf16 v[80:95], v[180:183], v[160:163], v[80:95]\n" \
  "global_load_dwordx4 v[180:183], v218, s[40:41] offset:0\n" \
  "global_load_dwordx4 v[160:163], v221, s[42:43] offset:0\n" \
  "v_exp_f32_e32 v27, v27\n" \
  "v_exp_f32_e32 v28, v28\n" \
  "v_exp_f32_e32 v29, v29\n" \
  "v_exp_f32_e32 v30, v30\n" \
  "v_exp_f32_e32 v31, v31\n" \
  "s_setprio 2\n" \
  "v_smfmac_f32_32x32x32_bf16 v[48:63], v[132:135], v[16:23], v217\n" \
  "ds_read_b128 v[140:143], v214 offset:2560\n" \
  "ds_read_b128 v[144:147], v214 offset:3200\n" \
  "ds_read_b128 v[128:131], v212 offset:3072\n" \
  "v_exp_f32_e32 v0, v0\n" \
  "v_exp_f32_e32 v1, v1\n" \
  "v_exp_f32_e64 v2, v2\n" \
  "v_smfmac_f32_32x32x32_bf16 v[48:63], v[136:139], v[24:31], v217\n" \
  "v_exp_f32_e32 v3, v3\n" \
  "v_exp_f32_e32 v4, v4\n" \
  "v_mfma_f32_32x32x16_bf16 v[16:31], v[124:127], v[116:119], 0\n" \
  "v_exp_f32_e32 v5, v5\n" \
  "v_exp_f32_e32 v6, v6\n" \
  "v_mfma_f32_32x32x8_bf16 v[16:31], v[152:153], v[122:123], v[16:31]\n" \
  "v_exp_f32_e32 v7, v7\n" \
  "v_exp_f32_e32 v8, v8\n" \
  "v_exp_f32_e32 v9, v9\n" \
  "v_exp_f32_e32 v10, v10\n" \
  "v_mfma_f32_32x32x16_bf16 v[64:79], v[192:195], v[184:187], v[64:79]\n" \
  "global_load_dwordx4 v[192:195], v218, s[40:41] offset:1024\n" \
  "v_exp_f32_e32 v11, v11\n" \
  "v_exp_f32_e32 v12, v12\n" \
  "v_exp_f32_e32 v13, v13\n" \
  "v_exp_f32_e32 v14, v14\n" \
  "v_exp_f32_e32 v15, v15\n" \
  "s_waitcnt lgkmcnt(0)\n" \
  "v_smfmac_f32_32x32x32_bf16 v[32:47], v[140:143], v[0:7], v217\n" \
  "v_add_u32_e32 v212, s47, v212\n" \
  "v_exp_f32_e32 v16, v16\n" \
  "v_exp_f32_e32 v17, v17\n" \
  "v_exp_f32_e32 v18, v18\n" \
  "v_smfmac_f32_32x32x32_bf16 v[32:47], v[144:147], v[8:15], v217\n" \
  "v_exp_f32_e32 v19, v19\n" \
  "v_exp_f32_e32 v20, v20\n" \
  "v_mfma_f32_32x32x16_bf16 v[0:15], v[128:131], v[112:115], 0\n" \
  "v_exp_f32_e32 v21, v21\n" \
  "v_exp_f32_e32 v22, v22\n" \
  "v_mfma_f32_32x32x8_bf16 v[0:15], v[154:155], v[120:121], v[0:15]\n" \
  "v_exp_f32_e32 v23, v23\n" \
  "v_exp_f32_e32 v24, v24\n" \
  "v_exp_f32_e32 v25, v25\n" \
  "v_exp_f32_e32 v26, v26\n" \
  "v_mfma_f32_32x32x16_bf16 v[80:95], v[196:199], v[184:187], v[80:95]\n" \
  "global_load_dwordx4 v[196:199], v218, s[40:41] offset:2048\n" \
  "v_exp_f32_e32 v27, v27\n" \
  "v_exp_f32_e32 v28, v28\n" \
  "v_exp_f32_e32 v29, v29\n" \
  "v_exp_f32_e32 v30, v30\n" \
  "v_exp_f32_e64 v31, v31\n" \
  "v_smfmac_f32_32x32x32_bf16 v[48:63], v[140:143], v[16:23], v217\n" \
  "ds_read_b128 v[132:135], v214 offset:3840\n" \
  "ds_read_b128 v[136:139], v214 offset:4480\n" \
  "ds_read_b128 v[124:127], v212 offset:0\n" \
  "ds_read_b128 v[148:151], v213 offset:0\n" \
  "v_exp_f32_e32 v0, v0\n" \
  "v_exp_f32_e32 v1, v1\n" \
  "v_exp_f32_e64 v2, v2\n" \
  "v_smfmac_f32_32x32x32_bf16 v[48:63], v[144:147], v[24:31], v217\n" \
  "v_exp_f32_e32 v3, v3\n" \
  "v_exp_f32_e32 v4, v4\n" \
  "v_mfma_f32_32x32x16_bf16 v[16:31], v[128:131], v[116:119], 0\n" \
  "v_exp_f32_e32 v5, v5\n" \
  "v_exp_f32_e32 v6, v6\n" \
  "v_mfma_f32_32x32x8_bf16 v[16:31], v[154:155], v[122:123], v[16:31]\n" \
  "v_exp_f32_e32 v7, v7\n" \
  "v_exp_f32_e32 v8, v8\n" \
  "v_exp_f32_e32 v9, v9\n" \
  "v_exp_f32_e32 v10, v10\n" \
  "v_mfma_f32_32x32x16_bf16 v[96:111], v[200:203], v[184:187], v[96:111]\n" \
  "global_load_dwordx4 v[200:203], v218, s[40:41] offset:3072\n" \
  "global_load_dwordx4 v[184:187], v220, s[42:43] offset:2048\n" \
  "v_exp_f32_e32 v11, v11\n" \
  "v_exp_f32_e32 v12, v12\n" \
  "v_mfma_f32_32x32x16_bf16 v[64:79], v[204:207], v[188:191], v[64:79]\n" \
  "global_load_dwordx4 v[204:207], v219, s[40:41] offset:0\n" \
  "v_exp_f32_e32 v13, v13\n" \
  "v_exp_f32_e32 v14, v14\n" \
  "v_exp_f32_e64 v15, v15\n" \
  "s_setprio 1\n" \
  "s_waitcnt vmcnt(5) lgkmcnt(0)\n" \
  "v_smfmac_f32_32x32x32_bf16 v[32:47], v[132:135], v[0:7], v217\n" \
  "v_mad_i32_i24 v214, v216, s47, v214\n" \
  "v_exp_f32_e32 v16, v16\n" \
  "v_exp_f32_e32 v17, v17\n" \
  "v_exp_f32_e64 v18, v18\n" \
  "v_smfmac_f32_32x32x32_bf16 v[32:47], v[136:139], v[8:15], v217\n" \
  "v_exp_f32_e32 v19, v19\n" \
  "v_exp_f32_e32 v20, v20\n" \
  "v_mfma_f32_32x32x16_bf16 v[0:15], v[124:127], v[112:115], 0\n" \
  "v_exp_f32_e32 v21, v21\n" \
  "v_exp_f32_e32 v22, v22\n" \
  "v_mfma_f32_32x32x8_bf16 v[0:15], v[148:149], v[120:121], v[0:15]\n" \
  "v_exp_f32_e32 v23, v23\n" \
  "v_exp_f32_e32 v24, v24\n" \
  "v_exp_f32_e32 v25, v25\n" \
  "v_exp_f32_e32 v26, v26\n" \
  "v_mfma_f32_32x32x16_bf16 v[80:95], v[208:211], v[188:191], v[80:95]\n" \
  "global_load_dwordx4 v[208:211], v219, s[40:41] offset:1024\n" \
  "global_load_dwordx4 v[188:191], v221, s[42:43] offset:2048\n" \
  "s_add_u32 s40, s40, 10240\n" \
  "s_addc_u32 s41, s41, 0\n" \
  "s_add_u32 s42, s42, 8192\n" \
  "s_addc_u32 s43, s43, 0\n" \
  "v_exp_f32_e32 v27, v27\n" \
  "v_exp_f32_e32 v28, v28\n" \
  "v_exp_f32_e32 v29, v29\n" \
  "v_exp_f32_e32 v30, v30\n" \
  "v_exp_f32_e64 v31, v31\n" \
  "s_barrier\n" \
  "s_setprio 3\n" \
  "v_smfmac_f32_32x32x32_bf16 v[48:63], v[132:135], v[16:23], v217\n" \
  "ds_read_b128 v[140:143], v214 offset:0\n" \
  "ds_read_b128 v[144:147], v214 offset:640\n" \
  "ds_read_b128 v[128:131], v212 offset:1024\n" \
  "s_add_u32 m0, s46, 0\n" \
  "s_add_u32 s44, s44, 8192\n" \
  "s_addc_u32 s45, s45, 0\n" \
  "global_load_lds_dwordx4 v222, s[44:45] offset:0\n" \
  "v_exp_f32_e32 v0, v0\n" \
  "v_exp_f32_e32 v1, v1\n" \
  "v_exp_f32_e64 v2, v2\n" \
  "v_smfmac_f32_32x32x32_bf16 v[48:63], v[136:139], v[24:31], v217\n" \
  "v_exp_f32_e32 v3, v3\n" \
  "v_exp_f32_e32 v4, v4\n" \
  "v_mfma_f32_32x32x16_bf16 v[16:31], v[124:127], v[116:119], 0\n" \
  "v_exp_f32_e32 v5, v5\n" \
  "v_exp_f32_e32 v6, v6\n" \
  "v_mfma_f32_32x32x8_bf16 v[16:31], v[148:149], v[122:123], v[16:31]\n" \
  "v_exp_f32_e32 v7, v7\n" \
  "v_exp_f32_e32 v8, v8\n" \
  "v_exp_f32_e32 v9, v9\n" \
  "v_exp_f32_e32 v10, v10\n" \
  "v_mfma_f32_32x32x16_bf16 v[64:79], v[164:167], v[156:159], v[64:79]\n" \
  "global_load_dwordx4 v[164:167], v218, s[40:41] offset:-4096\n" \
  "v_exp_f32_e32 v11, v11\n" \
  "v_exp_f32_e32 v12, v12\n" \
  "v_exp_f32_e32 v13, v13\n" \
  "v_exp_f32_e32 v14, v14\n" \
  "v_exp_f32_e32 v15, v15\n" \
  "s_waitcnt lgkmcnt(0)\n" \
  "v_smfmac_f32_32x32x32_bf16 v[32:47], v[140:143], v[0:7], v217\n" \
  "s_add_u32 m0, s46, 8192\n" \
  "s_add_u32 s44, s44, 8192\n" \
  "s_addc_u32 s45, s45, 0\n" \
  "global_load_lds_dwordx4 v222, s[44:45] offset:0\n" \
  "v_exp_f32_e32 v16, v16\n" \
  "v_exp_f32_e32 v17, v17\n" \
  "v_exp_f32_e32 v18, v18\n" \
  "v_smfmac_f32_32x32x32_bf16 v[32:47], v[144:147], v[8:15], v217\n" \
  "v_exp_f32_e32 v19, v19\n" \
  "v_exp_f32_e32 v20, v20\n" \
  "v_mfma_f32_32x32x16_bf16 v[0:15], v[128:131], v[112:115], 0\n" \
  "v_exp_f32_e32 v21, v21\n" \
  "v_exp_f32_e32 v22, v22\n" \
  "v_mfma_f32_32x32x8_bf16 v[0:15], v[150:151], v[120:121], v[0:15]\n" \
  "v_exp_f32_e32 v23, v23\n" \
  "v_exp_f32_e32 v24, v24\n" \
  "v_exp_f32_e32 v25, v25\n" \
  "v_exp_f32_e32 v26, v26\n" \
  "v_mfma_f32_32x32x16_bf16 v[80:95], v[168:171], v[156:159], v[80:95]\n" \
  "global_load_dwordx4 v[168:171], v218, s[40:41] offset:-3072\n" \
  "v_exp_f32_e32 v27, v27\n" \
  "v_exp_f32_e32 v28, v28\n" \
  "v_exp_f32_e32 v29, v29\n" \
  "v_exp_f32_e32 v30, v30\n" \
  "v_exp_f32_e64 v31, v31\n" \
  "v_smfmac_f32_32x32x32_bf16 v[48:63], v[140:143], v[16:23], v217\n" \
  "ds_read_b128 v[132:135], v214 offset:1280\n" \
  "ds_read_b128 v[136:139], v214 offset:1920\n" \
  "ds_read_b128 v[124:127], v212 offset:2048\n" \
  "ds_read_b128 v[152:155], v213 offset:512\n" \
  "s_add_u32 m0, s46, 16384\n" \
  "s_add_u32 s44, s44, 8192\n" \
  "s_addc_u32 s45, s45, 0\n" \
  "global_load_lds_dwordx4 v222, s[44:45] offset:0\n" \
  "v_exp_f32_e32 v0, v0\n" \
  "v_exp_f32_e32 v1, v1\n" \
  "v_exp_f32_e32 v2, v2\n" \
  "v_smfmac_f32_32x32x32_bf16 v[48:63], v[144:147], v[24:31], v217\n" \
  "v_exp_f32_e32 v3, v3\n" \
  "v_exp_f32_e32 v4, v4\n" \
  "v_mfma_f32_32x32x16_bf16 v[16:31], v[128:131], v[116:119], 0\n" \
  "v_exp_f32_e32 v5, v5\n" \
  "v_exp_f32_e32 v6, v6\n" \
  "v_mfma_f32_32x32x8_bf16 v[16:31], v[150:151], v[122:123], v[16:31]\n" \
  "v_exp_f32_e32 v7, v7\n" \
  "v_exp_f32_e32 v8, v8\n" \
  "v_exp_f32_e32 v9, v9\n" \
  "v_exp_f32_e32 v10, v10\n" \
  "v_mfma_f32_32x32x16_bf16 v[96:111], v[172:175], v[156:159], v[96:111]\n" \
  "global_load_dwordx4 v[172:175], v218, s[40:41] offset:-2048\n" \
  "global_load_dwordx4 v[156:159], v220, s[42:43] offset:-4096\n" \
  "v_exp_f32_e32 v11, v11\n" \
  "v_exp_f32_e32 v12, v12\n" \
  "v_mfma_f32_32x32x16_bf16 v[64:79], v[176:179], v[160:163], v[64:79]\n" \
  "global_load_dwordx4 v[176:179], v218, s[40:41] offset:-1024\n" \
  "v_exp_f32_e32 v13, v13\n" \
  "v_exp_f32_e32 v14, v14\n" \
  "v_exp_f32_e32 v15, v15\n" \
  "s_waitcnt vmcnt(8) lgkmcnt(0)\n" \
  "v_smfmac_f32_32x32x32_bf16 v[32:47], v[132:135], v[0:7], v217\n" \
  "v_mad_i32_i24 v213, v215, s47, v213\n" \
  "s_add_u32 m0, s46, 24576\n" \
  "s_add_u32 s44, s44, 8192\n" \
  "s_addc_u32 s45, s45, 0\n" \
  "global_load_lds_dwordx4 v222, s[44:45] offset:0\n" \
  "v_exp_f32_e32 v16, v16\n" \
  "v_exp_f32_e32 v17, v17\n" \
  "v_exp_f32_e32 v18, v18\n" \
  "v_smfmac_f32_32x32x32_bf16 v[32:47], v[136:139], v[8:15], v217\n" \
  "v_exp_f32_e32 v19, v19\n" \
  "v_exp_f32_e32 v20, v20\n" \
  "v_mfma_f32_32x32x16_bf16 v[0:15], v[124:127], v[112:115], 0\n" \
  "v_exp_f32_e32 v21, v21\n" \
  "v_exp_f32_e32 v22, v22\n" \
  "v_mfma_f32_32x32x8_bf16 v[0:15], v[152:153], v[120:121], v[0:15]\n" \
  "v_exp_f32_e32 v23, v23\n" \
  "v_exp_f32_e32 v24, v24\n" \
  "v_exp_f32_e32 v25, v25\n" \
  "v_exp_f32_e32 v26, v26\n" \
  "v_mfma_f32_32x32x16_bf16 v[80:95], v[180:183], v[160:163], v[80:95]\n" \
  "global_load_dwordx4 v[180:183], v218, s[40:41] offset:0\n" \
  "global_load_dwordx4 v[160:163], v221, s[42:43] offset:-4096\n" \
  "v_exp_f32_e32 v27, v27\n" \
  "v_exp_f32_e32 v28, v28\n" \
  "v_exp_f32_e32 v29, v29\n" \
  "v_exp_f32_e32 v30, v30\n" \
  "v_exp_f32_e64 v31, v31\n" \
  "v_smfmac_f32_32x32x32_bf16 v[48:63], v[132:135], v[16:23], v217\n" \
  "ds_read_b128 v[140:143], v214 offset:2560\n" \
  "ds_read_b128 v[144:147], v214 offset:3200\n" \
  "ds_read_b128 v[128:131], v212 offset:3072\n" \
  "s_add_u32 m0, s46, 32768\n" \
  "s_add_u32 s44, s44, 8192\n" \
  "s_addc_u32 s45, s45, 0\n" \
  "global_load_lds_dwordx4 v222, s[44:45] offset:0\n" \
  "v_exp_f32_e32 v0, v0\n" \
  "v_exp_f32_e32 v1, v1\n" \
  "v_exp_f32_e32 v2, v2\n" \
  "v_smfmac_f32_32x32x32_bf16 v[48:63], v[136:139], v[24:31], v217\n" \
  "v_exp_f32_e32 v3, v3\n" \
  "v_exp_f32_e32 v4, v4\n" \
  "v_mfma_f32_32x32x16_bf16 v[16:31], v[124:127], v[116:119], 0\n" \
  "v_exp_f32_e32 v5, v5\n" \
  "v_exp_f32_e32 v6, v6\n" \
  "v_mfma_f32_32x32x8_bf16 v[16:31], v[152:153], v[122:123], v[16:31]\n" \
  "v_exp_f32_e32 v7, v7\n" \
  "v_exp_f32_e32 v8, v8\n" \
  "v_exp_f32_e32 v9, v9\n" \
  "v_exp_f32_e32 v10, v10\n" \
  "v_mfma_f32_32x32x16_bf16 v[64:79], v[192:195], v[184:187], v[64:79]\n" \
  "global_load_dwordx4 v[192:195], v218, s[40:41] offset:1024\n" \
  "v_exp_f32_e32 v11, v11\n" \
  "v_exp_f32_e32 v12, v12\n" \
  "v_exp_f32_e32 v13, v13\n" \
  "v_exp_f32_e32 v14, v14\n" \
  "v_exp_f32_e32 v15, v15\n" \
  "s_waitcnt lgkmcnt(0)\n" \
  "v_smfmac_f32_32x32x32_bf16 v[32:47], v[140:143], v[0:7], v217\n" \
  "v_add_u32_e32 v212, s47, v212\n" \
  "v_exp_f32_e32 v16, v16\n" \
  "v_exp_f32_e32 v17, v17\n" \
  "v_exp_f32_e32 v18, v18\n" \
  "v_smfmac_f32_32x32x32_bf16 v[32:47], v[144:147], v[8:15], v217\n" \
  "v_exp_f32_e32 v19, v19\n" \
  "v_exp_f32_e32 v20, v20\n" \
  "v_mfma_f32_32x32x16_bf16 v[0:15], v[128:131], v[112:115], 0\n" \
  "v_exp_f32_e32 v21, v21\n" \
  "v_exp_f32_e32 v22, v22\n" \
  "v_mfma_f32_32x32x8_bf16 v[0:15], v[154:155], v[120:121], v[0:15]\n" \
  "v_exp_f32_e32 v23, v23\n" \
  "v_exp_f32_e32 v24, v24\n" \
  "v_exp_f32_e32 v25, v25\n" \
  "v_exp_f32_e32 v26, v26\n" \
  "v_mfma_f32_32x32x16_bf16 v[80:95], v[196:199], v[184:187], v[80:95]\n" \
  "global_load_dwordx4 v[196:199], v218, s[40:41] offset:2048\n" \
  "v_exp_f32_e32 v27, v27\n" \
  "v_exp_f32_e32 v28, v28\n" \
  "v_exp_f32_e32 v29, v29\n" \
  "v_exp_f32_e32 v30, v30\n" \
  "v_exp_f32_e64 v31, v31\n" \
  "v_smfmac_f32_32x32x32_bf16 v[48:63], v[140:143], v[16:23], v217\n" \
  "ds_read_b128 v[132:135], v214 offset:3840\n" \
  "ds_read_b128 v[136:139], v214 offset:4480\n" \
  "ds_read_b128 v[124:127], v212 offset:0\n" \
  "ds_read_b128 v[148:151], v213 offset:0\n" \
  "v_exp_f32_e32 v0, v0\n" \
  "v_exp_f32_e32 v1, v1\n" \
  "v_exp_f32_e64 v2, v2\n" \
  "v_smfmac_f32_32x32x32_bf16 v[48:63], v[144:147], v[24:31], v217\n" \
  "v_exp_f32_e32 v3, v3\n" \
  "v_exp_f32_e32 v4, v4\n" \
  "v_mfma_f32_32x32x16_bf16 v[16:31], v[128:131], v[116:119], 0\n" \
  "v_exp_f32_e32 v5, v5\n" \
  "v_exp_f32_e32 v6, v6\n" \
  "v_mfma_f32_32x32x8_bf16 v[16:31], v[154:155], v[122:123], v[16:31]\n" \
  "v_exp_f32_e32 v7, v7\n" \
  "v_exp_f32_e32 v8, v8\n" \
  "v_exp_f32_e32 v9, v9\n" \
  "v_exp_f32_e32 v10, v10\n" \
  "v_mfma_f32_32x32x16_bf16 v[96:111], v[200:203], v[184:187], v[96:111]\n" \
  "global_load_dwordx4 v[200:203], v218, s[40:41] offset:3072\n" \
  "global_load_dwordx4 v[184:187], v220, s[42:43] offset:-2048\n" \
  "v_exp_f32_e32 v11, v11\n" \
  "v_exp_f32_e32 v12, v12\n" \
  "v_mfma_f32_32x32x16_bf16 v[64:79], v[204:207], v[188:191], v[64:79]\n" \
  "global_load_dwordx4 v[204:207], v219, s[40:41] offset:0\n" \
  "v_exp_f32_e32 v13, v13\n" \
  "v_exp_f32_e32 v14, v14\n" \
  "v_exp_f32_e32 v15, v15\n" \
  "s_waitcnt vmcnt(6) lgkmcnt(0)\n" \
  "v_smfmac_f32_32x32x32_bf16 v[32:47], v[132:135], v[0:7], v217\n" \
  "v_mad_i32_i24 v214, v216, s47, v214\n" \
  "v_exp_f32_e32 v16, v16\n" \
  "v_exp_f32_e32 v17, v17\n" \
  "v_exp_f32_e64 v18, v18\n" \
  "v_smfmac_f32_32x32x32_bf16 v[32:47], v[136:139], v[8:15], v217\n" \
  "v_exp_f32_e32 v19, v19\n" \
  "v_exp_f32_e32 v20, v20\n" \
  "v_mfma_f32_32x32x16_bf16 v[0:15], v[124:127], v[112:115], 0\n" \
  "v_exp_f32_e32 v21, v21\n" \
  "v_exp_f32_e32 v22, v22\n" \
  "v_mfma_f32_32x32x8_bf16 v[0:15], v[148:149], v[120:121], v[0:15]\n" \
  "v_exp_f32_e32 v23, v23\n" \
  "v_exp_f32_e32 v24, v24\n" \
  "v_exp_f32_e32 v25, v25\n" \
  "v_exp_f32_e32 v26, v26\n" \
  "v_mfma_f32_32x32x16_bf16 v[80:95], v[208:211], v[188:191], v[80:95]\n" \
  "global_load_dwordx4 v[208:211], v219, s[40:41] offset:1024\n" \
  "global_load_dwordx4 v[188:191], v221, s[42:43] offset:-2048\n" \
  "s_add_u32 s40, s40, 10240\n" \
  "s_addc_u32 s41, s41, 0\n" \
  "s_add_u32 s44, s40, 53248\n" \
  "s_addc_u32 s45, s41, 0\n" \
  "v_exp_f32_e32 v27, v27\n" \
  "v_exp_f32_e32 v28, v28\n" \
  "v_exp_f32_e32 v29, v29\n" \
  "v_exp_f32_e32 v30, v30\n" \
  "v_exp_f32_e64 v31, v31\n" \
  "v_smfmac_f32_32x32x32_bf16 v[48:63], v[132:135], v[16:23], v217\n" \
  "ds_read_b128 v[140:143], v214 offset:0\n" \
  "ds_read_b128 v[144:147], v214 offset:640\n" \
  "ds_read_b128 v[128:131], v212 offset:1024\n" \
  "v_exp_f32_e32 v0, v0\n" \
  "v_exp_f32_e32 v1, v1\n" \
  "v_exp_f32_e64 v2, v2\n" \
  "v_smfmac_f32_32x32x32_bf16 v[48:63], v[136:139], v[24:31], v217\n" \
  "v_exp_f32_e32 v3, v3\n" \
  "v_exp_f32_e32 v4, v4\n" \
  "v_mfma_f32_32x32x16_bf16 v[16:31], v[124:127], v[116:119], 0\n" \
  "v_exp_f32_e32 v5, v5\n" \
  "v_exp_f32_e32 v6, v6\n" \
  "v_mfma_f32_32x32x8_bf16 v[16:31], v[148:149], v[122:123], v[16:31]\n" \
  "v_exp_f32_e32 v7, v7\n" \
  "v_exp_f32_e32 v8, v8\n" \
  "v_exp_f32_e32 v9, v9\n" \
  "v_exp_f32_e32 v10, v10\n" \
  "v_mfma_f32_32x32x16_bf16 v[64:79], v[164:167], v[156:159], v[64:79]\n" \
  "global_load_dwordx4 v[164:167], v218, s[40:41] offset:-4096\n" \
  "v_exp_f32_e32 v11, v11\n" \
  "v_exp_f32_e32 v12, v12\n" \
  "v_exp_f32_e32 v13, v13\n" \
  "v_exp_f32_e32 v14, v14\n" \
  "v_exp_f32_e32 v15, v15\n" \
  "s_waitcnt lgkmcnt(0)\n" \
  "v_smfmac_f32_32x32x32_bf16 v[32:47], v[140:143], v[0:7], v217\n" \
  "v_exp_f32_e32 v16, v16\n" \
  "v_exp_f32_e32 v17, v17\n" \
  "v_exp_f32_e64 v18, v18\n" \
  "v_smfmac_f32_32x32x32_bf16 v[32:47], v[144:147], v[8:15], v217\n" \
  "v_exp_f32_e32 v19, v19\n" \
  "v_exp_f32_e32 v20, v20\n" \
  "v_mfma_f32_32x32x16_bf16 v[0:15], v[128:131], v[112:115], 0\n" \
  "v_exp_f32_e32 v21, v21\n" \
  "v_exp_f32_e32 v22, v22\n" \
  "v_mfma_f32_32x32x8_bf16 v[0:15], v[150:151], v[120:121], v[0:15]\n" \
  "v_exp_f32_e32 v23, v23\n" \
  "v_exp_f32_e32 v24, v24\n" \
  "v_exp_f32_e32 v25, v25\n" \
  "v_exp_f32_e32 v26, v26\n" \
  "v_mfma_f32_32x32x16_bf16 v[80:95], v[168:171], v[156:159], v[80:95]\n" \
  "global_load_dwordx4 v[168:171], v218, s[40:41] offset:-3072\n" \
  "v_exp_f32_e32 v27, v27\n" \
  "v_exp_f32_e32 v28, v28\n" \
  "v_exp_f32_e32 v29, v29\n" \
  "v_exp_f32_e32 v30, v30\n" \
  "v_exp_f32_e64 v31, v31\n" \
  "v_smfmac_f32_32x32x32_bf16 v[48:63], v[140:143], v[16:23], v217\n" \
  "ds_read_b128 v[132:135], v214 offset:1280\n" \
  "ds_read_b128 v[136:139], v214 offset:1920\n" \
  "ds_read_b128 v[124:127], v212 offset:2048\n" \
  "ds_read_b128 v[152:155], v213 offset:512\n" \
  "v_exp_f32_e32 v0, v0\n" \
  "v_exp_f32_e32 v1, v1\n" \
  "v_exp_f32_e64 v2, v2\n" \
  "v_smfmac_f32_32x32x32_bf16 v[48:63], v[144:147], v[24:31], v217\n" \
  "v_exp_f32_e32 v3, v3\n" \
  "v_exp_f32_e32 v4, v4\n" \
  "v_mfma_f32_32x32x16_bf16 v[16:31], v[128:131], v[116:119], 0\n" \
  "v_exp_f32_e32 v5, v5\n" \
  "v_exp_f32_e32 v6, v6\n" \
  "v_mfma_f32_32x32x8_bf16 v[16:31], v[150:151], v[122:123], v[16:31]\n" \
  "v_exp_f32_e32 v7, v7\n" \
  "v_exp_f32_e32 v8, v8\n" \
  "v_exp_f32_e32 v9, v9\n" \
  "v_exp_f32_e32 v10, v10\n" \
  "v_mfma_f32_32x32x16_bf16 v[96:111], v[172:175], v[156:159], v[96:111]\n" \
  "global_load_dwordx4 v[172:175], v218, s[40:41] offset:-2048\n" \
  "global_load_dwordx4 v[156:159], v220, s[42:43] offset:0\n" \
  "v_exp_f32_e32 v11, v11\n" \
  "v_exp_f32_e32 v12, v12\n" \
  "v_mfma_f32_32x32x16_bf16 v[64:79], v[176:179], v[160:163], v[64:79]\n" \
  "global_load_dwordx4 v[176:179], v218, s[40:41] offset:-1024\n" \
  "v_exp_f32_e32 v13, v13\n" \
  "v_exp_f32_e32 v14, v14\n" \
  "v_exp_f32_e32 v15, v15\n" \
  "s_waitcnt vmcnt(5) lgkmcnt(0)\n" \
  "v_smfmac_f32_32x32x32_bf16 v[32:47], v[132:135], v[0:7], v217\n" \
  "v_mad_i32_i24 v213, v215, s47, v213\n" \
  "v_exp_f32_e32 v16, v16\n" \
  "v_exp_f32_e32 v17, v17\n" \
  "v_exp_f32_e64 v18, v18\n" \
  "v_smfmac_f32_32x32x32_bf16 v[32:47], v[136:139], v[8:15], v217\n" \
  "v_exp_f32_e32 v19, v19\n" \
  "v_exp_f32_e32 v20, v20\n" \
  "v_mfma_f32_32x32x16_bf16 v[0:15], v[124:127], v[112:115], 0\n" \
  "v_exp_f32_e32 v21, v21\n" \
  "v_exp_f32_e32 v22, v22\n" \
  "v_mfma_f32_32x32x8_bf16 v[0:15], v[152:153], v[120:121], v[0:15]\n" \
  "v_exp_f32_e32 v23, v23\n" \
  "v_exp_f32_e32 v24, v24\n" \
  "v_exp_f32_e32 v25, v25\n" \
  "v_exp_f32_e32 v26, v26\n" \
  "v_mfma_f32_32x32x16_bf16 v[80:95], v[180:183], v[160:163], v[80:95]\n" \
  "global_load_dwordx4 v[180:183], v218, s[40:41] offset:0\n" \
  "global_load_dwordx4 v[160:163], v221, s[42:43] offset:0\n" \
  "v_exp_f32_e32 v27, v27\n" \
  "v_exp_f32_e32 v28, v28\n" \
  "v_exp_f32_e32 v29, v29\n" \
  "v_exp_f32_e32 v30, v30\n" \
  "v_exp_f32_e64 v31, v31\n" \
  "v_smfmac_f32_32x32x32_bf16 v[48:63], v[132:135], v[16:23], v217\n" \
  "ds_read_b128 v[140:143], v214 offset:2560\n" \
  "ds_read_b128 v[144:147], v214 offset:3200\n" \
  "ds_read_b128 v[128:131], v212 offset:3072\n" \
  "v_exp_f32_e32 v0, v0\n" \
  "v_exp_f32_e32 v1, v1\n" \
  "v_exp_f32_e64 v2, v2\n" \
  "v_smfmac_f32_32x32x32_bf16 v[48:63], v[136:139], v[24:31], v217\n" \
  "v_exp_f32_e32 v3, v3\n" \
  "v_exp_f32_e32 v4, v4\n" \
  "v_mfma_f32_32x32x16_bf16 v[16:31], v[124:127], v[116:119], 0\n" \
  "v_exp_f32_e32 v5, v5\n" \
  "v_exp_f32_e32 v6, v6\n" \
  "v_mfma_f32_32x32x8_bf16 v[16:31], v[152:153], v[122:123], v[16:31]\n" \
  "v_exp_f32_e32 v7, v7\n" \
  "v_exp_f32_e32 v8, v8\n" \
  "v_exp_f32_e32 v9, v9\n" \
  "v_exp_f32_e32 v10, v10\n" \
  "v_mfma_f32_32x32x16_bf16 v[64:79], v[192:195], v[184:187], v[64:79]\n" \
  "global_load_dwordx4 v[192:195], v218, s[40:41] offset:1024\n" \
  "v_exp_f32_e32 v11, v11\n" \
  "v_exp_f32_e32 v12, v12\n" \
  "v_exp_f32_e32 v13, v13\n" \
  "v_exp_f32_e32 v14, v14\n" \
  "v_exp_f32_e32 v15, v15\n" \
  "s_waitcnt lgkmcnt(0)\n" \
  "v_smfmac_f32_32x32x32_bf16 v[32:47], v[140:143], v[0:7], v217\n" \
  "v_add_u32_e32 v212, s47, v212\n" \
  "v_exp_f32_e32 v16, v16\n" \
  "v_exp_f32_e32 v17, v17\n" \
  "v_exp_f32_e32 v18, v18\n" \
  "v_smfmac_f32_32x32x32_bf16 v[32:47], v[144:147], v[8:15], v217\n" \
  "v_exp_f32_e32 v19, v19\n" \
  "v_exp_f32_e32 v20, v20\n" \
  "v_mfma_f32_32x32x16_bf16 v[0:15], v[128:131], v[112:115], 0\n" \
  "v_exp_f32_e32 v21, v21\n" \
  "v_exp_f32_e32 v22, v22\n" \
  "v_mfma_f32_32x32x8_bf16 v[0:15], v[154:155], v[120:121], v[0:15]\n" \
  "v_exp_f32_e32 v23, v23\n" \
  "v_exp_f32_e32 v24, v24\n" \
  "v_exp_f32_e32 v25, v25\n" \
  "v_exp_f32_e32 v26, v26\n" \
  "v_mfma_f32_32x32x16_bf16 v[80:95], v[196:199], v[184:187], v[80:95]\n" \
  "global_load_dwordx4 v[196:199], v218, s[40:41] offset:2048\n" \
  "v_exp_f32_e32 v27, v27\n" \
  "v_exp_f32_e32 v28, v28\n" \
  "v_exp_f32_e32 v29, v29\n" \
  "v_exp_f32_e32 v30, v30\n" \
  "v_exp_f32_e64 v31, v31\n" \
  "v_smfmac_f32_32x32x32_bf16 v[48:63], v[140:143], v[16:23], v217\n" \
  "ds_read_b128 v[132:135], v214 offset:3840\n" \
  "ds_read_b128 v[136:139], v214 offset:4480\n" \
  "ds_read_b128 v[124:127], v212 offset:0\n" \
  "ds_read_b128 v[148:151], v213 offset:0\n" \
  "v_exp_f32_e32 v0, v0\n" \
  "v_exp_f32_e32 v1, v1\n" \
  "v_exp_f32_e64 v2, v2\n" \
  "v_smfmac_f32_32x32x32_bf16 v[48:63], v[144:147], v[24:31], v217\n" \
  "v_exp_f32_e32 v3, v3\n" \
  "v_exp_f32_e32 v4, v4\n" \
  "v_mfma_f32_32x32x16_bf16 v[16:31], v[128:131], v[116:119], 0\n" \
  "v_exp_f32_e32 v5, v5\n" \
  "v_exp_f32_e32 v6, v6\n" \
  "v_mfma_f32_32x32x8_bf16 v[16:31], v[154:155], v[122:123], v[16:31]\n" \
  "v_exp_f32_e32 v7, v7\n" \
  "v_exp_f32_e32 v8, v8\n" \
  "v_exp_f32_e32 v9, v9\n" \
  "v_exp_f32_e32 v10, v10\n" \
  "v_mfma_f32_32x32x16_bf16 v[96:111], v[200:203], v[184:187], v[96:111]\n" \
  "global_load_dwordx4 v[200:203], v218, s[40:41] offset:3072\n" \
  "global_load_dwordx4 v[184:187], v220, s[42:43] offset:2048\n" \
  "v_exp_f32_e32 v11, v11\n" \
  "v_exp_f32_e32 v12, v12\n" \
  "v_mfma_f32_32x32x16_bf16 v[64:79], v[204:207], v[188:191], v[64:79]\n" \
  "global_load_dwordx4 v[204:207], v219, s[40:41] offset:0\n" \
  "v_exp_f32_e32 v13, v13\n" \
  "v_exp_f32_e32 v14, v14\n" \
  "v_exp_f32_e32 v15, v15\n" \
  "s_waitcnt vmcnt(5) lgkmcnt(0)\n" \
  "v_smfmac_f32_32x32x32_bf16 v[32:47], v[132:135], v[0:7], v217\n" \
  "v_mad_i32_i24 v214, v216, s47, v214\n" \
  "v_exp_f32_e32 v16, v16\n" \
  "v_exp_f32_e32 v17, v17\n" \
  "v_exp_f32_e64 v18, v18\n" \
  "v_smfmac_f32_32x32x32_bf16 v[32:47], v[136:139], v[8:15], v217\n" \
  "v_exp_f32_e32 v19, v19\n" \
  "v_exp_f32_e32 v20, v20\n" \
  "v_mfma_f32_32x32x16_bf16 v[0:15], v[124:127], v[112:115], 0\n" \
  "v_exp_f32_e32 v21, v21\n" \
  "v_exp_f32_e32 v22, v22\n" \
  "v_mfma_f32_32x32x8_bf16 v[0:15], v[148:149], v[120:121], v[0:15]\n" \
  "v_exp_f32_e32 v23, v23\n" \
  "v_exp_f32_e32 v24, v24\n" \
  "v_exp_f32_e32 v25, v25\n" \
  "v_exp_f32_e32 v26, v26\n" \
  "v_mfma_f32_32x32x16_bf16 v[80:95], v[208:211], v[188:191], v[80:95]\n" \
  "global_load_dwordx4 v[208:211], v219, s[40:41] offset:1024\n" \
  "global_load_dwordx4 v[188:191], v221, s[42:43] offset:2048\n" \
  "s_add_u32 s40, s40, 10240\n" \
  "s_addc_u32 s41, s41, 0\n" \
  "s_add_u32 s42, s42, 8192\n" \
  "s_addc_u32 s43, s43, 0\n" \
  "v_exp_f32_e32 v27, v27\n" \
  "v_exp_f32_e32 v28, v28\n" \
  "v_exp_f32_e32 v29, v29\n" \
  "v_exp_f32_e32 v30, v30\n" \
  "v_exp_f32_e64 v31, v31\n" \
  "v_smfmac_f32_32x32x32_bf16 v[48:63], v[132:135], v[16:23], v217\n" \
  "ds_read_b128 v[140:143], v214 offset:0\n" \
  "ds_read_b128 v[144:147], v214 offset:640\n" \
  "ds_read_b128 v[128:131], v212 offset:1024\n" \
  "v_exp_f32_e32 v0, v0\n" \
  "v_exp_f32_e32 v1, v1\n" \
  "v_exp_f32_e64 v2, v2\n" \
  "v_smfmac_f32_32x32x32_bf16 v[48:63], v[136:139], v[24:31], v217\n" \
  "v_exp_f32_e32 v3, v3\n" \
  "v_exp_f32_e32 v4, v4\n" \
  "v_mfma_f32_32x32x16_bf16 v[16:31], v[124:127], v[116:119], 0\n" \
  "v_exp_f32_e32 v5, v5\n" \
  "v_exp_f32_e32 v6, v6\n" \
  "v_mfma_f32_32x32x8_bf16 v[16:31], v[148:149], v[122:123], v[16:31]\n" \
  "v_exp_f32_e32 v7, v7\n" \
  "v_exp_f32_e32 v8, v8\n" \
  "v_exp_f32_e32 v9, v9\n" \
  "v_exp_f32_e32 v10, v10\n" \
  "v_mfma_f32_32x32x16_bf16 v[64:79], v[164:167], v[156:159], v[64:79]\n" \
  "global_load_dwordx4 v[164:167], v218, s[40:41] offset:-4096\n" \
  "v_exp_f32_e32 v11, v11\n" \
  "v_exp_f32_e32 v12, v12\n" \
  "v_exp_f32_e32 v13, v13\n" \
  "v_exp_f32_e32 v14, v14\n" \
  "v_exp_f32_e32 v15, v15\n" \
  "s_waitcnt lgkmcnt(0)\n" \
  "v_smfmac_f32_32x32x32_bf16 v[32:47], v[140:143], v[0:7], v217\n" \
  "v_exp_f32_e32 v16, v16\n" \
  "v_exp_f32_e32 v17, v17\n" \
  "v_exp_f32_e64 v18, v18\n" \
  "v_smfmac_f32_32x32x32_bf16 v[32:47], v[144:147], v[8:15], v217\n" \
  "v_exp_f32_e32 v19, v19\n" \
  "v_exp_f32_e32 v20, v20\n" \
  "v_mfma_f32_32x32x16_bf16 v[0:15], v[128:131], v[112:115], 0\n" \
  "v_exp_f32_e32 v21, v21\n" \
  "v_exp_f32_e32 v22, v22\n" \
  "v_mfma_f32_32x32x8_bf16 v[0:15], v[150:151], v[120:121], v[0:15]\n" \
  "v_exp_f32_e32 v23, v23\n" \
  "v_exp_f32_e32 v24, v24\n" \
  "v_exp_f32_e32 v25, v25\n" \
  "v_exp_f32_e32 v26, v26\n" \
  "v_mfma_f32_32x32x16_bf16 v[80:95], v[168:171], v[156:159], v[80:95]\n" \
  "global_load_dwordx4 v[168:171], v218, s[40:41] offset:-3072\n" \
  "v_exp_f32_e32 v27, v27\n" \
  "v_exp_f32_e32 v28, v28\n" \
  "v_exp_f32_e32 v29, v29\n" \
  "v_exp_f32_e32 v30, v30\n" \
  "v_exp_f32_e64 v31, v31\n" \
  "v_smfmac_f32_32x32x32_bf16 v[48:63], v[140:143], v[16:23], v217\n" \
  "ds_read_b128 v[132:135], v214 offset:1280\n" \
  "ds_read_b128 v[136:139], v214 offset:1920\n" \
  "ds_read_b128 v[124:127], v212 offset:2048\n" \
  "ds_read_b128 v[152:155], v213 offset:512\n" \
  "v_exp_f32_e32 v0, v0\n" \
  "v_exp_f32_e32 v1, v1\n" \
  "v_exp_f32_e64 v2, v2\n" \
  "v_smfmac_f32_32x32x32_bf16 v[48:63], v[144:147], v[24:31], v217\n" \
  "v_exp_f32_e32 v3, v3\n" \
  "v_exp_f32_e32 v4, v4\n" \
  "v_mfma_f32_32x32x16_bf16 v[16:31], v[128:131], v[116:119], 0\n" \
  "v_exp_f32_e32 v5, v5\n" \
  "v_exp_f32_e32 v6, v6\n" \
  "v_mfma_f32_32x32x8_bf16 v[16:31], v[150:151], v[122:123], v[16:31]\n" \
  "v_exp_f32_e32 v7, v7\n" \
  "v_exp_f32_e32 v8, v8\n" \
  "v_exp_f32_e32 v9, v9\n" \
  "v_exp_f32_e32 v10, v10\n" \
  "v_mfma_f32_32x32x16_bf16 v[96:111], v[172:175], v[156:159], v[96:111]\n" \
  "global_load_dwordx4 v[172:175], v218, s[40:41] offset:-2048\n" \
  "global_load_dwordx4 v[156:159], v220, s[42:43] offset:-4096\n" \
  "v_exp_f32_e32 v11, v11\n" \
  "v_exp_f32_e32 v12, v12\n" \
  "v_mfma_f32_32x32x16_bf16 v[64:79], v[176:179], v[160:163], v[64:79]\n" \
  "global_load_dwordx4 v[176:179], v218, s[40:41] offset:-1024\n" \
  "v_exp_f32_e32 v13, v13\n" \
  "v_exp_f32_e32 v14, v14\n" \
  "v_exp_f32_e64 v15, v15\n" \
  "s_setprio 2\n" \
  "s_waitcnt vmcnt(5) lgkmcnt(0)\n" \
  "v_smfmac_f32_32x32x32_bf16 v[32:47], v[132:135], v[0:7], v217\n" \
  "v_mad_i32_i24 v213, v215, s47, v213\n" \
  "v_exp_f32_e32 v16, v16\n" \
  "v_exp_f32_e32 v17, v17\n" \
  "v_exp_f32_e64 v18, v18\n" \
  "v_smfmac_f32_32x32x32_bf16 v[32:47], v[136:139], v[8:15], v217\n" \
  "v_exp_f32_e32 v19, v19\n" \
  "v_exp_f32_e32 v20, v20\n" \
  "v_mfma_f32_32x32x16_bf16 v[0:15], v[124:127], v[112:115], 0\n" \
  "v_exp_f32_e32 v21, v21\n" \
  "v_exp_f32_e32 v22, v22\n" \
  "v_mfma_f32_32x32x8_bf16 v[0:15], v[152:153], v[120:121], v[0:15]\n" \
  "v_exp_f32_e32 v23, v23\n" \
  "v_exp_f32_e32 v24, v24\n" \
  "v_exp_f32_e32 v25, v25\n" \
  "v_exp_f32_e32 v26, v26\n" \
  "v_mfma_f32_32x32x16_bf16 v[80:95], v[180:183], v[160:163], v[80:95]\n" \
  "global_load_dwordx4 v[180:183], v218, s[40:41] offset:0\n" \
  "global_load_dwordx4 v[160:163], v221, s[42:43] offset:-4096\n" \
  "v_exp_f32_e32 v27, v27\n" \
  "v_exp_f32_e32 v28, v28\n" \
  "v_exp_f32_e32 v29, v29\n" \
  "v_exp_f32_e32 v30, v30\n" \
  "v_exp_f32_e64 v31, v31\n" \
  "v_smfmac_f32_32x32x32_bf16 v[48:63], v[132:135], v[16:23], v217\n" \
  "ds_read_b128 v[140:143], v214 offset:2560\n" \
  "ds_read_b128 v[144:147], v214 offset:3200\n" \
  "ds_read_b128 v[128:131], v212 offset:3072\n" \
  "v_exp_f32_e32 v0, v0\n" \
  "v_exp_f32_e32 v1, v1\n" \
  "v_exp_f32_e64 v2, v2\n" \
  "v_smfmac_f32_32x32x32_bf16 v[48:63], v[136:139], v[24:31], v217\n" \
  "v_exp_f32_e32 v3, v3\n" \
  "v_exp_f32_e32 v4, v4\n" \
  "v_mfma_f32_32x32x16_bf16 v[16:31], v[124:127], v[116:119], 0\n" \
  "v_exp_f32_e32 v5, v5\n" \
  "v_exp_f32_e32 v6, v6\n" \
  "v_mfma_f32_32x32x8_bf16 v[16:31], v[152:153], v[122:123], v[16:31]\n" \
  "v_exp_f32_e32 v7, v7\n" \
  "v_exp_f32_e32 v8, v8\n" \
  "v_exp_f32_e32 v9, v9\n" \
  "v_exp_f32_e32 v10, v10\n" \
  "v_mfma_f32_32x32x16_bf16 v[64:79], v[192:195], v[184:187], v[64:79]\n" \
  "global_load_dwordx4 v[192:195], v218, s[40:41] offset:1024\n" \
  "v_exp_f32_e32 v11, v11\n" \
  "v_exp_f32_e32 v12, v12\n" \
  "v_exp_f32_e32 v13, v13\n" \
  "v_exp_f32_e32 v14, v14\n" \
  "v_exp_f32_e32 v15, v15\n" \
  "s_waitcnt lgkmcnt(0)\n" \
  "v_smfmac_f32_32x32x32_bf16 v[32:47], v[140:143], v[0:7], v217\n" \
  "v_add_u32_e32 v212, s47, v212\n" \
  "v_exp_f32_e32 v16, v16\n" \
  "v_exp_f32_e32 v17, v17\n" \
  "v_exp_f32_e32 v18, v18\n" \
  "v_smfmac_f32_32x32x32_bf16 v[32:47], v[144:147], v[8:15], v217\n" \
  "v_exp_f32_e32 v19, v19\n" \
  "v_exp_f32_e32 v20, v20\n" \
  "v_mfma_f32_32x32x16_bf16 v[0:15], v[128:131], v[112:115], 0\n" \
  "v_exp_f32_e32 v21, v21\n" \
  "v_exp_f32_e32 v22, v22\n" \
  "v_mfma_f32_32x32x8_bf16 v[0:15], v[154:155], v[120:121], v[0:15]\n" \
  "v_exp_f32_e32 v23, v23\n" \
  "v_exp_f32_e32 v24, v24\n" \
  "v_exp_f32_e32 v25, v25\n" \
  "v_exp_f32_e32 v26, v26\n" \
  "v_mfma_f32_32x32x16_bf16 v[80:95], v[196:199], v[184:187], v[80:95]\n" \
  "global_load_dwordx4 v[196:199], v218, s[40:41] offset:2048\n" \
  "v_exp_f32_e32 v27, v27\n" \
  "v_exp_f32_e32 v28, v28\n" \
  "v_exp_f32_e32 v29, v29\n" \
  "v_exp_f32_e32 v30, v30\n" \
  "v_exp_f32_e64 v31, v31\n" \
  "v_smfmac_f32_32x32x32_bf16 v[48:63], v[140:143], v[16:23], v217\n" \
  "ds_read_b128 v[132:135], v214 offset:3840\n" \
  "ds_read_b128 v[136:139], v214 offset:4480\n" \
  "ds_read_b128 v[124:127], v212 offset:0\n" \
  "ds_read_b128 v[148:151], v213 offset:0\n" \
  "v_exp_f32_e32 v0, v0\n" \
  "v_exp_f32_e32 v1, v1\n" \
  "v_exp_f32_e64 v2, v2\n" \
  "v_smfmac_f32_32x32x32_bf16 v[48:63], v[144:147], v[24:31], v217\n" \
  "v_exp_f32_e32 v3, v3\n" \
  "v_exp_f32_e32 v4, v4\n" \
  "v_mfma_f32_32x32x16_bf16 v[16:31], v[128:131], v[116:119], 0\n" \
  "v_exp_f32_e32 v5, v5\n" \
  "v_exp_f32_e32 v6, v6\n" \
  "v_mfma_f32_32x32x8_bf16 v[16:31], v[154:155], v[122:123], v[16:31]\n" \
  "v_exp_f32_e32 v7, v7\n" \
  "v_exp_f32_e32 v8, v8\n" \
  "v_exp_f32_e32 v9, v9\n" \
  "v_exp_f32_e32 v10, v10\n" \
  "v_mfma_f32_32x32x16_bf16 v[96:111], v[200:203], v[184:187], v[96:111]\n" \
  "global_load_dwordx4 v[200:203], v218, s[40:41] offset:3072\n" \
  "global_load_dwordx4 v[184:187], v220, s[42:43] offset:-2048\n" \
  "v_exp_f32_e32 v11, v11\n" \
  "v_exp_f32_e32 v12, v12\n" \
  "v_mfma_f32_32x32x16_bf16 v[64:79], v[204:207], v[188:191], v[64:79]\n" \
  "global_load_dwordx4 v[204:207], v219, s[40:41] offset:0\n" \
  "v_exp_f32_e32 v13, v13\n" \
  "v_exp_f32_e32 v14, v14\n" \
  "v_exp_f32_e64 v15, v15\n" \
  "s_setprio 1\n" \
  "s_waitcnt vmcnt(5) lgkmcnt(0)\n" \
  "v_smfmac_f32_32x32x32_bf16 v[32:47], v[132:135], v[0:7], v217\n" \
  "v_mad_i32_i24 v214, v216, s47, v214\n" \
  "v_exp_f32_e32 v16, v16\n" \
  "v_exp_f32_e32 v17, v17\n" \
  "v_exp_f32_e64 v18, v18\n" \
  "v_smfmac_f32_32x32x32_bf16 v[32:47], v[136:139], v[8:15], v217\n" \
  "v_exp_f32_e32 v19, v19\n" \
  "v_exp_f32_e32 v20, v20\n" \
  "v_mfma_f32_32x32x16_bf16 v[0:15], v[124:127], v[112:115], 0\n" \
  "v_exp_f32_e32 v21, v21\n" \
  "v_exp_f32_e32 v22, v22\n" \
  "v_mfma_f32_32x32x8_bf16 v[0:15], v[148:149], v[120:121], v[0:15]\n" \
  "v_exp_f32_e32 v23, v23\n" \
  "v_exp_f32_e32 v24, v24\n" \
  "v_exp_f32_e32 v25, v25\n" \
  "v_exp_f32_e32 v26, v26\n" \
  "v_mfma_f32_32x32x16_bf16 v[80:95], v[208:211], v[188:191], v[80:95]\n" \
  "global_load_dwordx4 v[208:211], v219, s[40:41] offset:1024\n" \
  "global_load_dwordx4 v[188:191], v221, s[42:43] offset:-2048\n" \
  "s_add_u32 s40, s40, 10240\n" \
  "s_addc_u32 s41, s41, 0\n" \
  "v_exp_f32_e32 v27, v27\n" \
  "v_exp_f32_e32 v28, v28\n" \
  "v_exp_f32_e32 v29, v29\n" \
  "v_exp_f32_e32 v30, v30\n" \
  "v_exp_f32_e32 v31, v31\n" \
  "s_barrier\n" \
  "s_setprio 3\n" \
  "v_smfmac_f32_32x32x32_bf16 v[48:63], v[132:135], v[16:23], v217\n" \
  "ds_read_b128 v[140:143], v214 offset:0\n" \
  "ds_read_b128 v[144:147], v214 offset:640\n" \
  "ds_read_b128 v[128:131], v212 offset:1024\n" \
  "v_exp_f32_e32 v0, v0\n" \
  "v_exp_f32_e32 v1, v1\n" \
  "v_exp_f32_e64 v2, v2\n" \
  "v_smfmac_f32_32x32x32_bf16 v[48:63], v[136:139], v[24:31], v217\n" \
  "v_exp_f32_e32 v3, v3\n" \
  "v_exp_f32_e32 v4, v4\n" \
  "v_mfma_f32_32x32x16_bf16 v[16:31], v[124:127], v[116:119], 0\n" \
  "v_exp_f32_e32 v5, v5\n" \
  "v_exp_f32_e32 v6, v6\n" \
  "v_mfma_f32_32x32x8_bf16 v[16:31], v[148:149], v[122:123], v[16:31]\n" \
  "v_exp_f32_e32 v7, v7\n" \
  "v_exp_f32_e32 v8, v8\n" \
  "v_exp_f32_e32 v9, v9\n" \
  "v_exp_f32_e32 v10, v10\n" \
  "global_load_dwordx4 v[224:227], v218, s[44:45] offset:-2048\n" \
  "v_mfma_f32_32x32x16_bf16 v[64:79], v[164:167], v[156:159], v[64:79]\n" \
  "global_load_dwordx4 v[164:167], v218, s[40:41] offset:-4096\n" \
  "v_exp_f32_e32 v11, v11\n" \
  "v_exp_f32_e32 v12, v12\n" \
  "v_exp_f32_e32 v13, v13\n" \
  "v_exp_f32_e32 v14, v14\n" \
  "v_exp_f32_e32 v15, v15\n" \
  "s_waitcnt lgkmcnt(0)\n" \
  "v_smfmac_f32_32x32x32_bf16 v[32:47], v[140:143], v[0:7], v217\n" \
  "v_exp_f32_e32 v16, v16\n" \
  "v_exp_f32_e32 v17, v17\n" \
  "v_exp_f32_e64 v18, v18\n" \
  "v_smfmac_f32_32x32x32_bf16 v[32:47], v[144:147], v[8:15], v217\n" \
  "v_exp_f32_e32 v19, v19\n" \
  "v_exp_f32_e32 v20, v20\n" \
  "v_mfma_f32_32x32x16_bf16 v[0:15], v[128:131], v[112:115], 0\n" \
  "v_exp_f32_e32 v21, v21\n" \
  "v_exp_f32_e32 v22, v22\n" \
  "v_mfma_f32_32x32x8_bf16 v[0:15], v[150:151], v[120:121], v[0:15]\n" \
  "v_exp_f32_e32 v23, v23\n" \
  "v_exp_f32_e32 v24, v24\n" \
  "v_exp_f32_e32 v25, v25\n" \
  "v_exp_f32_e32 v26, v26\n" \
  "global_load_dwordx4 v[228:231], v218, s[44:45] offset:-1024\n" \
  "v_mfma_f32_32x32x16_bf16 v[80:95], v[168:171], v[156:159], v[80:95]\n" \
  "global_load_dwordx4 v[168:171], v218, s[40:41] offset:-3072\n" \
  "v_exp_f32_e32 v27, v27\n" \
  "v_exp_f32_e32 v28, v28\n" \
  "v_exp_f32_e32 v29, v29\n" \
  "v_exp_f32_e32 v30, v30\n" \
  "v_exp_f32_e64 v31, v31\n" \
  "v_smfmac_f32_32x32x32_bf16 v[48:63], v[140:143], v[16:23], v217\n" \
  "ds_read_b128 v[132:135], v214 offset:1280\n" \
  "ds_read_b128 v[136:139], v214 offset:1920\n" \
  "ds_read_b128 v[124:127], v212 offset:2048\n" \
  "ds_read_b128 v[152:155], v213 offset:512\n" \
  "v_exp_f32_e32 v0, v0\n" \
  "v_exp_f32_e32 v1, v1\n" \
  "v_exp_f32_e64 v2, v2\n" \
  "v_smfmac_f32_32x32x32_bf16 v[48:63], v[144:147], v[24:31], v217\n" \
  "v_exp_f32_e32 v3, v3\n" \
  "v_exp_f32_e32 v4, v4\n" \
  "v_mfma_f32_32x32x16_bf16 v[16:31], v[128:131], v[116:119], 0\n" \
  "v_exp_f32_e32 v5, v5\n" \
  "v_exp_f32_e32 v6, v6\n" \
  "v_mfma_f32_32x32x8_bf16 v[16:31], v[150:151], v[122:123], v[16:31]\n" \
  "v_exp_f32_e32 v7, v7\n" \
  "v_exp_f32_e32 v8, v8\n" \
  "v_exp_f32_e32 v9, v9\n" \
  "v_exp_f32_e32 v10, v10\n" \
  "v_mfma_f32_32x32x16_bf16 v[96:111], v[172:175], v[156:159], v[96:111]\n" \
  "global_load_dwordx4 v[172:175], v218, s[40:41] offset:-2048\n" \
  "global_load_dwordx4 v[156:159], v220, s[42:43] offset:0\n" \
  "v_exp_f32_e32 v11, v11\n" \
  "v_exp_f32_e32 v12, v12\n" \
  "v_mfma_f32_32x32x16_bf16 v[64:79], v[176:179], v[160:163], v[64:79]\n" \
  "global_load_dwordx4 v[176:179], v218, s[40:41] offset:-1024\n" \
  "v_exp_f32_e32 v13, v13\n" \
  "v_exp_f32_e32 v14, v14\n" \
  "v_exp_f32_e32 v15, v15\n" \
  "s_waitcnt vmcnt(7) lgkmcnt(0)\n" \
  "v_smfmac_f32_32x32x32_bf16 v[32:47], v[132:135], v[0:7], v217\n" \
  "v_mad_i32_i24 v213, v215, s48, v213\n" \
  "v_exp_f32_e32 v16, v16\n" \
  "v_exp_f32_e32 v17, v17\n" \
  "v_exp_f32_e64 v18, v18\n" \
  "v_smfmac_f32_32x32x32_bf16 v[32:47], v[136:139], v[8:15], v217\n" \
  "v_exp_f32_e32 v19, v19\n" \
  "v_exp_f32_e32 v20, v20\n" \
  "v_mfma_f32_32x32x16_bf16 v[0:15], v[124:127], v[112:115], 0\n" \
  "v_exp_f32_e32 v21, v21\n" \
  "v_exp_f32_e32 v22, v22\n" \
  "v_mfma_f32_32x32x8_bf16 v[0:15], v[152:153], v[120:121], v[0:15]\n" \
  "v_exp_f32_e32 v23, v23\n" \
  "v_exp_f32_e32 v24, v24\n" \
  "v_exp_f32_e32 v25, v25\n" \
  "v_exp_f32_e32 v26, v26\n" \
  "v_mfma_f32_32x32x16_bf16 v[80:95], v[180:183], v[160:163], v[80:95]\n" \
  "global_load_dwordx4 v[180:183], v218, s[40:41] offset:0\n" \
  "global_load_dwordx4 v[160:163], v221, s[42:43] offset:0\n" \
  "v_exp_f32_e32 v27, v27\n" \
  "v_exp_f32_e32 v28, v28\n" \
  "v_exp_f32_e32 v29, v29\n" \
  "v_exp_f32_e32 v30, v30\n" \
  "v_exp_f32_e64 v31, v31\n" \
  "v_smfmac_f32_32x32x32_bf16 v[48:63], v[132:135], v[16:23], v217\n" \
  "ds_read_b128 v[140:143], v214 offset:2560\n" \
  "ds_read_b128 v[144:147], v214 offset:3200\n" \
  "ds_read_b128 v[128:131], v212 offset:3072\n" \
  "v_exp_f32_e32 v0, v0\n" \
  "v_exp_f32_e32 v1, v1\n" \
  "v_exp_f32_e64 v2, v2\n" \
  "v_smfmac_f32_32x32x32_bf16 v[48:63], v[136:139], v[24:31], v217\n" \
  "v_exp_f32_e32 v3, v3\n" \
  "v_exp_f32_e32 v4, v4\n" \
  "v_mfma_f32_32x32x16_bf16 v[16:31], v[124:127], v[116:119], 0\n" \
  "v_exp_f32_e32 v5, v5\n" \
  "v_exp_f32_e32 v6, v6\n" \
  "v_mfma_f32_32x32x8_bf16 v[16:31], v[152:153], v[122:123], v[16:31]\n" \
  "v_exp_f32_e32 v7, v7\n" \
  "v_exp_f32_e32 v8, v8\n" \
  "v_exp_f32_e32 v9, v9\n" \
  "v_exp_f32_e32 v10, v10\n" \
  "global_load_dwordx4 v[232:235], v218, s[44:45] offset:0\n" \
  "v_mfma_f32_32x32x16_bf16 v[64:79], v[192:195], v[184:187], v[64:79]\n" \
  "global_load_dwordx4 v[192:195], v218, s[40:41] offset:1024\n" \
  "v_exp_f32_e32 v11, v11\n" \
  "v_exp_f32_e32 v12, v12\n" \
  "v_exp_f32_e32 v13, v13\n" \
  "v_exp_f32_e32 v14, v14\n" \
  "v_exp_f32_e32 v15, v15\n" \
  "s_waitcnt lgkmcnt(0)\n" \
  "v_smfmac_f32_32x32x32_bf16 v[32:47], v[140:143], v[0:7], v217\n" \
  "v_add_u32_e32 v212, s48, v212\n" \
  "v_exp_f32_e32 v16, v16\n" \
  "v_exp_f32_e32 v17, v17\n" \
  "v_exp_f32_e32 v18, v18\n" \
  "v_smfmac_f32_32x32x32_bf16 v[32:47], v[144:147], v[8:15], v217\n" \
  "v_exp_f32_e32 v19, v19\n" \
  "v_exp_f32_e32 v20, v20\n" \
  "v_mfma_f32_32x32x16_bf16 v[0:15], v[128:131], v[112:115], 0\n" \
  "v_exp_f32_e32 v21, v21\n" \
  "v_exp_f32_e32 v22, v22\n" \
  "v_mfma_f32_32x32x8_bf16 v[0:15], v[154:155], v[120:121], v[0:15]\n" \
  "v_exp_f32_e32 v23, v23\n" \
  "v_exp_f32_e32 v24, v24\n" \
  "v_exp_f32_e32 v25, v25\n" \
  "v_exp_f32_e32 v26, v26\n" \
  "global_load_dwordx4 v[236:239], v218, s[44:45] offset:1024\n" \
  "v_mfma_f32_32x32x16_bf16 v[80:95], v[196:199], v[184:187], v[80:95]\n" \
  "global_load_dwordx4 v[196:199], v218, s[40:41] offset:2048\n" \
  "v_exp_f32_e32 v27, v27\n" \
  "v_exp_f32_e32 v28, v28\n" \
  "v_exp_f32_e32 v29, v29\n" \
  "v_exp_f32_e32 v30, v30\n" \
  "v_exp_f32_e64 v31, v31\n" \
  "v_smfmac_f32_32x32x32_bf16 v[48:63], v[140:143], v[16:23], v217\n" \
  "ds_read_b128 v[132:135], v214 offset:3840\n" \
  "ds_read_b128 v[136:139], v214 offset:4480\n" \
  "ds_read_b128 v[124:127], v212 offset:0\n" \
  "ds_read_b128 v[148:151], v213 offset:0\n" \
  "v_exp_f32_e32 v0, v0\n" \
  "v_exp_f32_e32 v1, v1\n" \
  "v_exp_f32_e64 v2, v2\n" \
  "v_smfmac_f32_32x32x32_bf16 v[48:63], v[144:147], v[24:31], v217\n" \
  "v_exp_f32_e32 v3, v3\n" \
  "v_exp_f32_e32 v4, v4\n" \
  "v_mfma_f32_32x32x16_bf16 v[16:31], v[128:131], v[116:119], 0\n" \
  "v_exp_f32_e32 v5, v5\n" \
  "v_exp_f32_e32 v6, v6\n" \
  "v_mfma_f32_32x32x8_bf16 v[16:31], v[154:155], v[122:123], v[16:31]\n" \
  "v_exp_f32_e32 v7, v7\n" \
  "v_exp_f32_e32 v8, v8\n" \
  "v_exp_f32_e32 v9, v9\n" \
  "v_exp_f32_e32 v10, v10\n" \
  "v_mfma_f32_32x32x16_bf16 v[96:111], v[200:203], v[184:187], v[96:111]\n" \
  "global_load_dwordx4 v[200:203], v218, s[40:41] offset:3072\n" \
  "global_load_dwordx4 v[184:187], v220, s[42:43] offset:2048\n" \
  "v_exp_f32_e32 v11, v11\n" \
  "v_exp_f32_e32 v12, v12\n" \
  "v_mfma_f32_32x32x16_bf16 v[64:79], v[204:207], v[188:191], v[64:79]\n" \
  "global_load_dwordx4 v[204:207], v219, s[40:41] offset:0\n" \
  "v_exp_f32_e32 v13, v13\n" \
  "v_exp_f32_e32 v14, v14\n" \
  "v_exp_f32_e32 v15, v15\n" \
  "s_waitcnt vmcnt(7) lgkmcnt(0)\n" \
  "v_smfmac_f32_32x32x32_bf16 v[32:47], v[132:135], v[0:7], v217\n" \
  "v_mad_i32_i24 v214, v216, s48, v214\n" \
  "v_exp_f32_e32 v16, v16\n" \
  "v_exp_f32_e32 v17, v17\n" \
  "v_exp_f32_e64 v18, v18\n" \
  "v_smfmac_f32_32x32x32_bf16 v[32:47], v[136:139], v[8:15], v217\n" \
  "v_exp_f32_e32 v19, v19\n" \
  "v_exp_f32_e32 v20, v20\n" \
  "v_mfma_f32_32x32x16_bf16 v[0:15], v[124:127], v[112:115], 0\n" \
  "v_exp_f32_e32 v21, v21\n" \
  "v_exp_f32_e32 v22, v22\n" \
  "v_mfma_f32_32x32x8_bf16 v[0:15], v[148:149], v[120:121], v[0:15]\n" \
  "v_exp_f32_e32 v23, v23\n" \
  "v_exp_f32_e32 v24, v24\n" \
  "v_exp_f32_e32 v25, v25\n" \
  "v_exp_f32_e32 v26, v26\n" \
  "v_mfma_f32_32x32x16_bf16 v[80:95], v[208:211], v[188:191], v[80:95]\n" \
  "global_load_dwordx4 v[208:211], v219, s[40:41] offset:1024\n" \
  "global_load_dwordx4 v[188:191], v221, s[42:43] offset:2048\n" \
  "s_add_u32 s40, s40, 10240\n" \
  "s_addc_u32 s41, s41, 0\n" \
  "s_add_u32 s42, s42, 8192\n" \
  "s_addc_u32 s43, s43, 0\n" \
  "v_exp_f32_e32 v27, v27\n" \
  "v_exp_f32_e32 v28, v28\n" \
  "v_exp_f32_e32 v29, v29\n" \
  "v_exp_f32_e32 v30, v30\n" \
  "v_exp_f32_e64 v31, v31\n" \
  "v_smfmac_f32_32x32x32_bf16 v[48:63], v[132:135], v[16:23], v217\n" \
  "ds_read_b128 v[140:143], v214 offset:0\n" \
  "ds_read_b128 v[144:147], v214 offset:640\n" \
  "ds_read_b128 v[128:131], v212 offset:1024\n" \
  "v_exp_f32_e32 v0, v0\n" \
  "v_exp_f32_e32 v1, v1\n" \
  "v_exp_f32_e64 v2, v2\n" \
  "v_smfmac_f32_32x32x32_bf16 v[48:63], v[136:139], v[24:31], v217\n" \
  "v_exp_f32_e32 v3, v3\n" \
  "v_exp_f32_e32 v4, v4\n" \
  "v_mfma_f32_32x32x16_bf16 v[16:31], v[124:127], v[116:119], 0\n" \
  "v_exp_f32_e32 v5, v5\n" \
  "v_exp_f32_e32 v6, v6\n" \
  "v_mfma_f32_32x32x8_bf16 v[16:31], v[148:149], v[122:123], v[16:31]\n" \
  "v_exp_f32_e32 v7, v7\n" \
  "v_exp_f32_e32 v8, v8\n" \
  "v_exp_f32_e32 v9, v9\n" \
  "v_exp_f32_e32 v10, v10\n" \
  "global_load_dwordx4 v[240:243], v218, s[44:45] offset:2048\n" \
  "v_mfma_f32_32x32x16_bf16 v[64:79], v[164:167], v[156:159], v[64:79]\n" \
  "global_load_dwordx4 v[164:167], v218, s[40:41] offset:-4096\n" \
  "v_exp_f32_e32 v11, v11\n" \
  "v_exp_f32_e32 v12, v12\n" \
  "v_exp_f32_e32 v13, v13\n" \
  "v_exp_f32_e32 v14, v14\n" \
  "v_exp_f32_e32 v15, v15\n" \
  "s_waitcnt lgkmcnt(0)\n" \
  "v_smfmac_f32_32x32x32_bf16 v[32:47], v[140:143], v[0:7], v217\n" \
  "v_exp_f32_e32 v16, v16\n" \
  "v_exp_f32_e32 v17, v17\n" \
  "v_exp_f32_e64 v18, v18\n" \
  "v_smfmac_f32_32x32x32_bf16 v[32:47], v[144:147], v[8:15], v217\n" \
  "v_exp_f32_e32 v19, v19\n" \
  "v_exp_f32_e32 v20, v20\n" \
  "v_mfma_f32_32x32x16_bf16 v[0:15], v[128:131], v[112:115], 0\n" \
  "v_exp_f32_e32 v21, v21\n" \
  "v_exp_f32_e32 v22, v22\n" \
  "v_mfma_f32_32x32x8_bf16 v[0:15], v[150:151], v[120:121], v[0:15]\n" \
  "v_exp_f32_e32 v23, v23\n" \
  "v_exp_f32_e32 v24, v24\n" \
  "v_exp_f32_e32 v25, v25\n" \
  "v_exp_f32_e32 v26, v26\n" \
  "global_load_dwordx4 v[244:247], v218, s[44:45] offset:3072\n" \
  "v_mfma_f32_32x32x16_bf16 v[80:95], v[168:171], v[156:159], v[80:95]\n" \
  "global_load_dwordx4 v[168:171], v218, s[40:41] offset:-3072\n" \
  "v_exp_f32_e32 v27, v27\n" \
  "v_exp_f32_e32 v28, v28\n" \
  "v_exp_f32_e32 v29, v29\n" \
  "v_exp_f32_e32 v30, v30\n" \
  "v_exp_f32_e64 v31, v31\n" \
  "v_smfmac_f32_32x32x32_bf16 v[48:63], v[140:143], v[16:23], v217\n" \
  "ds_read_b128 v[132:135], v214 offset:1280\n" \
  "ds_read_b128 v[136:139], v214 offset:1920\n" \
  "ds_read_b128 v[124:127], v212 offset:2048\n" \
  "ds_read_b128 v[152:155], v213 offset:512\n" \
  "v_exp_f32_e32 v0, v0\n" \
  "v_exp_f32_e32 v1, v1\n" \
  "v_exp_f32_e64 v2, v2\n" \
  "v_smfmac_f32_32x32x32_bf16 v[48:63], v[144:147], v[24:31], v217\n" \
  "v_exp_f32_e32 v3, v3\n" \
  "v_exp_f32_e32 v4, v4\n" \
  "v_mfma_f32_32x32x16_bf16 v[16:31], v[128:131], v[116:119], 0\n" \
  "v_exp_f32_e32 v5, v5\n" \
  "v_exp_f32_e32 v6, v6\n" \
  "v_mfma_f32_32x32x8_bf16 v[16:31], v[150:151], v[122:123], v[16:31]\n" \
  "v_exp_f32_e32 v7, v7\n" \
  "v_exp_f32_e32 v8, v8\n" \
  "v_exp_f32_e32 v9, v9\n" \
  "v_exp_f32_e32 v10, v10\n" \
  "v_mfma_f32_32x32x16_bf16 v[96:111], v[172:175], v[156:159], v[96:111]\n" \
  "global_load_dwordx4 v[172:175], v218, s[40:41] offset:-2048\n" \
  "global_load_dwordx4 v[156:159], v220, s[42:43] offset:-4096\n" \
  "v_exp_f32_e32 v11, v11\n" \
  "v_exp_f32_e32 v12, v12\n" \
  "v_mfma_f32_32x32x16_bf16 v[64:79], v[176:179], v[160:163], v[64:79]\n" \
  "global_load_dwordx4 v[176:179], v218, s[40:41] offset:-1024\n" \
  "v_exp_f32_e32 v13, v13\n" \
  "v_exp_f32_e32 v14, v14\n" \
  "v_exp_f32_e32 v15, v15\n" \
  "s_waitcnt vmcnt(7) lgkmcnt(0)\n" \
  "v_smfmac_f32_32x32x32_bf16 v[32:47], v[132:135], v[0:7], v217\n" \
  "v_mad_i32_i24 v213, v215, s47, v213\n" \
  "v_exp_f32_e32 v16, v16\n" \
  "v_exp_f32_e32 v17, v17\n" \
  "v_exp_f32_e64 v18, v18\n" \
  "v_smfmac_f32_32x32x32_bf16 v[32:47], v[136:139], v[8:15], v217\n" \
  "v_exp_f32_e32 v19, v19\n" \
  "v_exp_f32_e32 v20, v20\n" \
  "v_mfma_f32_32x32x16_bf16 v[0:15], v[124:127], v[112:115], 0\n" \
  "v_exp_f32_e32 v21, v21\n" \
  "v_exp_f32_e32 v22, v22\n" \
  "v_mfma_f32_32x32x8_bf16 v[0:15], v[152:153], v[120:121], v[0:15]\n" \
  "v_exp_f32_e32 v23, v23\n" \
  "v_exp_f32_e32 v24, v24\n" \
  "v_exp_f32_e32 v25, v25\n" \
  "v_exp_f32_e32 v26, v26\n" \
  "v_mfma_f32_32x32x16_bf16 v[80:95], v[180:183], v[160:163], v[80:95]\n" \
  "global_load_dwordx4 v[180:183], v218, s[40:41] offset:0\n" \
  "global_load_dwordx4 v[160:163], v221, s[42:43] offset:-4096\n" \
  "v_exp_f32_e32 v27, v27\n" \
  "v_exp_f32_e32 v28, v28\n" \
  "v_exp_f32_e32 v29, v29\n" \
  "v_exp_f32_e32 v30, v30\n" \
  "v_exp_f32_e64 v31, v31\n" \
  "v_smfmac_f32_32x32x32_bf16 v[48:63], v[132:135], v[16:23], v217\n" \
  "ds_read_b128 v[140:143], v214 offset:2560\n" \
  "ds_read_b128 v[144:147], v214 offset:3200\n" \
  "ds_read_b128 v[128:131], v212 offset:3072\n" \
  "v_exp_f32_e32 v0, v0\n" \
  "v_exp_f32_e32 v1, v1\n" \
  "v_exp_f32_e64 v2, v2\n" \
  "v_smfmac_f32_32x32x32_bf16 v[48:63], v[136:139], v[24:31], v217\n" \
  "v_exp_f32_e32 v3, v3\n" \
  "v_exp_f32_e32 v4, v4\n" \
  "v_mfma_f32_32x32x16_bf16 v[16:31], v[124:127], v[116:119], 0\n" \
  "v_exp_f32_e32 v5, v5\n" \
  "v_exp_f32_e32 v6, v6\n" \
  "v_mfma_f32_32x32x8_bf16 v[16:31], v[152:153], v[122:123], v[16:31]\n" \
  "v_exp_f32_e32 v7, v7\n" \
  "v_exp_f32_e32 v8, v8\n" \
  "v_exp_f32_e32 v9, v9\n" \
  "v_exp_f32_e32 v10, v10\n" \
  "global_load_dwordx4 v[248:251], v219, s[44:45] offset:0\n" \
  "v_mfma_f32_32x32x16_bf16 v[64:79], v[192:195], v[184:187], v[64:79]\n" \
  "global_load_dwordx4 v[192:195], v218, s[40:41] offset:1024\n" \
  "v_exp_f32_e32 v11, v11\n" \
  "v_exp_f32_e32 v12, v12\n" \
  "v_exp_f32_e32 v13, v13\n" \
  "v_exp_f32_e32 v14, v14\n" \
  "v_exp_f32_e32 v15, v15\n" \
  "s_waitcnt lgkmcnt(0)\n" \
  "v_smfmac_f32_32x32x32_bf16 v[32:47], v[140:143], v[0:7], v217\n" \
  "v_add_u32_e32 v212, s47, v212\n" \
  "v_exp_f32_e32 v16, v16\n" \
  "v_exp_f32_e32 v17, v17\n" \
  "v_exp_f32_e32 v18, v18\n" \
  "v_smfmac_f32_32x32x32_bf16 v[32:47], v[144:147], v[8:15], v217\n" \
  "v_exp_f32_e32 v19, v19\n" \
  "v_exp_f32_e32 v20, v20\n" \
  "v_mfma_f32_32x32x16_bf16 v[0:15], v[128:131], v[112:115], 0\n" \
  "v_exp_f32_e32 v21, v21\n" \
  "v_exp_f32_e32 v22, v22\n" \
  "v_mfma_f32_32x32x8_bf16 v[0:15], v[154:155], v[120:121], v[0:15]\n" \
  "v_exp_f32_e32 v23, v23\n" \
  "v_exp_f32_e32 v24, v24\n" \
  "v_exp_f32_e32 v25, v25\n" \
  "v_exp_f32_e32 v26, v26\n" \
  "global_load_dwordx4 v[252:255], v219, s[44:45] offset:1024\n" \
  "v_mfma_f32_32x32x16_bf16 v[80:95], v[196:199], v[184:187], v[80:95]\n" \
  "global_load_dwordx4 v[196:199], v218, s[40:41] offset:2048\n" \
  "v_exp_f32_e32 v27, v27\n" \
  "v_exp_f32_e32 v28, v28\n" \
  "v_exp_f32_e32 v29, v29\n" \
  "v_exp_f32_e32 v30, v30\n" \
  "v_exp_f32_e64 v31, v31\n" \
  "v_smfmac_f32_32x32x32_bf16 v[48:63], v[140:143], v[16:23], v217\n" \
  "ds_read_b128 v[132:135], v214 offset:3840\n" \
  "ds_read_b128 v[136:139], v214 offset:4480\n" \
  "ds_read_b128 v[124:127], v212 offset:0\n" \
  "ds_read_b128 v[148:151], v213 offset:0\n" \
  "v_exp_f32_e32 v0, v0\n" \
  "v_exp_f32_e32 v1, v1\n" \
  "v_exp_f32_e64 v2, v2\n" \
  "v_smfmac_f32_32x32x32_bf16 v[48:63], v[144:147], v[24:31], v217\n" \
  "v_exp_f32_e32 v3, v3\n" \
  "v_exp_f32_e32 v4, v4\n" \
  "v_mfma_f32_32x32x16_bf16 v[16:31], v[128:131], v[116:119], 0\n" \
  "v_exp_f32_e32 v5, v5\n" \
  "v_exp_f32_e32 v6, v6\n" \
  "v_mfma_f32_32x32x8_bf16 v[16:31], v[154:155], v[122:123], v[16:31]\n" \
  "v_exp_f32_e32 v7, v7\n" \
  "v_exp_f32_e32 v8, v8\n" \
  "v_exp_f32_e32 v9, v9\n" \
  "v_exp_f32_e32 v10, v10\n" \
  "v_mfma_f32_32x32x16_bf16 v[96:111], v[200:203], v[184:187], v[96:111]\n" \
  "global_load_dwordx4 v[200:203], v218, s[40:41] offset:3072\n" \
  "global_load_dwordx4 v[184:187], v220, s[42:43] offset:-2048\n" \
  "v_exp_f32_e32 v11, v11\n" \
  "v_exp_f32_e32 v12, v12\n" \
  "v_mfma_f32_32x32x16_bf16 v[64:79], v[204:207], v[188:191], v[64:79]\n" \
  "global_load_dwordx4 v[204:207], v219, s[40:41] offset:0\n" \
  "v_exp_f32_e32 v13, v13\n" \
  "v_exp_f32_e32 v14, v14\n" \
  "v_exp_f32_e32 v15, v15\n" \
  "s_waitcnt vmcnt(7) lgkmcnt(0)\n" \
  "v_smfmac_f32_32x32x32_bf16 v[32:47], v[132:135], v[0:7], v217\n" \
  "v_mad_i32_i24 v214, v216, s47, v214\n" \
  "v_exp_f32_e32 v16, v16\n" \
  "v_exp_f32_e32 v17, v17\n" \
  "v_exp_f32_e64 v18, v18\n" \
  "v_smfmac_f32_32x32x32_bf16 v[32:47], v[136:139], v[8:15], v217\n" \
  "v_exp_f32_e32 v19, v19\n" \
  "v_exp_f32_e32 v20, v20\n" \
  "v_mfma_f32_32x32x16_bf16 v[0:15], v[124:127], v[112:115], 0\n" \
  "v_exp_f32_e32 v21, v21\n" \
  "v_exp_f32_e32 v22, v22\n" \
  "v_mfma_f32_32x32x8_bf16 v[0:15], v[148:149], v[120:121], v[0:15]\n" \
  "v_exp_f32_e32 v23, v23\n" \
  "v_exp_f32_e32 v24, v24\n" \
  "v_exp_f32_e32 v25, v25\n" \
  "v_exp_f32_e32 v26, v26\n" \
  "v_mfma_f32_32x32x16_bf16 v[80:95], v[208:211], v[188:191], v[80:95]\n" \
  "global_load_dwordx4 v[208:211], v219, s[40:41] offset:1024\n" \
  "global_load_dwordx4 v[188:191], v221, s[42:43] offset:-2048\n" \
  "s_add_u32 s40, s40, 10240\n" \
  "s_addc_u32 s41, s41, 0\n" \
  "v_exp_f32_e32 v27, v27\n" \
  "v_exp_f32_e32 v28, v28\n" \
  "v_exp_f32_e32 v29, v29\n" \
  "v_exp_f32_e32 v30, v30\n" \
  "v_exp_f32_e32 v31, v31\n" \
  "v_smfmac_f32_32x32x32_bf16 v[48:63], v[132:135], v[16:23], v217\n" \
  "ds_read_b128 v[140:143], v214 offset:0\n" \
  "ds_read_b128 v[144:147], v214 offset:640\n" \
  "ds_read_b128 v[128:131], v212 offset:1024\n" \
  "v_exp_f32_e32 v0, v0\n" \
  "v_exp_f32_e32 v1, v1\n" \
  "v_exp_f32_e64 v2, v2\n" \
  "v_smfmac_f32_32x32x32_bf16 v[48:63], v[136:139], v[24:31], v217\n" \
  "v_exp_f32_e32 v3, v3\n" \
  "v_exp_f32_e32 v4, v4\n" \
  "v_mfma_f32_32x32x16_bf16 v[16:31], v[124:127], v[116:119], 0\n" \
  "v_exp_f32_e32 v5, v5\n" \
  "v_exp_f32_e32 v6, v6\n" \
  "v_mfma_f32_32x32x8_bf16 v[16:31], v[148:149], v[122:123], v[16:31]\n" \
  "v_exp_f32_e32 v7, v7\n" \
  "v_exp_f32_e32 v8, v8\n" \
  "v_exp_f32_e32 v9, v9\n" \
  "v_exp_f32_e32 v10, v10\n" \
  "v_mfma_f32_32x32x16_bf16 v[64:79], v[164:167], v[156:159], v[64:79]\n" \
  "global_load_dwordx4 v[164:167], v218, s[40:41] offset:-4096\n" \
  "v_exp_f32_e32 v11, v11\n" \
  "v_exp_f32_e32 v12, v12\n" \
  "v_exp_f32_e32 v13, v13\n" \
  "v_exp_f32_e32 v14, v14\n" \
  "v_exp_f32_e32 v15, v15\n" \
  "s_waitcnt lgkmcnt(0)\n" \
  "v_smfmac_f32_32x32x32_bf16 v[32:47], v[140:143], v[0:7], v217\n" \
  "v_exp_f32_e32 v16, v16\n" \
  "v_exp_f32_e32 v17, v17\n" \
  "v_exp_f32_e64 v18, v18\n" \
  "v_smfmac_f32_32x32x32_bf16 v[32:47], v[144:147], v[8:15], v217\n" \
  "v_exp_f32_e32 v19, v19\n" \
  "v_exp_f32_e32 v20, v20\n" \
  "v_mfma_f32_32x32x16_bf16 v[0:15], v[128:131], v[112:115], 0\n" \
  "v_exp_f32_e32 v21, v21\n" \
  "v_exp_f32_e32 v22, v22\n" \
  "v_mfma_f32_32x32x8_bf16 v[0:15], v[150:151], v[120:121], v[0:15]\n" \
  "v_exp_f32_e32 v23, v23\n" \
  "v_exp_f32_e32 v24, v24\n" \
  "v_exp_f32_e32 v25, v25\n" \
  "v_exp_f32_e32 v26, v26\n" \
  "v_mfma_f32_32x32x16_bf16 v[80:95], v[168:171], v[156:159], v[80:95]\n" \
  "global_load_dwordx4 v[168:171], v218, s[40:41] offset:-3072\n" \
  "v_exp_f32_e32 v27, v27\n" \
  "v_exp_f32_e32 v28, v28\n" \
  "v_exp_f32_e32 v29, v29\n" \
  "v_exp_f32_e32 v30, v30\n" \
  "v_exp_f32_e64 v31, v31\n" \
  "v_smfmac_f32_32x32x32_bf16 v[48:63], v[140:143], v[16:23], v217\n" \
  "ds_read_b128 v[132:135], v214 offset:1280\n" \
  "ds_read_b128 v[136:139], v214 offset:1920\n" \
  "ds_read_b128 v[124:127], v212 offset:2048\n" \
  "ds_read_b128 v[152:155], v213 offset:512\n" \
  "v_exp_f32_e32 v0, v0\n" \
  "v_exp_f32_e32 v1, v1\n" \
  "v_exp_f32_e64 v2, v2\n" \
  "v_smfmac_f32_32x32x32_bf16 v[48:63], v[144:147], v[24:31], v217\n" \
  "v_exp_f32_e32 v3, v3\n" \
  "v_exp_f32_e32 v4, v4\n" \
  "v_mfma_f32_32x32x16_bf16 v[16:31], v[128:131], v[116:119], 0\n" \
  "v_exp_f32_e32 v5, v5\n" \
  "v_exp_f32_e32 v6, v6\n" \
  "v_mfma_f32_32x32x8_bf16 v[16:31], v[150:151], v[122:123], v[16:31]\n" \
  "v_exp_f32_e32 v7, v7\n" \
  "v_exp_f32_e32 v8, v8\n" \
  "v_exp_f32_e32 v9, v9\n" \
  "v_exp_f32_e32 v10, v10\n" \
  "v_mfma_f32_32x32x16_bf16 v[96:111], v[172:175], v[156:159], v[96:111]\n" \
  "global_load_dwordx4 v[172:175], v218, s[40:41] offset:-2048\n" \
  "global_load_dwordx4 v[156:159], v219, s[40:41] offset:2048\n" \
  "v_exp_f32_e32 v11, v11\n" \
  "v_exp_f32_e32 v12, v12\n" \
  "v_mfma_f32_32x32x16_bf16 v[64:79], v[176:179], v[160:163], v[64:79]\n" \
  "global_load_dwordx4 v[176:179], v218, s[40:41] offset:-1024\n" \
  "v_exp_f32_e32 v13, v13\n" \
  "v_exp_f32_e32 v14, v14\n" \
  "v_exp_f32_e64 v15, v15\n" \
  "s_setprio 2\n" \
  "s_waitcnt vmcnt(5) lgkmcnt(0)\n" \
  "v_smfmac_f32_32x32x32_bf16 v[32:47], v[132:135], v[0:7], v217\n" \
  "v_exp_f32_e32 v16, v16\n" \
  "v_exp_f32_e32 v17, v17\n" \
  "v_exp_f32_e64 v18, v18\n" \
  "v_smfmac_f32_32x32x32_bf16 v[32:47], v[136:139], v[8:15], v217\n" \
  "v_exp_f32_e32 v19, v19\n" \
  "v_exp_f32_e32 v20, v20\n" \
  "v_mfma_f32_32x32x16_bf16 v[0:15], v[124:127], v[112:115], 0\n" \
  "v_exp_f32_e32 v21, v21\n" \
  "v_exp_f32_e32 v22, v22\n" \
  "v_mfma_f32_32x32x8_bf16 v[0:15], v[152:153], v[120:121], v[0:15]\n" \
  "v_exp_f32_e32 v23, v23\n" \
  "v_exp_f32_e32 v24, v24\n" \
  "v_exp_f32_e32 v25, v25\n" \
  "v_exp_f32_e32 v26, v26\n" \
  "v_mfma_f32_32x32x16_bf16 v[80:95], v[180:183], v[160:163], v[80:95]\n" \
  "global_load_dwordx4 v[180:183], v218, s[40:41] offset:0\n" \
  "global_load_dwordx4 v[160:163], v219, s[40:41] offset:3072\n" \
  "v_exp_f32_e32 v27, v27\n" \
  "v_exp_f32_e32 v28, v28\n" \
  "v_exp_f32_e32 v29, v29\n" \
  "v_exp_f32_e32 v30, v30\n" \
  "v_exp_f32_e64 v31, v31\n" \
  "v_smfmac_f32_32x32x32_bf16 v[48:63], v[132:135], v[16:23], v217\n" \
  "ds_read_b128 v[140:143], v214 offset:2560\n" \
  "ds_read_b128 v[144:147], v214 offset:3200\n" \
  "ds_read_b128 v[128:131], v212 offset:3072\n" \
  "v_exp_f32_e32 v0, v0\n" \
  "v_exp_f32_e32 v1, v1\n" \
  "v_exp_f32_e64 v2, v2\n" \
  "v_smfmac_f32_32x32x32_bf16 v[48:63], v[136:139], v[24:31], v217\n" \
  "v_exp_f32_e32 v3, v3\n" \
  "v_exp_f32_e32 v4, v4\n" \
  "v_mfma_f32_32x32x16_bf16 v[16:31], v[124:127], v[116:119], 0\n" \
  "v_exp_f32_e32 v5, v5\n" \
  "v_exp_f32_e32 v6, v6\n" \
  "v_mfma_f32_32x32x8_bf16 v[16:31], v[152:153], v[122:123], v[16:31]\n" \
  "v_exp_f32_e32 v7, v7\n" \
  "v_exp_f32_e32 v8, v8\n" \
  "v_exp_f32_e32 v9, v9\n" \
  "v_exp_f32_e32 v10, v10\n" \
  "v_mfma_f32_32x32x16_bf16 v[64:79], v[192:195], v[184:187], v[64:79]\n" \
  "global_load_dwordx4 v[192:195], v218, s[40:41] offset:1024\n" \
  "v_exp_f32_e32 v11, v11\n" \
  "v_exp_f32_e32 v12, v12\n" \
  "v_exp_f32_e32 v13, v13\n" \
  "v_exp_f32_e32 v14, v14\n" \
  "v_exp_f32_e32 v15, v15\n" \
  "s_waitcnt lgkmcnt(0)\n" \
  "v_smfmac_f32_32x32x32_bf16 v[32:47], v[140:143], v[0:7], v217\n" \
  "v_exp_f32_e32 v16, v16\n" \
  "v_exp_f32_e32 v17, v17\n" \
  "v_exp_f32_e64 v18, v18\n" \
  "v_smfmac_f32_32x32x32_bf16 v[32:47], v[144:147], v[8:15], v217\n" \
  "v_exp_f32_e32 v19, v19\n" \
  "v_exp_f32_e32 v20, v20\n" \
  "v_mfma_f32_32x32x16_bf16 v[0:15], v[128:131], v[112:115], 0\n" \
  "v_exp_f32_e32 v21, v21\n" \
  "v_exp_f32_e32 v22, v22\n" \
  "v_mfma_f32_32x32x8_bf16 v[0:15], v[154:155], v[120:121], v[0:15]\n" \
  "v_exp_f32_e32 v23, v23\n" \
  "v_exp_f32_e32 v24, v24\n" \
  "v_exp_f32_e32 v25, v25\n" \
  "v_exp_f32_e32 v26, v26\n" \
  "v_mfma_f32_32x32x16_bf16 v[80:95], v[196:199], v[184:187], v[80:95]\n" \
  "global_load_dwordx4 v[196:199], v218, s[40:41] offset:2048\n" \
  "v_exp_f32_e32 v27, v27\n" \
  "v_exp_f32_e32 v28, v28\n" \
  "v_exp_f32_e32 v29, v29\n" \
  "v_exp_f32_e32 v30, v30\n" \
  "v_exp_f32_e64 v31, v31\n" \
  "v_smfmac_f32_32x32x32_bf16 v[48:63], v[140:143], v[16:23], v217\n" \
  "ds_read_b128 v[132:135], v214 offset:3840\n" \
  "ds_read_b128 v[136:139], v214 offset:4480\n" \
  "v_exp_f32_e32 v0, v0\n" \
  "v_exp_f32_e32 v1, v1\n" \
  "v_exp_f32_e64 v2, v2\n" \
  "v_smfmac_f32_32x32x32_bf16 v[48:63], v[144:147], v[24:31], v217\n" \
  "v_exp_f32_e32 v3, v3\n" \
  "v_exp_f32_e32 v4, v4\n" \
  "v_mfma_f32_32x32x16_bf16 v[16:31], v[128:131], v[116:119], 0\n" \
  "v_exp_f32_e32 v5, v5\n" \
  "v_exp_f32_e32 v6, v6\n" \
  "v_mfma_f32_32x32x8_bf16 v[16:31], v[154:155], v[122:123], v[16:31]\n" \
  "v_exp_f32_e32 v7, v7\n" \
  "v_exp_f32_e32 v8, v8\n" \
  "v_exp_f32_e32 v9, v9\n" \
  "v_exp_f32_e32 v10, v10\n" \
  "v_mfma_f32_32x32x16_bf16 v[96:111], v[200:203], v[184:187], v[96:111]\n" \
  "global_load_dwordx4 v[200:203], v218, s[40:41] offset:3072\n" \
  "global_load_dwordx4 v[184:187], v218, s[44:45] offset:-4096\n" \
  "v_exp_f32_e32 v11, v11\n" \
  "v_exp_f32_e32 v12, v12\n" \
  "v_mfma_f32_32x32x16_bf16 v[64:79], v[204:207], v[188:191], v[64:79]\n" \
  "global_load_dwordx4 v[204:207], v219, s[40:41] offset:0\n" \
  "v_exp_f32_e32 v13, v13\n" \
  "v_exp_f32_e32 v14, v14\n" \
  "v_exp_f32_e64 v15, v15\n" \
  "s_setprio 1\n" \
  "s_waitcnt lgkmcnt(0)\n" \
  "v_smfmac_f32_32x32x32_bf16 v[32:47], v[132:135], v[0:7], v217\n" \
  "v_exp_f32_e32 v16, v16\n" \
  "v_exp_f32_e32 v17, v17\n" \
  "v_exp_f32_e64 v18, v18\n" \
  "v_smfmac_f32_32x32x32_bf16 v[32:47], v[136:139], v[8:15], v217\n" \
  "v_exp_f32_e32 v19, v19\n" \
  "v_exp_f32_e32 v20, v20\n" \
  "v_exp_f32_e32 v21, v21\n" \
  "v_exp_f32_e32 v22, v22\n" \
  "v_exp_f32_e32 v23, v23\n" \
  "v_exp_f32_e32 v24, v24\n" \
  "v_exp_f32_e32 v25, v25\n" \
  "v_exp_f32_e32 v26, v26\n" \
  "v_mfma_f32_32x32x16_bf16 v[80:95], v[208:211], v[188:191], v[80:95]\n" \
  "global_load_dwordx4 v[208:211], v219, s[40:41] offset:1024\n" \
  "global_load_dwordx4 v[188:191], v218, s[44:45] offset:-3072\n" \
  "v_exp_f32_e32 v27, v27\n" \
  "v_exp_f32_e32 v28, v28\n" \
  "v_exp_f32_e32 v29, v29\n" \
  "v_exp_f32_e32 v30, v30\n" \
  "v_exp_f32_e64 v31, v31\n" \
  "v_smfmac_f32_32x32x32_bf16 v[48:63], v[132:135], v[16:23], v217\n" \
  "s_nop 0\n" \
  "v_smfmac_f32_32x32x32_bf16 v[48:63], v[136:139], v[24:31], v217\n" \
  "global_load_dwordx4 v[124:127], v219, s[44:45] offset:2048\n" \
  "global_load_dwordx4 v[128:131], v219, s[44:45] offset:3072\n" \
  "s_add_u32 s44, s44, 4096\n" \
  "s_addc_u32 s45, s45, 0\n" \
  "global_load_dwordx4 v[148:151], v219, s[44:45] offset:0\n" \
  "s_setprio 0\n" \
  "s_nop 15\n"
#define K2_CLOB "v0","v1","v2","v3","v4","v5","v6","v7","v8","v9","v10","v11","v12","v13","v14","v15","v16","v17","v18","v19","v20","v21","v22","v23","v24","v25","v26","v27","v28","v29","v30","v31","v132","v133","v134","v135","v136","v137","v138","v139","v140","v141","v142","v143","v144","v145","v146","v147","v152","v153","v154","v155"
#define NOQPF
#pragma clang diagnostic ignored "-Wint-to-pointer-cast"
#include <hip/hip_runtime.h>
#include <hip/hip_bf16.h>
#include <stdint.h>

typedef __attribute__((ext_vector_type(8))) __bf16 bf16x8;
typedef __attribute__((ext_vector_type(16))) __bf16 bf16x16;
typedef __attribute__((ext_vector_type(2))) __bf16 bf16x2;
typedef __attribute__((ext_vector_type(4))) short s16x4;
typedef __attribute__((ext_vector_type(16))) float f32x16;
typedef __attribute__((ext_vector_type(8))) float f32x8;
typedef __attribute__((ext_vector_type(4))) float f32x4;
typedef __attribute__((ext_vector_type(2))) float f32x2;
typedef __attribute__((ext_vector_type(4))) unsigned u32x4;
typedef __attribute__((ext_vector_type(2))) unsigned u32x2;
#define LAS __attribute__((address_space(3)))
#define GAS __attribute__((address_space(1)))
#define DEVI __device__ __forceinline__

constexpr int S_ = 2048, D_ = 640, F_ = 1728;
constexpr int KS2 = 152;
constexpr size_t WS_Q = 0;
constexpr size_t WS_KV = WS_Q + 32 * 16 * 5120;
constexpr size_t WS_A2 = WS_KV + 32 * 16 * 10240;
constexpr size_t WS_W2 = WS_A2 + 64 * KS2 * 1024;
constexpr size_t WS_P1 = WS_W2 + 20 * KS2 * 1024;
constexpr size_t WS_CNT = WS_P1 + 2048 * 640 * 4;

constexpr size_t WS_DBG = WS_CNT + 65536;
#define STAMP(k, i)
#define CSTAMP(k, i)

DEVI unsigned pk2(float a, float b) {
  f32x2 v = {a, b};
  return __builtin_bit_cast(unsigned, __builtin_convertvector(v, bf16x2));
}
DEVI void glds16(const void* sbase, unsigned voff, unsigned ldsaddr) {
  asm volatile("s_mov_b32 m0, %2\n\ts_nop 0\n\tglobal_load_lds_dwordx4 %0, %1" ::"v"(voff), "s"(sbase), "s"(ldsaddr) : "memory");
}
DEVI void glds4(const void* sbase, unsigned voff, unsigned ldsaddr) {
  asm volatile("s_mov_b32 m0, %2\n\ts_nop 0\n\tglobal_load_lds_dword %0, %1" ::"v"(voff), "s"(sbase), "s"(ldsaddr) : "memory");
}
DEVI bf16x8 trfrag(unsigned a0, unsigned a1) {
  s16x4 lo = __builtin_amdgcn_ds_read_tr16_b64_v4i16((LAS s16x4*)a0);
  s16x4 hi = __builtin_amdgcn_ds_read_tr16_b64_v4i16((LAS s16x4*)a1);
  return __builtin_bit_cast(bf16x8, __builtin_shufflevector(lo, hi, 0, 1, 2, 3, 4, 5, 6, 7));
}
DEVI unsigned sreg(unsigned v) { asm("" : "+s"(v)); return v; }
DEVI float sfma(float a, float b, float c) { float r; asm("v_fma_f32 %0, %1, %2, %3" : "=v"(r) : "v"(a), "v"(b), "v"(c)); return r; }
#define MFMA(a, b, c) __builtin_amdgcn_mfma_f32_32x32x16_bf16(a, b, c, 0, 0, 0)
#define WAITBAR(n) asm volatile("s_waitcnt vmcnt(" #n ") lgkmcnt(0)\n\ts_barrier" ::: "memory")

constexpr int K1_LDS = 163840;
typedef __attribute__((ext_vector_type(16))) unsigned u32x16;
typedef __attribute__((ext_vector_type(8))) unsigned u32x8;
#define K1_THR 256

DEVI unsigned k1_qk_tab(int q, int rb, int m, int tok) {
  const bool isK = q >= 5;
  const unsigned c4 = (q - (isK ? 5 : 0)) * 32 + tok, head = (c4 * 205) >> 10, dg = c4 - head * 5;
  return (unsigned)(isK ? WS_KV : WS_Q) + (head * 16 + rb) * (isK ? 10240 : 5120) +
         (dg < 4 ? m * 1024 + (dg >> 1) * 512 + (dg & 1) * 8 : 4096 + (m >> 1) * 512 + (m & 1) * 8);
}
DEVI void stw(char* b, unsigned o, u32x4 v) { asm volatile("global_store_dwordx4 %0, %1, %2 sc1\n\ts_nop 1" ::"v"(o), "v"(v), "s"(b) : "memory"); }
DEVI void stw2(char* b, unsigned o, u32x2 v) { asm volatile("global_store_dwordx2 %0, %1, %2" ::"v"(o), "v"(v), "s"(b) : "memory"); }
DEVI void swp(u32x2& a, u32x2& b) {
#pragma unroll
  for (int e = 0; e < 2; e++) { const auto s = __builtin_amdgcn_permlane32_swap(a[e], b[e], false, false); a[e] = s[0]; b[e] = s[1]; }
}
template <int I0>
DEVI void k1_epi_qk_ph(u32x2 (&o)[16], char* ws, unsigned tab, int lane) {
  const bool h = lane >> 5;
  const int hh = lane >> 5, t16 = (lane & 31) * 16;
#define QK_AT(c, hs) ws, (unsigned)(__builtin_amdgcn_ds_bpermute(hh * (4 * (hs)) + 4 * (c), tab) + t16)
#pragma unroll
  for (int i = 0; i < 16; i++) {
    const int p = (i + 5 - I0) % 5;
    if (p == 0 && i + 1 < 16) {
      swp(o[i], o[i + 1]);
      stw(QK_AT(2 * i, 2), u32x4{o[i][0], o[i][1], o[i + 1][0], o[i + 1][1]});
    } else if (p == 2 && i + 2 < 16) {
      swp(o[i], o[i + 2]);
      const u32x2 a = h ? o[i + 1] : o[i + 2], b = h ? o[i] : o[i + 1];
      stw(QK_AT(2 * i + 1, 2), u32x4{a[0], a[1], b[0], b[1]});
      stw2(QK_AT(2 * i, 5), h ? o[i + 2] : o[i]);
    } else if (!((p == 1 && i >= 1) || (p == 3 && i >= 1 && i + 1 < 16) || (p == 4 && i >= 2))) {
      stw2(QK_AT(2 * i, 1), o[i]);
    }
  }
}
DEVI void k1_epi_qk(f32x16 (&acc)[4], char* ws, int q, unsigned tab, int lane) {
  const float sc = q >= 5 ? 1.0f : 0.22360679774997896f * 1.4426950408889634f;
  u32x2 o[16];
#pragma unroll
  for (int j = 0; j < 4; j++)
#pragma unroll
    for (int g = 0; g < 4; g++)
      o[8 * (j & 1) + 4 * (j >> 1) + g] = u32x2{pk2(acc[j][4 * g] * sc, acc[j][4 * g + 1] * sc), pk2(acc[j][4 * g + 2] * sc, acc[j][4 * g + 3] * sc)};
  const int ph = q >= 5 ? q - 5 : q;
  if (ph == 0) k1_epi_qk_ph<0>(o, ws, tab, lane);
  else if (ph == 1) k1_epi_qk_ph<4>(o, ws, tab, lane);
  else if (ph == 2) k1_epi_qk_ph<3>(o, ws, tab, lane);
  else if (ph == 3) k1_epi_qk_ph<2>(o, ws, tab, lane);
  else k1_epi_qk_ph<1>(o, ws, tab, lane);
}
DEVI void k1_epi_v(f32x16 (&acc)[4], char* ws, int q, int rb, int m, int lane) {
  const int h = lane >> 5;
#pragma unroll
  for (int j = 0; j < 4; j++) {
    const unsigned col = (q - 10) * 128 + 64 * (j & 1) + 32 * (j >> 1) + (lane & 31);
    const unsigned head = (col * 3277) >> 16, dim = col - head * 20;
    const unsigned dst = (unsigned)(WS_KV + (head * 16 + rb) * 10240 + 5120 + 2 * m * 640 + dim * 16 + h * 320);
#pragma unroll
    for (int g = 0; g < 4; g += 2) {
      u32x2 a = {pk2(acc[j][4 * g], acc[j][4 * g + 1]), pk2(acc[j][4 * g + 2], acc[j][4 * g + 3])};
      u32x2 b = {pk2(acc[j][4 * g + 4], acc[j][4 * g + 5]), pk2(acc[j][4 * g + 6], acc[j][4 * g + 7])};
      swp(a, b);
      stw(ws, dst + (g >> 1) * 640, u32x4{a[0], a[1], b[0], b[1]});
    }
  }
}

DEVI void k1_relayout(const char* wd, const char* wo, char* ws, int id, int w, int lane) {
  for (int i = 0; i < 5; i++) {
    const int ks = id + 32 * i;
    if (ks >= KS2 || (ks >= 108 && ks < 112)) continue;
    for (int p = w; p < 20; p += 4) {
      const unsigned o = p * 1024 + lane * 16, sg = o / 1280, col = o - sg * 1280;
      unsigned row; const char* src;
      if (ks < 108) { src = wd; row = ks * 16 + sg; }
      else {
        src = wo; const int g = (ks - 112) / 5, e = (ks - 112) - g * 5;
        row = e < 4 ? 20 * (4 * g + e) + sg : 20 * (4 * g + 2 * ((sg >> 2) & 1) + (sg >> 3)) + 16 + (sg & 3);
      }
      glds16(src, row * 1280 + col, i * 20480 + p * 1024);
    }
  }
  WAITBAR(0);
  const unsigned la = (4 * (lane >> 5) + ((lane >> 2) & 3)) * 1280 + ((lane >> 4) & 1) * 32 + (lane & 3) * 8;
  for (int i = 0; i < 5; i++) {
    const int ks = id + 32 * i;
    if (ks >= KS2) continue;
    for (int nt = w; nt < 20; nt += 4) {
      const unsigned a = la + i * 20480 + nt * 64;
      u32x4 v = __builtin_bit_cast(u32x4, trfrag(a, a + 8 * 1280));
      if (ks >= 108 && ks < 112) v = u32x4{0, 0, 0, 0};
      const int ng = nt / 5, j = nt - ng * 5;
      const int kq = ks < 112 ? ks / 28 : (ks - 112) / 10, r = ks < 112 ? ks - kq * 28 : ks - 112 - kq * 10 + 28;
      const int wh = j < 4 ? j >> 1 : r & 1, slot = j < 4 ? ((r & 1) == wh ? 0 : 3) + (j & 1) : 2;
      asm volatile("global_store_dwordx4 %0, %1, off sc1\n\ts_nop 1" ::"v"(ws + WS_W2 + ((size_t)((ng * 8 + wh * 4 + kq) * 19 + (r >> 1)) * 5 + slot) * 1024 + lane * 16), "v"(v) : "memory");
    }
  }
}

__global__ __launch_bounds__(256, 1) void k1(const char* __restrict__ x, const char* __restrict__ wqkv, const char* __restrict__ wo,
                                             const char* __restrict__ wg, const char* __restrict__ wu, const char* __restrict__ wd,
                                             char* __restrict__ ws, const char* out) {
  extern __shared__ __attribute__((aligned(256))) char smem[];
  const int tid = threadIdx.x, lane = tid & 63, w = __builtin_amdgcn_readfirstlane(tid >> 6);
  const int xcd = blockIdx.x & 7, jb = blockIdx.x >> 3;
  STAMP(0, 0)
  CSTAMP(0, 13)
  CSTAMP(3, 0)
  uint64_t pc0; unsigned pclen;
  asm volatile("s_getpc_b64 %0\n.Lcode_first:\n\ts_mov_b32 %1, .Lcode_last-.Lcode_first" : "=s"(pc0), "=s"(pclen));
  if (jb >= 28) {
    for (unsigned o = tid * 128; o < pclen; o += K1_THR * 128) (void)*(volatile const GAS unsigned*)(pc0 + o);
    if (blockIdx.x == 255 && tid < 256) ((unsigned*)(ws + WS_CNT))[tid * 32] = 0;
    for (int i = 0; i < 2; i++)
      stw(ws + WS_A2 + ((size_t)((xcd * 4 + jb - 28) * 2 + i) * KS2 + 108) * 1024, tid * 16, u32x4{0, 0, 0, 0});
    k1_relayout(wd, wo, ws, xcd * 4 + jb - 28, w, lane);
    STAMP(0, 15)
    return;
  }
  const int rb = 4 * (xcd >> 1) + (jb & 3), cb = 7 * (xcd & 1) + (jb >> 2);
#define LOHI(p) sreg((unsigned)(p)), sreg((unsigned)((p) >> 32))
  int w2 = w, lane2 = lane;
  constexpr unsigned RF = F_ * 2, RQ = 3840;
  const uint64_t pg = (uint64_t)(wg + cb * 128), pu = (uint64_t)(wu + cb * 128);
  const u32x8 s0 = {LOHI(pg), LOHI(pu - 1024), LOHI(pg + 8 * RF - 2048), LOHI(pu + 8 * RF - 3072)};
  const char* xb = x + (size_t)(rb * 128 + w * 32) * 1280;
  const unsigned krow = 16 * w + (lane >> 3), ch = ((lane & 7) ^ (((lane >> 4) & 1) << 2)) * 16;
  unsigned vo0 = krow * RF + ch;
  const unsigned vx0 = (lane >> 3) * 1280 + ((lane & 7) ^ (lane >> 4)) * 16, vx1 = (vx0 ^ 64) + 10240 - 1024;
  const u32x4 vx = {vx0, vx1, vx0 + 20480 - 2048, vx1 + 20480 - 2048};
  u32x16 xd0, xd1;
  asm volatile(K1_ASM0
               : "+{v228}"(vo0), "+{s[68:69]}"(xb), "+s"(w2), "+v"(lane2), "=&{v[192:207]}"(xd0), "=&{v[208:223]}"(xd1)
               : "{v[96:99]}"(vx), "{s[40:47]}"(s0), "{s64}"(64u * RF), "{s67}"(w * 4096), "{v235}"((lane & 31) * 1280 + (lane >> 5) * 16)
               : "scc", "memory");
  const int m = __builtin_amdgcn_readfirstlane(w2), h = lane2 >> 5, row = lane2 & 31;
  const unsigned voffF = vo0 - K1_NW0 * 64u * RF, voffQ = (16 * m + (lane2 >> 3)) * RQ + ((lane2 & 7) ^ (((lane2 >> 4) & 1) << 2)) * 16;
  const int q2 = cb + 1;
  const bool sp = cb == 13;
  const uint64_t p10 = sp ? (uint64_t)wqkv : pg + 14 * 128, p11 = sp ? (uint64_t)wqkv + 128 : pu + 14 * 128;
  const unsigned r1 = sp ? RQ : RF;
  const uint64_t p20 = (uint64_t)(wqkv + q2 * 256);
  const u32x16 s12 = {LOHI(p10), LOHI(p11 - 1024), LOHI(p10 + 8 * r1 - 2048), LOHI(p11 + 8 * r1 - 3072),
                      LOHI(p20), LOHI(p20 + 128 - 1024), LOHI(p20 + 8 * RQ - 2048), LOHI(p20 + 128 + 8 * RQ - 3072)};
  const uint64_t o0 = (uint64_t)(ws + WS_A2 + ((size_t)(rb * 4 + m) * KS2 + 4 * cb) * 1024);
  const uint64_t o1 = sp ? (uint64_t)(ws + WS_Q + rb * 5120) : o0 + 56 * 1024;
  const float c0 = -2.0f * 1.4426950408889634f * 0.7978845608028654f, c1 = c0 * 0.044715f;
  const u32x4 so = {LOHI(o0), LOHI(o1)};
  const u32x4 sc = {0x3f803f80u, __builtin_bit_cast(unsigned, c0), 0xffff0000u,
                    __builtin_bit_cast(unsigned, 0.22360679774997896f * 1.4426950408889634f)};
  const unsigned ax0 = m * 4096 + row * 128 + ((h ^ ((row >> 1) & 7)) * 16);
  const u32x16 pv = {vx[0], vx[1], vx[2], vx[3], ax0, ax0 ^ 32, ax0 ^ 64, ax0 ^ 96, 0, 0, 0, 0, 0, 0, 0, 0};
  const unsigned q_ = (lane2 >> 2) & 3;
  const unsigned aK = h * 2048 + q_ * 128 + ((lane2 >> 4) & 1) * 32 + (lane2 & 3) * 8 + ((q_ >> 1) << 6);
  const u32x4 ar = {aK, aK ^ 64, aK + 65536, (aK ^ 64) + 65536};
  u32x4 vo = {vo0, sp ? voffQ : voffF, voffQ, (unsigned)lane2 * 16};
  unsigned tab = k1_qk_tab(sreg(q2), sreg(rb), sreg(m), row);
  f32x16 acc[4];
  acc[0] = __builtin_bit_cast(f32x16, pv);
  uint64_t t0, t1, t2, u0, u1, u2;
  asm volatile(K1_ASM
               : "+{v[96:111]}"(acc[0]), "=&{v[112:127]}"(acc[1]), "=&{v[128:143]}"(acc[2]), "=&{v[144:159]}"(acc[3]),
                 "+{v[228:231]}"(vo), "+{s[68:69]}"(xb), "+{v[192:207]}"(xd0), "+{v[208:223]}"(xd1), "+{v244}"(tab)
               : "{v[224:227]}"(ar), "{v232}"(c1), "{s[40:47]}"(s0), "{s[48:63]}"(s12), "{s64}"(64u * RF), "{s65}"(sp ? 64u * RQ : 64u * RF), "{s66}"(64u * RQ), "{s67}"(w * 4096),
                 "{s70}"(cb < 9 ? 0 : sp ? 2 : 1), "{s71}"(m * 1024), "{s[72:75]}"(so), "{s[76:79]}"(sc), "{s80}"(4096 + (m >> 1) * 512 + (m & 1) * 8)
               : K1_CLOB, "scc", "memory");
  STAMP(0, 7)
  {
    const int ln = vo[3] >> 4;
    if (q2 < 10) k1_epi_qk(acc, ws, q2, tab, ln);
    else k1_epi_v(acc, ws, q2, rb, m, ln);
  }
  STAMP(0, 8)
  STAMP(0, 9)
  CSTAMP(0, 14)
}
#define NOCODEPF2
#define ZROWS
#define POLLSLP 1
constexpr int K2_ONES = 122880, K2_MX = 129024, K2_LDS = 163840;
typedef __attribute__((ext_vector_type(4))) __bf16 bf16x4;
#define LO8(S) __builtin_bit_cast(bf16x16, __builtin_shufflevector(S, S, 0, 1, 2, 3, 4, 5, 6, 7))
#define HI8(S) __builtin_bit_cast(bf16x16, __builtin_shufflevector(S, S, 8, 9, 10, 11, 12, 13, 14, 15))
#define SMF(a, b, c) __builtin_amdgcn_smfmac_f32_32x32x32_bf16(a, b, c, sidx, 0, 0)
#define MFMA8(a, b, c) __builtin_amdgcn_mfma_f32_32x32x8bf16_1k(a, b, c, 0, 0, 0)
#define SB() __builtin_amdgcn_sched_barrier(0)
#define EXP4(S, b) { for (int i_ = b; i_ < b + 4; i_++) S[i_] = __builtin_amdgcn_exp2f(S[i_]); SB(); }
DEVI s16x4 lo4(u32x4 v) { return __builtin_bit_cast(s16x4, u32x2{v[0], v[1]}); }
DEVI s16x4 hi4(u32x4 v) { return __builtin_bit_cast(s16x4, u32x2{v[2], v[3]}); }
DEVI bf16x8 ldsf(unsigned a) { return *(LAS bf16x8*)a; }
DEVI bf16x8 gldf(const char* p) { return *(const bf16x8*)p; }

#define GMODE 3
#define XM8 MFMA8
struct G2 { bf16x8 aa, ab, b0a, b1a, b4a, b0b, b1b; };

DEVI void g2_reduce(f32x16 (&ga)[3], int w, int lane, int tid, int mt, int ng, u32x4 xv, u32x2 xw, char* out) {
  const unsigned wa = w * 13824 + (lane & 31) * 144 + (lane >> 5) * 16;
#pragma unroll
  for (int j = 0; j < 3; j++)
#pragma unroll
    for (int g = 0; g < 4; g++)
      *(LAS f32x4*)(wa + j * 4608 + g * 32) = f32x4{ga[j][4 * g], ga[j][4 * g + 1], ga[j][4 * g + 2], ga[j][4 * g + 3]};
  asm volatile("s_waitcnt lgkmcnt(0)\n\ts_barrier" ::: "memory");
  {
    const int tok = tid >> 4, n8 = tid & 15;
    const unsigned ra = (n8 >> 3) * 4 * 13824 + ((n8 >> 2) & 1) * 4608 + tok * 144 + (n8 & 3) * 32;
    f32x4 u = *(LAS f32x4*)ra, v = *(LAS f32x4*)(ra + 16);
#pragma unroll
    for (int k = 1; k < 4; k++) { u += *(LAS f32x4*)(ra + k * 13824); v += *(LAS f32x4*)(ra + k * 13824 + 16); }
    const size_t e = (size_t)(mt * 32 + tok) * 640 + ng * 160 + n8 * 8;
    u32x4 o;
#pragma unroll
    for (int i = 0; i < 2; i++) {
      o[i] = pk2(u[2 * i] + __builtin_bit_cast(float, xv[i] << 16), u[2 * i + 1] + __builtin_bit_cast(float, xv[i] & 0xffff0000u));
      o[2 + i] = pk2(v[2 * i] + __builtin_bit_cast(float, xv[2 + i] << 16), v[2 * i + 1] + __builtin_bit_cast(float, xv[2 + i] & 0xffff0000u));
    }
    asm volatile("global_store_dwordx4 %0, %1, off sc1\n\ts_nop 1" ::"v"(out + e * 2), "v"(o) : "memory");
  }
  if (tid < 256) {
    const int tok = tid >> 3, n4 = tid & 7;
    const unsigned ra = 2 * 4608 + tok * 144 + n4 * 16;
    f32x4 u = *(LAS f32x4*)ra;
#pragma unroll
    for (int k = 1; k < 8; k++) u += *(LAS f32x4*)(ra + k * 13824);
    const size_t e = (size_t)(mt * 32 + tok) * 640 + ng * 160 + 128 + n4 * 4;
    u32x2 o;
#pragma unroll
    for (int i = 0; i < 2; i++)
      o[i] = pk2(u[2 * i] + __builtin_bit_cast(float, xw[i] << 16), u[2 * i + 1] + __builtin_bit_cast(float, xw[i] & 0xffff0000u));
    asm volatile("global_store_dwordx2 %0, %1, off sc1" ::"v"(out + e * 2), "v"(o) : "memory");
  }
}
DEVI void st16(char* sbase, unsigned voff, u32x4 v) { asm volatile("global_store_dwordx4 %0, %1, %2 sc1\n\ts_nop 1" ::"v"(voff), "v"(v), "s"(sbase) : "memory"); }
DEVI void st8(char* sbase, unsigned voff, u32x2 v) { asm volatile("global_store_dwordx2 %0, %1, %2 sc1" ::"v"(voff), "v"(v), "s"(sbase) : "memory"); }

DEVI float max16(const f32x16& s, float m) {
#pragma unroll
  for (int i = 0; i < 16; i += 2) m = fmaxf(fmaxf(s[i], s[i + 1]), m);
  return m;
}

__global__ __launch_bounds__(512, 2) void k2(const char* x, char* __restrict__ ws, char* out) {
  extern __shared__ __attribute__((aligned(256))) char smem[];
  uint64_t wse;
  asm volatile("s_load_dwordx2 %0, %1, 0x8" : "=&s"(wse) : "s"(__builtin_amdgcn_kernarg_segment_ptr()));
  SB();
  const int tid = threadIdx.x, lane = tid & 63, w = __builtin_amdgcn_readfirstlane(tid >> 6);
  const int xcd = blockIdx.x & 7, jb = blockIdx.x >> 3;
  const int head = 4 * xcd + (jb & 3), qblk = jb >> 2, mt = 8 * (jb >> 2) + xcd, ng = jb & 3;
  const int h = lane >> 5, row = lane & 31;
  const unsigned l16 = lane * 16;
  STAMP(1, 0)
  const int kq = w & 3, wh = w >> 2;
  const int qt = qblk * 8 + 2 * kq + wh;
  bf16x8 qma, qmb;
  u32x4 qr4;
  {
    const unsigned qo = (unsigned)WS_Q + (head * 16 + (qt >> 2)) * 5120 + (kq & 1) * 2048;
    unsigned vk = (unsigned)WS_KV + head * 163840 + tid * 16;
    asm volatile("s_mov_b32 m0, %8\n\ts_waitcnt lgkmcnt(0)\n\tglobal_load_lds_dwordx4 %3, %7\n\t"
                 "s_addk_i32 m0, 0x2000\n\tv_add_u32 %3, %9, %3\n\tglobal_load_lds_dwordx4 %3, %7\n\t"
                 "global_load_dwordx4 %0, %4, %7\n\tglobal_load_dwordx4 %1, %5, %7\n\tglobal_load_dwordx4 %2, %6, %7\n\ts_barrier\n"
                 ".Lk2pre:\n\ts_addk_i32 m0, 0x2000\n\tv_add_u32 %3, %9, %3\n\tglobal_load_lds_dwordx4 %3, %7\n\t"
                 "s_cmp_lt_u32 m0, " K2_PRE_END "\n\ts_cbranch_scc1 .Lk2pre"
                 : "=&v"(qma), "=&v"(qmb), "=&v"(qr4), "+v"(vk)
                 : "v"(qo + wh * 1024 + l16), "v"(qo + 1024 - wh * 1024 + l16), "v"(qo + 4096 - (kq & 1) * 1536 + row * 16), "s"(wse), "s"(w * 1024), "s"(8192)
                 : "scc", "memory");
  }
  const char* kv = ws + WS_KV + (size_t)head * 163840;
  if (tid < 384) *(LAS u32x4*)(K2_ONES + tid * 16) = u32x4{0x3f803f80u, 0x3f803f80u, 0x3f803f80u, 0x3f803f80u};
  int sidx = 0xdddd;
  unsigned ka = l16 + wh * 10240;
  unsigned ra = (h == 0 ? 4096 + wh * 10240 : K2_ONES) + row * 16;
  unsigned va = (row < 20 ? 5120 + wh * 10240 : (row & 3) == 0 && row < 28 ? K2_ONES : 196608) + h * 320 + row * 16;
    const char* pa = ws + WS_A2 + ((size_t)mt * KS2 + kq * 28) * 1024 + 4096;
    const char* pw = ws + WS_W2 + (size_t)(ng * 8 + w) * 19 * 5120;
    const char* pkv = kv + w * 1024 + (K2_PRE - 1) * 8192;
  f32x16 oa = {}, ob = {};
  u32x4 cv = {l16 + 4096, l16 + 8192, l16 + wh * 1024, l16 + 1024 - wh * 1024};
  asm volatile("s_waitcnt vmcnt(" K2_PRE_W ") lgkmcnt(0)\n\ts_barrier" : "+v"(qma), "+v"(qmb), "+v"(qr4), "+v"(sidx), "+s"(x), "+s"(out), "+v"(ka), "+v"(ra), "+v"(va), "+v"(oa), "+v"(ob), "+v"(cv)
               : "s"(pa), "s"(pw), "s"(pkv), "s"(ws) : "memory");
  u32x2 qra = wh ? u32x2{qr4[2], qr4[3]} : u32x2{qr4[0], qr4[1]}, qrb = wh ? u32x2{qr4[0], qr4[1]} : u32x2{qr4[2], qr4[3]};
  if (h) { qra = u32x2{0, 0}; qrb = u32x2{0, 0}; }
  const f32x16 z = {};
  u32x4 kr = *(LAS u32x4*)ra;
  bf16x8 kc = ldsf(ka);
  {
    const s16x4 qa = __builtin_bit_cast(s16x4, qra);
    const u32x4 r1 = *(LAS u32x4*)(ra + 512);
    f32x16 s0 = MFMA(kc, qma, z), s1 = MFMA(ldsf(ka + 1024), qma, z), s2 = MFMA(ldsf(ka + 2048), qma, z), s3 = MFMA(ldsf(ka + 3072), qma, z);
    s0 = MFMA8(lo4(kr), qa, s0); s1 = MFMA8(hi4(kr), qa, s1); s2 = MFMA8(lo4(r1), qa, s2); s3 = MFMA8(hi4(r1), qa, s3);
    SB();
    float mx = max16(s3, max16(s2, max16(s1, max16(s0, -3.0e38f))));
    {
      const unsigned mu = __builtin_bit_cast(unsigned, mx);
      const auto sw = __builtin_amdgcn_permlane32_swap(mu, mu, false, false);
      mx = fmaxf(__builtin_bit_cast(float, (unsigned)sw[0]), __builtin_bit_cast(float, (unsigned)sw[1]));
    }
    const unsigned nm = pk2(-(mx + 96.0f), 0.0f);
    *(LAS unsigned*)(K2_MX + w * 128 + row * 4) = nm;
    WAITBAR(0);
    const unsigned nmb = *(LAS unsigned*)(K2_MX + (w ^ 4) * 128 + row * 4);
    if (h) { qra[0] = nm; qrb[0] = nmb; }
  }
  STAMP(1, 2)
  CSTAMP(2, 0)
  f32x16 ga[3];
  u32x16 wf0, wf1, wf2, wf4, wf5;
  u32x8 wf3;
  u32x4 wf6;
  unsigned l16b = l16;
  {
    u32x4 qr2 = {qra[0], qra[1], qrb[0], qrb[1]};
    asm volatile(K2_ASM
                 : "+{v[32:47]}"(oa), "+{v[48:63]}"(ob), "={v[64:79]}"(ga[0]), "={v[80:95]}"(ga[1]), "={v[96:111]}"(ga[2]),
                   "+{v[124:127]}"(kc), "+{v[148:151]}"(kr), "+{v212}"(ka), "+{v213}"(ra), "+{v214}"(va),
                   "+{s[40:41]}"(pw), "+{s[42:43]}"(pa), "+{s[44:45]}"(pkv), "+{v222}"(l16b), "+{v217}"(sidx),
                   "=&{v[156:171]}"(wf0), "=&{v[172:187]}"(wf1), "=&{v[188:203]}"(wf2), "=&{v[204:211]}"(wf3), "=&{v[224:239]}"(wf4),
                   "=&{v[240:255]}"(wf5), "=&{v[128:131]}"(wf6), "+{v[112:115]}"(qma), "+{v[120:123]}"(qr2)
                 : "{v[116:119]}"(qmb), "{v215}"(h == 0 ? 1 : 0), "{v216}"(row < 20 ? 1 : 0),
                   "{v[218:221]}"(cv),
                   "{s[46:47]}"(u32x2{(unsigned)w * 1024, 20480u}), "{s48}"(-102400)
                 : K2_CLOB, "scc", "memory");
    qra = u32x2{qr2[0], qr2[1]};
  }
  {
  const unsigned l16 = l16b;
  const int lane = l16 >> 4, tid = w * 64 + lane, h = lane >> 5, row = lane & 31;
  STAMP(1, 8)
  CSTAMP(2, 1)
  {
    const unsigned xa = 131072 + w * 4096 + l16;
#pragma unroll
    for (int i = 0; i < 4; i++) *(LAS f32x4*)(xa + i * 1024) = f32x4{ob[4 * i], ob[4 * i + 1], ob[4 * i + 2], ob[4 * i + 3]};
    asm volatile("s_waitcnt lgkmcnt(0)\n\ts_barrier" ::: "memory");
#pragma unroll
    for (int i = 0; i < 4; i++) {
      const f32x4 v = *(LAS f32x4*)(131072 + (w ^ 4) * 4096 + l16 + i * 1024);
#pragma unroll
      for (int e = 0; e < 4; e++) oa[4 * i + e] += v[e];
    }
  }
  if (h) qra[0] = 0;
  const s16x4 qa = __builtin_bit_cast(s16x4, qra);
  f32x16 o = oa, sa;
  const bf16x8 qm = qma;
  float l = h ? o[8] : o[12];
  {
    float chk = 0.0f;
#pragma unroll
    for (int i = 0; i < 12; i++) chk = __builtin_fmaf(o[i], 0.0f, chk);
    const bool bad = !(l < 0x1p124f) || !(l > 0x1p-100f) || !(chk == 0.0f);
    if (__builtin_expect(__builtin_amdgcn_ballot_w64(bad) != 0, 0)) {
      STAMP(1, 12)
      asm volatile("s_waitcnt vmcnt(0)" ::: "memory");
      float mx = -3.0e38f;
      o = z;
      const bf16x8 ones = __builtin_bit_cast(bf16x8, u32x4{0x3f803f80u, 0x3f803f80u, 0x3f803f80u, 0x3f803f80u});
#pragma unroll 1
      for (int t = 0; t < 64; t += 2) {
        const char* cp = kv + (t >> 2) * 10240;
        const u32x4 r = *(const u32x4*)(cp + 4096 + ((t & 3) >> 1) * 512 + row * 16);
        const bf16x8 k0 = gldf(cp + (t & 3) * 1024 + l16), k1 = gldf(cp + (t & 3) * 1024 + 1024 + l16);
        const char* vp = cp + 5120 + (t & 3) * 1280 + h * 320 + min(row, 19) * 16;
        bf16x8 a[4] = {gldf(vp), gldf(vp + 640), gldf(vp + 1280), gldf(vp + 1920)};
        SB();
#pragma unroll
        for (int u = 0; u < 2; u++) {
          sa = MFMA8(u ? hi4(r) : lo4(r), qa, MFMA(u ? k1 : k0, qm, z));
          float m2 = max16(sa, mx);
          m2 = fmaxf(m2, __shfl_xor(m2, 32));
          const float f = __builtin_amdgcn_exp2f(mx - m2);
          mx = m2;
#pragma unroll
          for (int i = 0; i < 16; i++) { sa[i] = __builtin_amdgcn_exp2f(sa[i] - mx); o[i] *= f; }
          o = SMF(row >= 20 ? ones : a[2 * u], LO8(sa), o);
          o = SMF(row >= 20 ? ones : a[2 * u + 1], HI8(sa), o);
          SB();
        }
      }
      l = h ? o[8] : o[12];
    }
  }
  {
    const float ri = __builtin_amdgcn_rcpf(l);
    char* op = ws + WS_A2 + ((size_t)qt * KS2 + 112 + 5 * (head >> 2)) * 1024;
    st16(op + (head & 3) * 1024, l16,
         u32x4{pk2(o[0] * ri, o[1] * ri), pk2(o[2] * ri, o[3] * ri), pk2(o[4] * ri, o[5] * ri), pk2(o[6] * ri, o[7] * ri)});
    if (h == 0) st8(op + 4096 + ((head & 3) >> 1) * 512 + (head & 1) * 8, row * 16,
                    u32x2{pk2(o[8] * ri, o[9] * ri), pk2(o[10] * ri, o[11] * ri)});
  }
  WAITBAR(0);
  STAMP(1, 9)
  unsigned* cnt = (unsigned*)(ws + WS_CNT);
  if (tid == 0) __hip_atomic_fetch_add(cnt + qblk * 64, 1u, __ATOMIC_RELAXED, __HIP_MEMORY_SCOPE_AGENT);
  SB();
  const u32x4 xv = *(const GAS u32x4*)((const GAS char*)x + ((size_t)(mt * 32 + (tid >> 4)) * 640 + ng * 160 + (tid & 15) * 8) * 2);
  const u32x2 xw = *(const GAS u32x2*)((const GAS char*)x + ((size_t)(mt * 32 + ((tid >> 3) & 31)) * 640 + ng * 160 + 128 + (tid & 7) * 4) * 2);
  SB();
  if (w == 0)
    while (__builtin_amdgcn_readfirstlane(__hip_atomic_load(cnt + (mt >> 3) * 64, __ATOMIC_RELAXED, __HIP_MEMORY_SCOPE_AGENT)) < 32u)
      __builtin_amdgcn_s_sleep(POLLSLP);
  asm volatile("s_barrier" ::: "memory");
  STAMP(1, 10)
  {
    const char* pa = ws + WS_A2 + ((size_t)mt * KS2 + 112 + kq * 10) * 1024 + 4096;
    const unsigned la = l16 + wh * 1024, lb = l16 + 1024 - wh * 1024;
    asm volatile("global_load_dwordx4 v[0:3], %12, %14 offset:-4096 sc1\n\tglobal_load_dwordx4 v[4:7], %13, %14 offset:-4096 sc1\n\t"
"global_load_dwordx4 v[8:11], %12, %14 offset:-2048 sc1\n\tglobal_load_dwordx4 v[12:15], %13, %14 offset:-2048 sc1\n\t"
"global_load_dwordx4 v[16:19], %12, %14 offset:0 sc1\n\tglobal_load_dwordx4 v[20:23], %13, %14 offset:0 sc1\n\t"
"global_load_dwordx4 v[24:27], %12, %14 offset:2048 sc1\n\tglobal_load_dwordx4 v[28:31], %13, %14 offset:2048 sc1\n\t"
"global_load_dwordx4 v[32:35], %12, %15 offset:0 sc1\n\tglobal_load_dwordx4 v[36:39], %13, %15 offset:0 sc1\n\t"
"s_waitcnt vmcnt(9)\n\t"
"v_mfma_f32_32x32x16_bf16 v[64:79], v[164:167], v[0:3], v[64:79]\n\t"
"v_mfma_f32_32x32x16_bf16 v[80:95], v[168:171], v[0:3], v[80:95]\n\t"
"v_mfma_f32_32x32x16_bf16 v[96:111], v[172:175], v[0:3], v[96:111]\n\t"
"s_waitcnt vmcnt(8)\n\t"
"v_mfma_f32_32x32x16_bf16 v[64:79], v[176:179], v[4:7], v[64:79]\n\t"
"v_mfma_f32_32x32x16_bf16 v[80:95], v[180:183], v[4:7], v[80:95]\n\t"
"s_waitcnt vmcnt(7)\n\t"
"v_mfma_f32_32x32x16_bf16 v[64:79], v[192:195], v[8:11], v[64:79]\n\t"
"v_mfma_f32_32x32x16_bf16 v[80:95], v[196:199], v[8:11], v[80:95]\n\t"
"v_mfma_f32_32x32x16_bf16 v[96:111], v[200:203], v[8:11], v[96:111]\n\t"
"s_waitcnt vmcnt(6)\n\t"
"v_mfma_f32_32x32x16_bf16 v[64:79], v[204:207], v[12:15], v[64:79]\n\t"
"v_mfma_f32_32x32x16_bf16 v[80:95], v[208:211], v[12:15], v[80:95]\n\t"
"s_waitcnt vmcnt(5)\n\t"
"v_mfma_f32_32x32x16_bf16 v[64:79], v[156:159], v[16:19], v[64:79]\n\t"
"v_mfma_f32_32x32x16_bf16 v[80:95], v[160:163], v[16:19], v[80:95]\n\t"
"v_mfma_f32_32x32x16_bf16 v[96:111], v[184:187], v[16:19], v[96:111]\n\t"
"s_waitcnt vmcnt(4)\n\t"
"v_mfma_f32_32x32x16_bf16 v[64:79], v[188:191], v[20:23], v[64:79]\n\t"
"v_mfma_f32_32x32x16_bf16 v[80:95], v[224:227], v[20:23], v[80:95]\n\t"
"s_waitcnt vmcnt(3)\n\t"
"v_mfma_f32_32x32x16_bf16 v[64:79], v[228:231], v[24:27], v[64:79]\n\t"
"v_mfma_f32_32x32x16_bf16 v[80:95], v[232:235], v[24:27], v[80:95]\n\t"
"v_mfma_f32_32x32x16_bf16 v[96:111], v[236:239], v[24:27], v[96:111]\n\t"
"s_waitcnt vmcnt(2)\n\t"
"v_mfma_f32_32x32x16_bf16 v[64:79], v[240:243], v[28:31], v[64:79]\n\t"
"v_mfma_f32_32x32x16_bf16 v[80:95], v[244:247], v[28:31], v[80:95]\n\t"
"s_waitcnt vmcnt(1)\n\t"
"v_mfma_f32_32x32x16_bf16 v[64:79], v[248:251], v[32:35], v[64:79]\n\t"
"v_mfma_f32_32x32x16_bf16 v[80:95], v[252:255], v[32:35], v[80:95]\n\t"
"v_mfma_f32_32x32x16_bf16 v[96:111], v[124:127], v[32:35], v[96:111]\n\t"
"s_waitcnt vmcnt(0)\n\t"
"v_mfma_f32_32x32x16_bf16 v[64:79], v[128:131], v[36:39], v[64:79]\n\t"
"v_mfma_f32_32x32x16_bf16 v[80:95], v[148:151], v[36:39], v[80:95]\n\t"
"s_nop 7\n\ts_nop 7\n.Lcode_last:\n\ts_nop 3"
                 : "+{v[64:79]}"(ga[0]), "+{v[80:95]}"(ga[1]), "+{v[96:111]}"(ga[2])
                 : "{v[156:171]}"(wf0), "{v[172:187]}"(wf1), "{v[188:203]}"(wf2), "{v[204:211]}"(wf3), "{v[224:239]}"(wf4),
                   "{v[240:255]}"(wf5), "{v[128:131]}"(wf6), "{v[124:127]}"(kc), "{v[148:151]}"(kr), "v"(la), "v"(lb), "s"(pa), "s"(pa + 4096)
                 : "v0","v1","v2","v3","v4","v5","v6","v7","v8","v9","v10","v11","v12","v13","v14","v15","v16","v17","v18","v19","v20","v21","v22","v23","v24","v25","v26","v27","v28","v29","v30","v31","v32","v33","v34","v35","v36","v37","v38","v39","v40","v41","vcc", "memory");
  }
  STAMP(1, 11)
  g2_reduce(ga, w, lane, tid, mt, ng, xv, xw, out);
  STAMP(1, 15)
  CSTAMP(1, 14)
  }
}

extern "C" void kernel_launch(void* const* d_in, const int* in_sizes, int n_in, void* d_out, int out_size, void* d_ws,
                              size_t ws_size, hipStream_t stream) {
  const char* x = (const char*)d_in[0];
  (void)hipFuncSetAttribute((const void*)k1, hipFuncAttributeMaxDynamicSharedMemorySize, K1_LDS);
  (void)hipFuncSetAttribute((const void*)k2, hipFuncAttributeMaxDynamicSharedMemorySize, K2_LDS);
  k1<<<256, K1_THR, K1_LDS, stream>>>(x, (const char*)d_in[1], (const char*)d_in[2], (const char*)d_in[4], (const char*)d_in[5],
                                   (const char*)d_in[6], (char*)d_ws, (const char*)d_out);
  k2<<<256, 512, K2_LDS, stream>>>(x, (char*)d_ws, (char*)d_out);
}
```
